```python
import torch

def _softmax(x, axis=-1):
    return torch.softmax(x, dim=axis)
def _gelu(x):
    return torch.nn.functional.gelu(x, approximate="tanh")
def _silu(x):
    return torch.nn.functional.silu(x)
def _sigmoid(x):
    return torch.sigmoid(x)
def _layernorm(x, axis=-1, eps=1e-5):
    m = x.mean(dim=axis, keepdim=True)
    v = x.var(dim=axis, keepdim=True, unbiased=False)
    return (x - m) / torch.sqrt(v + eps)
def _scatter_add(tgt, idx, src, axis=0):
    return tgt.clone().index_add_(axis, idx, src)
def _segment_sum(src, seg, n):
    out = torch.zeros((n,) + src.shape[1:], dtype=src.dtype)
    return out.index_add_(0, seg, src)
def _pad(x, pw):
    flat = tuple(v for pair in reversed(pw) for v in pair)
    return torch.nn.functional.pad(x, flat)

kernel_name = 'parallel_sum__ssm_conv_s4__dense_activation_mish__prompt'

def setup_inputs():

    _w = (lambda a: (lambda t: t.to(torch.bfloat16) if t.is_floating_point() else t)(torch.as_tensor(a)))
    def _A_setup():
        B, L, D, N = 1, 2048, 576, 128
        A_re = -(lambda _x: torch.exp(torch.as_tensor(_x)))((lambda *s: torch.randn(*s, dtype=torch.bfloat16) * 6.4)(D, N))
        A_im = torch.pi * (lambda *s: torch.randn(*s, dtype=torch.bfloat16) * 0.05)(D, N)
        B_re = (lambda *s: torch.randn(*s, dtype=torch.bfloat16) * 0.1)(D, N)
        B_im = (lambda *s: torch.randn(*s, dtype=torch.bfloat16) * 0.1)(D, N)
        C_re = (lambda *s: torch.randn(*s, dtype=torch.bfloat16) * 0.1)(D, N)
        C_im = (lambda *s: torch.randn(*s, dtype=torch.bfloat16) * 0.1)(D, N)
        log_dt = (lambda *s: torch.randn(*s, dtype=torch.bfloat16) * 0.05)(D) * 0.5 - 4.0
        Dskip = (lambda *s: torch.randn(*s, dtype=torch.bfloat16) * 0.05)(D)
        return {
            "u": _w((lambda *s: torch.randn(*s, dtype=torch.bfloat16) * 0.05)(B, L, D)),
            "A_re": _w(A_re),
            "A_im": _w(A_im),
            "B_re": _w(B_re),
            "B_im": _w(B_im),
            "C_re": _w(C_re),
            "C_im": _w(C_im),
            "log_dt": _w(log_dt),
            "Dskip": _w(Dskip),
        }
    _A_in = _A_setup()

    def _B_setup():
        B, S, D, F = 1, 2048, 576, 4608
        return {
            "x": _w((lambda *s: torch.randn(*s, dtype=torch.bfloat16) * 0.05)(B, S, D)),
            "w1": _w((lambda *s: torch.randn(*s, dtype=torch.bfloat16) * 0.8)(D, F) * (D ** -0.5)),
            "b1": _w((lambda *s: torch.randn(*s, dtype=torch.bfloat16) * 0.2)(F)),
            "w2": _w((lambda *s: torch.randn(*s, dtype=torch.bfloat16) * 0.2)(F, D) * (F ** -0.5)),
        }
    _B_in = _B_setup()

    return {
        "A_u": _A_in["u"],
        "A_A_re": _A_in["A_re"],
        "A_A_im": _A_in["A_im"],
        "A_B_re": _A_in["B_re"],
        "A_B_im": _A_in["B_im"],
        "A_C_re": _A_in["C_re"],
        "A_C_im": _A_in["C_im"],
        "A_log_dt": _A_in["log_dt"],
        "A_Dskip": _A_in["Dskip"],
        "B_x": _B_in["x"],
        "B_w1": _B_in["w1"],
        "B_b1": _B_in["b1"],
        "B_w2": _B_in["w2"],
    }

def reference(A_u, A_A_re, A_A_im, A_B_re, A_B_im, A_C_re, A_C_im, A_log_dt, A_Dskip, B_x, B_w1, B_b1, B_w2):
    def _A_ref(u, A_re, A_im, B_re, B_im, C_re, C_im, log_dt, Dskip):
        Bsz, L, D = u.shape
        dt = torch.exp(log_dt)[:, None]
        A = A_re + 1j * A_im
        Bc = B_re + 1j * B_im
        Cc = C_re + 1j * C_im
        logdA = dt * A
        dB = dt * Bc
        l = (lambda x, dt: x.to(dt))(torch.arange(L), A_re.dtype)
        P = torch.exp(logdA[:, :, None] * l[None, None, :])
        K = torch.einsum("dn,dnl,dn->dl", Cc, P, dB).real
        L2 = 2 * L
        Kp = _pad(K, ((0, 0), (0, L)))
        up = (lambda x, *d: x.permute(*d))(u, 0, 2, 1)
        up = _pad(up, ((0, 0), (0, 0), (0, L)))
        Kf = (lambda x, axes=(-2,-1): torch.fft.rfft2(x.float(), dim=axes))(Kp[:, None, :], axes=(-2, -1))
        Uf = (lambda x, axes=(-2,-1): torch.fft.rfft2(x.float(), dim=axes))(up[:, :, None, :], axes=(-2, -1))
        Yf = Uf * Kf[None, :, :, :]
        y = (lambda x, s, axes=(-2,-1): torch.fft.irfft2(x, s=s, dim=axes).to(torch.bfloat16))(Yf, (1, L2), axes=(-2, -1))[:, :, 0, :L]
        y = (lambda x, *d: x.permute(*d))(y, 0, 2, 1) + u * Dskip[None, None, :]
        return y

    def _B_ref(x, w1, b1, w2):
        h = torch.einsum("bsd,df->bsf", x, w1) + b1
        sp = torch.log1p(torch.exp(-torch.abs(h))) + torch.where(h > 0, h, torch.zeros_like(h))
        act = h * torch.tanh(sp)
        return torch.einsum("bsf,fd->bsd", act, w2)

    x = A_u
    xn = _layernorm(x, axis=-1)
    return x + _A_ref(xn, A_A_re, A_A_im, A_B_re, A_B_im, A_C_re, A_C_im, A_log_dt, A_Dskip) + _B_ref(xn, B_w1, B_b1, B_w2)
```

```cpp
#include <hip/hip_runtime.h>
#include <hip/hip_bf16.h>
#include <stdint.h>

typedef unsigned short u16;
typedef unsigned int u32;
using s16x8 = __attribute__((ext_vector_type(8))) short;
using s16x4 = __attribute__((ext_vector_type(4))) short;
using f32x4 = __attribute__((ext_vector_type(4))) float;
using u32x4 = __attribute__((ext_vector_type(4))) unsigned int;
using u32x2 = __attribute__((ext_vector_type(2))) unsigned int;
typedef u32x4 __attribute__((aligned(4))) u32x4_a4;

#define DI __device__ __forceinline__

constexpr int L_ = 2048, D_ = 576, N_ = 128, F_ = 4608;
constexpr int XT_PITCH = 2048 + 128;
constexpr int KX_NW = 1028;
constexpr int LUT_LO = 0x3A80, LUT_HI = 0x4300;
constexpr int LUT_ENTRIES = 8192;

constexpr size_t WS_XN   = 0;
constexpr size_t WS_XNT  = WS_XN + (size_t)L_ * D_ * 2;
constexpr size_t WS_KX   = WS_XNT + (size_t)D_ * XT_PITCH * 2;
constexpr size_t WS_LUT  = WS_KX + (size_t)D_ * 4 * KX_NW * 4;
constexpr size_t WS_YAT  = WS_LUT + LUT_ENTRIES * 2;
constexpr size_t WS_ACT  = WS_YAT + (size_t)D_ * L_ * 2;
constexpr size_t WS_PART = WS_ACT + (size_t)L_ * F_ * 2;

DI float bf2f(u16 b) { return __uint_as_float(((u32)b) << 16); }
DI u16 f2bf(float f) {
  __hip_bfloat16 h = __float2bfloat16(f);
  return *reinterpret_cast<u16*>(&h);
}
DI float rbf(float f) { return bf2f(f2bf(f)); }
DI u32 pack2(float lo, float hi) { return (u32)f2bf(lo) | ((u32)f2bf(hi) << 16); }

constexpr int LN_ROWS = 32;
constexpr int LN_BLOCKS = L_ / LN_ROWS;
constexpr int KG_BLOCKS = D_;
constexpr int LUT_BLOCKS = 2;
constexpr int PREP_BLOCKS = LN_BLOCKS + KG_BLOCKS + LUT_BLOCKS;
constexpr int LN_PITCH = D_ + 8;

DI void cexp_pos(float lr, float li, float pos, float& er, float& ei) {
  float re = lr * pos;
  float ph = li * pos;
  float mag = __builtin_amdgcn_exp2f(re * 1.44269504088896340736f);
  const float C_HI = 0.15915494309189535f;
  const float C_LO = 6.4206383e-09f;
  float r0 = ph * C_HI;
  float e = __builtin_fmaf(ph, C_HI, -r0);
  e = __builtin_fmaf(ph, C_LO, e);
  float f = (r0 - __builtin_rintf(r0)) + e;
  float s = __builtin_amdgcn_sinf(f);
  float c = __builtin_amdgcn_cosf(f);
  er = mag * c;
  ei = mag * s;
}

DI int kg_row_pos(int row) {
  if (row < 16) return 128 * (row >> 3) + (row & 7);
  if (row < 24) { int r = row - 16; return 256 + 128 * (r >> 2) + 2 * (r & 3); }
  if (row < 32) { int r = row - 24; return 512 + 128 * (r >> 1) + 4 * (r & 1); }
  if (row < 40) return 1024 + 128 * (row - 32);
  if (row == 40) return 2048;
  return 0;
}
DI int kg_pos_index(int p) {
  int row, col;
  if (p < 256) { int rem = p & 127; row = 8 * (p >> 7) + (rem & 7); col = rem >> 3; }
  else if (p < 512) { int q = p - 256, rem = q & 127; row = 16 + 4 * (q >> 7) + ((rem & 7) >> 1); col = rem >> 3; }
  else if (p < 1024) { int q = p - 512, rem = q & 127; row = 24 + 2 * (q >> 7) + ((rem & 7) >> 2); col = rem >> 3; }
  else if (p < 2048) { int q = p - 1024; row = 32 + (q >> 7); col = (q & 127) >> 3; }
  else { row = 40; col = 0; }
  return row * 16 + col;
}

__global__ __launch_bounds__(256) void prep_kernel(const u16* __restrict__ x, const u16* __restrict__ A_re, const u16* __restrict__ A_im,
                                                   const u16* __restrict__ B_re, const u16* __restrict__ B_im,
                                                   const u16* __restrict__ C_re, const u16* __restrict__ C_im,
                                                   const u16* __restrict__ log_dt, char* __restrict__ ws) {
  __shared__ __attribute__((aligned(16))) char smem[LN_ROWS * LN_PITCH * 2];
  const int tid = threadIdx.x, lane = tid & 63, wid = tid >> 6;
  const int bid = blockIdx.x;
  if (bid < LN_BLOCKS) {
    u16* tile = (u16*)smem;
    const int t0 = bid * LN_ROWS;
    for (int c = tid; c < LN_ROWS * 72; c += 256) {
      int r = c / 72, ch = c % 72;
      u32x4 v = *(const u32x4*)(x + (size_t)(t0 + r) * D_ + ch * 8);
      *(u32x4*)(tile + r * LN_PITCH + ch * 8) = v;
    }
    __syncthreads();
    for (int rr = 0; rr < 8; ++rr) {
      int r = wid * 8 + rr;
      float v[9];
      float s = 0.f;
#pragma unroll
      for (int i = 0; i < 9; ++i) { v[i] = bf2f(tile[r * LN_PITCH + lane + 64 * i]); s += v[i]; }
#pragma unroll
      for (int o = 32; o > 0; o >>= 1) s += __shfl_xor(s, o);
      float mean = s * (1.0f / 576.0f);
      float q = 0.f;
#pragma unroll
      for (int i = 0; i < 9; ++i) { float d = v[i] - mean; q += d * d; }
#pragma unroll
      for (int o = 32; o > 0; o >>= 1) q += __shfl_xor(q, o);
      float var = q * (1.0f / 576.0f);
      float mb = rbf(mean), vb = rbf(var);
      float den = rbf(sqrtf(rbf(vb + 1e-5f)));
#pragma unroll
      for (int i = 0; i < 9; ++i) {
        float xn = rbf(v[i] - mb) / den;
        tile[r * LN_PITCH + lane + 64 * i] = f2bf(xn);
      }
    }
    __syncthreads();
    u16* xn_g = (u16*)(ws + WS_XN);
    for (int c = tid; c < LN_ROWS * 72; c += 256) {
      int r = c / 72, ch = c % 72;
      *(u32x4*)(xn_g + (size_t)(t0 + r) * D_ + ch * 8) = *(const u32x4*)(tile + r * LN_PITCH + ch * 8);
    }
    u16* xt_g = (u16*)(ws + WS_XNT);
    for (int c = tid; c < D_ * 4; c += 256) {
      int d = c >> 2, tq = c & 3;
      u32x4 v;
#pragma unroll
      for (int j = 0; j < 4; ++j) {
        u32 lo = tile[(tq * 8 + 2 * j) * LN_PITCH + d], hi = tile[(tq * 8 + 2 * j + 1) * LN_PITCH + d];
        v[j] = lo | (hi << 16);
      }
      *(u32x4*)(xt_g + (size_t)d * XT_PITCH + 128 + t0 + tq * 8) = v;
    }
    if (tid < 9 * 16) {
      int d = bid * 9 + tid / 16, ch = tid % 16;
      *(u32x4*)(xt_g + (size_t)d * XT_PITCH + ch * 8) = u32x4{0u, 0u, 0u, 0u};
    }
  } else if (bid < LN_BLOCKS + KG_BLOCKS) {
    const int d = bid - LN_BLOCKS;
    float* red = (float*)smem;
    float* kv = red + 4 * 768;
    float* keff = kv + 768;
    const float dt = rbf(expf(bf2f(log_dt[d])));
    const int i16 = lane & 15, kq = lane >> 4;
    f32x4 acc[3] = {f32x4{0, 0, 0, 0}, f32x4{0, 0, 0, 0}, f32x4{0, 0, 0, 0}};
    float posA[3];
#pragma unroll
    for (int t = 0; t < 3; ++t) posA[t] = (float)kg_row_pos(16 * t + i16);
    const float posB = (float)(8 * i16);
    for (int g = 0; g < 8; ++g) {
      const int n = wid * 32 + g * 4 + kq;
      const size_t o = (size_t)d * N_ + n;
      const float lr = dt * bf2f(A_re[o]), li = dt * bf2f(A_im[o]);
      const float br = dt * bf2f(B_re[o]), bi = dt * bf2f(B_im[o]);
      const float cr = bf2f(C_re[o]), ci = bf2f(C_im[o]);
      const float kr = cr * br - ci * bi, ki = cr * bi + ci * br;
      float zr, zi;
      cexp_pos(lr, li, posB, zr, zi);
#pragma unroll
      for (int t = 0; t < 3; ++t) {
        float er, ei;
        cexp_pos(lr, li, posA[t], er, ei);
        float ar = kr * er - ki * ei, ai = kr * ei + ki * er;
        acc[t] = __builtin_amdgcn_mfma_f32_16x16x4f32(ar, zr, acc[t], 0, 0, 0);
        acc[t] = __builtin_amdgcn_mfma_f32_16x16x4f32(-ai, zi, acc[t], 0, 0, 0);
      }
    }
#pragma unroll
    for (int t = 0; t < 3; ++t)
#pragma unroll
      for (int j = 0; j < 4; ++j) red[wid * 768 + (16 * t + 4 * kq + j) * 16 + i16] = acc[t][j];
    __syncthreads();
    for (int e = tid; e < 768; e += 256) kv[e] = (red[e] + red[768 + e]) + (red[2 * 768 + e] + red[3 * 768 + e]);
    __syncthreads();
    for (int l = tid; l < 2048; l += 256) {
      int p = (int)rbf((float)l);
      keff[l] = kv[kg_pos_index(p)];
    }
    __syncthreads();
    u32* kx = (u32*)(ws + WS_KX) + (size_t)d * 4 * KX_NW;
    for (int w = tid; w < KX_NW; w += 256) {
      float k0, k1, k2;
      { int i = 2047 - 2 * w;     k0 = (i >= 0) ? keff[i] : 0.f; }
      { int i = 2047 - 2 * w - 1; k1 = (i >= 0) ? keff[i] : 0.f; }
      { int i = 2047 - 2 * w - 2; k2 = (i >= 0) ? keff[i] : 0.f; }
      float h0 = rbf(k0), h1 = rbf(k1), h2 = rbf(k2);
      kx[0 * KX_NW + w] = pack2(h0, h1);
      kx[1 * KX_NW + w] = pack2(h1, h2);
      kx[2 * KX_NW + w] = pack2(k0 - h0, k1 - h1);
      kx[3 * KX_NW + w] = pack2(k1 - h1, k2 - h2);
    }
  } else {
    const int sign = bid - (LN_BLOCKS + KG_BLOCKS);
    u16* lut = (u16*)(ws + WS_LUT);
    for (int e = tid; e < 4096; e += 256) {
      int a = LUT_LO + e;
      if (a > LUT_HI) a = LUT_HI;
      float h = bf2f((u16)(a | (sign << 15)));
      float ex = rbf(expf(-fabsf(h)));
      float lp = rbf(log1pf(ex));
      float sp = rbf(lp + (h > 0.f ? h : 0.f));
      float th = tanhf(sp);
      lut[sign * 4096 + e] = f2bf(th);
    }
  }
}

__global__ __launch_bounds__(256) void conv_kernel(const u16* __restrict__ Dskip, char* __restrict__ ws) {
  __shared__ __attribute__((aligned(16))) float ybuf[4 * 2048];
  const int tid = threadIdx.x, lane = tid & 63, g = tid >> 6;
  const int d = blockIdx.x;
  const u16* urow = (const u16*)(ws + WS_XNT) + (size_t)d * XT_PITCH + 128;
  const int i16 = lane & 15, kq = lane >> 4;
  s16x8 F[16], Bq[16];
#pragma unroll
  for (int dl = 0; dl < 16; ++dl) {
    int c = 16 * dl - 4 * g + i16 - kq;
    F[dl] = *(const s16x8*)(urow + 8 * c);
  }
  {
    const int rho = i16 & 7, hl = i16 >> 3;
    const int par = (1 - rho) & 1;
    const u32* kx = (const u32*)(ws + WS_KX) + ((size_t)d * 4 + hl * 2 + par) * KX_NW;
#pragma unroll
    for (int q = 0; q < 16; ++q) {
      int ap = 4 * (4 * q + g) + kq;
      int m0 = 2047 - 8 * ap - rho;
      u32x4 v = *(const u32x4_a4*)(kx + (m0 >> 1));
      Bq[q] = __builtin_bit_cast(s16x8, v);
    }
  }
  f32x4 acc[16];
#pragma unroll
  for (int R = 0; R < 16; ++R) acc[R] = f32x4{0, 0, 0, 0};
#pragma unroll
  for (int q = 0; q < 16; ++q)
#pragma unroll
    for (int R = q; R < 16; ++R)
      acc[R] = __builtin_amdgcn_mfma_f32_16x16x32_bf16(F[R - q], Bq[q], acc[R], 0, 0, 0);
#pragma unroll
  for (int R = 0; R < 16; ++R)
#pragma unroll
    for (int j = 0; j < 4; ++j) {
      float v = acc[R][j];
      v += __shfl_xor(v, 8);
      if (i16 < 8) ybuf[g * 2048 + 128 * R + 32 * kq + 8 * j + i16] = v;
    }
  __syncthreads();
  const float dsk = bf2f(Dskip[d]);
  u16* yat = (u16*)(ws + WS_YAT) + (size_t)d * L_;
  {
    const int t = tid * 8;
    s16x8 uv = *(const s16x8*)(urow + t);
    u32x4 o;
#pragma unroll
    for (int j = 0; j < 4; ++j) {
      float r[2];
#pragma unroll
      for (int h = 0; h < 2; ++h) {
        int tt = t + 2 * j + h;
        float s = (ybuf[tt] + ybuf[2048 + tt]) + (ybuf[4096 + tt] + ybuf[6144 + tt]);
        float un = bf2f((u16)uv[2 * j + h]);
        r[h] = rbf(s) + rbf(un * dsk);
      }
      o[j] = pack2(r[0], r[1]);
    }
    *(u32x4*)(yat + t) = o;
  }
}

template <int MODE> struct GemmCfg {
  static constexpr int WKN = MODE ? 2 : 1;
  static constexpr int BM = MODE ? 128 : 256;
  static constexpr int LDA = MODE ? F_ : D_;
  static constexpr int LDB = MODE ? D_ : F_;
  static constexpr int A_BYTES = 32768;
  static constexpr int B_BYTES = 64 * WKN * 288;
  static constexpr int STAGE = A_BYTES + B_BYTES;
  static constexpr int NB_INSTR = 18 * WKN;
  static constexpr int LUT_OFF = 2 * STAGE;
  static constexpr int SMEM = MODE ? 2 * STAGE : 2 * STAGE + LUT_ENTRIES * 2;
};

typedef __attribute__((address_space(3))) s16x4 lds_s16x4;

template <int MODE, int NB>
DI void gemm_wave_body(char* smem, const u16* __restrict__ Ag, const u16* __restrict__ Bg,
                       int wid, int lane, int wmv, int wk, int ncol0,
                       const int (&aoff)[4], const int (&boff)[5], f32x4 (&acc)[4][5]) {
  using C = GemmCfg<MODE>;
  constexpr int BK = 64 * C::WKN;
  const int i16 = lane & 15, kq = lane >> 4;
  const int arow = wmv * 64 + i16;
  const int a_rd0 = arow * 128 + (((0 + kq) ^ (arow & 7)) * 16);
  const int a_rd1 = arow * 128 + (((4 + kq) ^ (arow & 7)) * 16);
  const int q4 = i16 >> 2, p4 = i16 & 3;
  const int k_t0 = 8 * kq + q4, k_t1 = 8 * kq + q4 + 4;
  const int slot0 = k_t0 ^ ((k_t0 >> 1) & 4), slot1 = k_t1 ^ ((k_t1 >> 1) & 4);
  const int b_rd0 = C::A_BYTES + (wk * 64 + slot0) * 288 + p4 * 8 + ncol0 * 2;
  const int b_rd1 = C::A_BYTES + (wk * 64 + slot1) * 288 + p4 * 8 + ncol0 * 2;

  auto stage = [&](int kt, int buf) {
    char* sb = smem + buf * C::STAGE;
    const u16* ak = Ag + kt * BK;
    const u16* bk = Bg + (size_t)kt * BK * C::LDB;
#pragma unroll
    for (int i = 0; i < 4; ++i)
      __builtin_amdgcn_global_load_lds((const u32*)(ak + aoff[i]), (u32*)(sb + (wid * 4 + i) * 1024), 16, 0, 0);
#pragma unroll
    for (int i = 0; i < 5; ++i) {
      const int ins = wid + 8 * i;
      if (ins < C::NB_INSTR)
        __builtin_amdgcn_global_load_lds((const u32*)(bk + boff[i]), (u32*)(sb + C::A_BYTES + ins * 1024), 16, 0, 0);
    }
  };
  auto compute = [&](int buf) {
    char* sb = smem + buf * C::STAGE;
#pragma unroll
    for (int ks = 0; ks < 2; ++ks) {
      s16x8 af[4], bf[NB];
#pragma unroll
      for (int mi = 0; mi < 4; ++mi) af[mi] = *(const s16x8*)(sb + (ks ? a_rd1 : a_rd0) + mi * 16 * 128);
#pragma unroll
      for (int nb = 0; nb < NB; ++nb) {
        s16x4 lo = __builtin_amdgcn_ds_read_tr16_b64_v4i16((lds_s16x4*)(sb + b_rd0 + ks * 32 * 288 + nb * 32));
        s16x4 hi = __builtin_amdgcn_ds_read_tr16_b64_v4i16((lds_s16x4*)(sb + b_rd1 + ks * 32 * 288 + nb * 32));
        bf[nb] = s16x8{lo[0], lo[1], lo[2], lo[3], hi[0], hi[1], hi[2], hi[3]};
      }
#pragma unroll
      for (int mi = 0; mi < 4; ++mi)
#pragma unroll
        for (int nb = 0; nb < NB; ++nb)
          acc[mi][nb] = __builtin_amdgcn_mfma_f32_16x16x32_bf16(bf[nb], af[mi], acc[mi][nb], 0, 0, 0);
    }
  };

  stage(0, 0);
  asm volatile("s_waitcnt vmcnt(0)" ::: "memory");
  __syncthreads();
  for (int kt = 0; kt < 9; ++kt) {
    const int cur = kt & 1;
    if (kt + 1 < 9) stage(kt + 1, cur ^ 1);
    compute(cur);
    asm volatile("s_waitcnt vmcnt(0)" ::: "memory");
    __syncthreads();
  }
}

template <int MODE>
__global__ __launch_bounds__(512) void gemm_kernel(const u16* __restrict__ Ain, const u16* __restrict__ Bin,
                                                   const u16* __restrict__ bias, char* __restrict__ ws) {
  using C = GemmCfg<MODE>;
  __shared__ __attribute__((aligned(1024))) char smem[C::SMEM];
  const int tid = threadIdx.x, lane = tid & 63, wid = __builtin_amdgcn_readfirstlane(tid >> 6);
  const int bid = blockIdx.x;
  const int xcd = bid & 7, idx = bid >> 3;
  int m0, n0, ksplit = 0;
  if (MODE == 0) { int nt = 4 * xcd + (idx & 3), mt = idx >> 2; m0 = mt * 256; n0 = nt * 144; }
  else { ksplit = xcd >> 1; int mt = (xcd & 1) * 8 + (idx >> 2), nt = idx & 3; m0 = mt * 128; n0 = nt * 144; }
  const u16* Ag = Ain + (size_t)m0 * C::LDA + (MODE ? ksplit * 1152 : 0);
  const u16* Bg = Bin + (size_t)(MODE ? ksplit * 1152 : 0) * C::LDB + n0;

  int aoff[4], boff[5];
#pragma unroll
  for (int i = 0; i < 4; ++i) {
    int vrow = (wid * 4 + i) * 8 + (lane >> 3);
    int c = (lane & 7) ^ (vrow & 7);
    aoff[i] = (vrow & (C::BM - 1)) * C::LDA + (vrow / C::BM) * 64 + c * 8;
  }
#pragma unroll
  for (int i = 0; i < 5; ++i) {
    int Lc = (wid + 8 * i) * 64 + lane;
    int slot = Lc / 18, c8 = Lc % 18;
    int k = slot ^ ((slot >> 1) & 4);
    boff[i] = k * C::LDB + c8 * 8;
  }
  if (MODE == 0) {
    const u16* lut = (const u16*)(ws + WS_LUT);
#pragma unroll
    for (int i = 0; i < 2; ++i)
      __builtin_amdgcn_global_load_lds((const u32*)(lut + (wid * 2 + i) * 512 + lane * 8),
                                       (u32*)(smem + C::LUT_OFF + (wid * 2 + i) * 1024), 16, 0, 0);
  }
  const int wn = wid >> 2;
  const int wmv = wid & 3;
  const int wk = MODE ? (wmv >> 1) : 0;
  const int wm = MODE ? (wmv & 1) : wmv;
  const int ncol0 = wn * 80;
  f32x4 acc[4][5];
#pragma unroll
  for (int mi = 0; mi < 4; ++mi)
#pragma unroll
    for (int nb = 0; nb < 5; ++nb) acc[mi][nb] = f32x4{0, 0, 0, 0};
  if (wn == 0) gemm_wave_body<MODE, 5>(smem, Ag, Bg, wid, lane, wmv, wk, ncol0, aoff, boff, acc);
  else         gemm_wave_body<MODE, 4>(smem, Ag, Bg, wid, lane, wmv, wk, ncol0, aoff, boff, acc);

  const int i16 = lane & 15, kq = lane >> 4;
  const int NBW = wn == 0 ? 5 : 4;
  if (MODE == 0) {
    u16* act = (u16*)(ws + WS_ACT);
    const u16* lut = (const u16*)(smem + C::LUT_OFF);
#pragma unroll
    for (int nb = 0; nb < 5; ++nb) {
      if (nb < NBW) {
        const int n = n0 + ncol0 + nb * 16 + 4 * kq;
        u32x2 bb = *(const u32x2*)(bias + n);
        float bv[4] = {bf2f((u16)(bb[0] & 0xffff)), bf2f((u16)(bb[0] >> 16)), bf2f((u16)(bb[1] & 0xffff)), bf2f((u16)(bb[1] >> 16))};
#pragma unroll
        for (int mi = 0; mi < 4; ++mi) {
          const int m = m0 + wm * 64 + mi * 16 + i16;
          float r[4];
#pragma unroll
          for (int j = 0; j < 4; ++j) {
            u16 hb = f2bf(rbf(acc[mi][nb][j]) + bv[j]);
            int a = hb & 0x7fff;
            a = a < LUT_LO ? LUT_LO : (a > LUT_HI ? LUT_HI : a);
            int ix = (a - LUT_LO) | ((hb >> 15) << 12);
            r[j] = bf2f(hb) * bf2f(lut[ix]);
          }
          u32x2 o = {pack2(r[0], r[1]), pack2(r[2], r[3])};
          *(u32x2*)(act + (size_t)m * F_ + n) = o;
        }
      }
    }
  } else {
    f32x4* red = (f32x4*)smem;
    const int slot = wn * 2 + wm;
    if (wk == 1) {
#pragma unroll
      for (int mi = 0; mi < 4; ++mi)
#pragma unroll
        for (int nb = 0; nb < 5; ++nb)
          if (nb < NBW) red[(slot * 20 + mi * 5 + nb) * 64 + lane] = acc[mi][nb];
    }
    __syncthreads();
    if (wk == 0) {
      float* part = (float*)(ws + WS_PART) + (size_t)ksplit * L_ * D_;
#pragma unroll
      for (int mi = 0; mi < 4; ++mi)
#pragma unroll
        for (int nb = 0; nb < 5; ++nb)
          if (nb < NBW) {
            f32x4 v = acc[mi][nb] + red[(slot * 20 + mi * 5 + nb) * 64 + lane];
            const int m = m0 + wm * 64 + mi * 16 + i16;
            const int n = n0 + ncol0 + nb * 16 + 4 * kq;
            *(f32x4*)(part + (size_t)m * D_ + n) = v;
          }
    }
  }
}

__global__ __launch_bounds__(256) void final_kernel(const u16* __restrict__ x, const char* __restrict__ ws, u16* __restrict__ out) {
  __shared__ __attribute__((aligned(16))) u16 yt[64 * 72];
  const int tid = threadIdx.x;
  const int tt = blockIdx.x / 9, dt = blockIdx.x % 9;
  const int t0 = tt * 64, d0 = dt * 64;
  const u16* yat = (const u16*)(ws + WS_YAT);
  for (int c = tid; c < 512; c += 256) {
    int dd = c >> 3, ch = c & 7;
    *(u32x4*)(yt + dd * 72 + ch * 8) = *(const u32x4*)(yat + (size_t)(d0 + dd) * L_ + t0 + ch * 8);
  }
  __syncthreads();
  const float* part = (const float*)(ws + WS_PART);
  for (int c = tid; c < 512; c += 256) {
    int r = c >> 3, ch = c & 7;
    const size_t o = (size_t)(t0 + r) * D_ + d0 + ch * 8;
    u32x4 xv = *(const u32x4*)(x + o);
    float s[8];
    {
      f32x4 a0 = *(const f32x4*)(part + o), a1 = *(const f32x4*)(part + o + 4);
      f32x4 b0 = *(const f32x4*)(part + (size_t)L_ * D_ + o), b1 = *(const f32x4*)(part + (size_t)L_ * D_ + o + 4);
      f32x4 c0 = *(const f32x4*)(part + (size_t)2 * L_ * D_ + o), c1 = *(const f32x4*)(part + (size_t)2 * L_ * D_ + o + 4);
      f32x4 e0 = *(const f32x4*)(part + (size_t)3 * L_ * D_ + o), e1 = *(const f32x4*)(part + (size_t)3 * L_ * D_ + o + 4);
      f32x4 s0 = (a0 + b0) + (c0 + e0), s1 = (a1 + b1) + (c1 + e1);
#pragma unroll
      for (int j = 0; j < 4; ++j) { s[j] = s0[j]; s[4 + j] = s1[j]; }
    }
    u32x4 ov;
#pragma unroll
    for (int j = 0; j < 4; ++j) {
      float r2[2];
#pragma unroll
      for (int h = 0; h < 2; ++h) {
        int e = 2 * j + h;
        float xx = bf2f((u16)(h ? (xv[j] >> 16) : (xv[j] & 0xffff)));
        float ya = bf2f(yt[(ch * 8 + e) * 72 + r]);
        r2[h] = rbf(xx + ya) + rbf(s[e]);
      }
      ov[j] = pack2(r2[0], r2[1]);
    }
    *(u32x4*)(out + o) = ov;
  }
}

extern "C" void kernel_launch(void* const* d_in, const int* in_sizes, int n_in,
                              void* d_out, int out_size, void* d_ws, size_t ws_size,
                              hipStream_t stream) {
  const u16* x      = (const u16*)d_in[0];
  const u16* A_re   = (const u16*)d_in[1];
  const u16* A_im   = (const u16*)d_in[2];
  const u16* B_re   = (const u16*)d_in[3];
  const u16* B_im   = (const u16*)d_in[4];
  const u16* C_re   = (const u16*)d_in[5];
  const u16* C_im   = (const u16*)d_in[6];
  const u16* log_dt = (const u16*)d_in[7];
  const u16* Dskip  = (const u16*)d_in[8];
  const u16* w1     = (const u16*)d_in[10];
  const u16* b1     = (const u16*)d_in[11];
  const u16* w2     = (const u16*)d_in[12];
  char* ws = (char*)d_ws;
  prep_kernel<<<PREP_BLOCKS, 256, 0, stream>>>(x, A_re, A_im, B_re, B_im, C_re, C_im, log_dt, ws);
  conv_kernel<<<D_, 256, 0, stream>>>(Dskip, ws);
  gemm_kernel<0><<<256, 512, 0, stream>>>((const u16*)(ws + WS_XN), w1, b1, ws);
  gemm_kernel<1><<<256, 512, 0, stream>>>((const u16*)(ws + WS_ACT), w2, b1, ws);
  final_kernel<<<288, 256, 0, stream>>>(x, ws, (u16*)d_out);
}
```

```cpp
#include <hip/hip_runtime.h>
#include <hip/hip_bf16.h>
#include <stdint.h>

typedef unsigned short u16;
typedef unsigned int u32;
using s16x8 = __attribute__((ext_vector_type(8))) short;
using s16x4 = __attribute__((ext_vector_type(4))) short;
using f32x4 = __attribute__((ext_vector_type(4))) float;
using u32x4 = __attribute__((ext_vector_type(4))) unsigned int;
using u32x2 = __attribute__((ext_vector_type(2))) unsigned int;
typedef u32x4 __attribute__((aligned(4))) u32x4_a4;

#define DI __device__ __forceinline__
#define STAMP_DECL
#define STAMP_NOW()
#define STAMP_RT()
#define STAMP_VM()
#define STAMP_OUT(kid, ws)

constexpr int L_ = 2048, D_ = 576, N_ = 128, F_ = 4608;
constexpr int XT_PITCH = 2048 + 128;
constexpr int KX_NW = 1032;
constexpr int LUT_LO = 0x3A80, LUT_HI = 0x4300;
constexpr int LUT_ENTRIES = 8192;

constexpr size_t WS_CNT  = 0;
constexpr size_t WS_XN   = 1024;
constexpr size_t WS_XNT  = WS_XN + (size_t)L_ * D_ * 2;
constexpr size_t WS_KX   = WS_XNT + (size_t)D_ * XT_PITCH * 2;
constexpr size_t WS_LUT  = WS_KX + (size_t)D_ * 2 * KX_NW * 4;
constexpr size_t WS_YAT  = WS_LUT + LUT_ENTRIES * 4;
constexpr size_t WS_ACT  = WS_YAT + (size_t)D_ * L_ * 2;
constexpr size_t WS_PART = WS_ACT + (size_t)L_ * F_ * 2;

DI float bf2f(u16 b) { return __uint_as_float(((u32)b) << 16); }
DI u16 f2bf(float f) {
  __hip_bfloat16 h = __float2bfloat16(f);
  return *reinterpret_cast<u16*>(&h);
}
DI float rbf(float f) { return bf2f(f2bf(f)); }
typedef __bf16 bf16x2_t __attribute__((ext_vector_type(2)));
using f32x2 = __attribute__((ext_vector_type(2))) float;
DI u32 pack2(float lo, float hi) {
  f32x2 v = {lo, hi};
  return __builtin_bit_cast(u32, __builtin_convertvector(v, bf16x2_t));
}
DI float lo16(u32 v) { return __uint_as_float(v << 16); }
DI float hi16(u32 v) { return __uint_as_float(v & 0xffff0000u); }
DI void st16_wt(void* p, u32x4 v) { asm volatile("global_store_dwordx4 %0, %1, off sc1\n\ts_nop 1" ::"v"(p), "v"(v) : "memory"); }
DI void st8_wt(void* p, u32x2 v) { asm volatile("global_store_dwordx2 %0, %1, off sc1\n\ts_nop 1" ::"v"(p), "v"(v) : "memory"); }

DI int blk_swz(int i16) { return (-(i16 >> 2)) & 3; }

constexpr int LN_ROWS = 16;
constexpr int LN_BLOCKS = L_ / LN_ROWS;
constexpr int KG_BLOCKS = D_;
constexpr int LUT_BLOCKS = 32;
constexpr int PREP_BLOCKS = LN_BLOCKS + KG_BLOCKS + LUT_BLOCKS;
constexpr int LN_PITCH = D_ + 8;

DI void cexp_pos(float lr2, float lh, float pos, float& er, float& ei) {
  const float mag = __builtin_amdgcn_exp2f(lr2 * pos);
  const float f = __builtin_amdgcn_fractf(lh * pos);
  er = mag * __builtin_amdgcn_cosf(f);
  ei = mag * __builtin_amdgcn_sinf(f);
}

DI int kg_row_pos(int row) {
  if (row < 16) return 128 * (row >> 3) + (row & 7);
  if (row < 24) { int r = row - 16; return 256 + 128 * (r >> 2) + 2 * (r & 3); }
  if (row < 32) { int r = row - 24; return 512 + 128 * (r >> 1) + 4 * (r & 1); }
  if (row < 40) return 1024 + 128 * (row - 32);
  if (row == 40) return 2048;
  return 0;
}
struct KIdxTab { u32 v[1032]; };
constexpr int kg_bf16_round(int l) {
  int sft = l >= 1024 ? 3 : l >= 512 ? 2 : l >= 256 ? 1 : 0;
  if (sft == 0) return l;
  int half = 1 << (sft - 1), q = l >> sft;
  return ((l + half - 1 + (q & 1)) >> sft) << sft;
}
constexpr int kg_pos_index(int p) {
  int sg = p >= 2048 ? 4 : p >= 1024 ? 3 : p >= 512 ? 2 : p >= 256 ? 1 : 0;
  int base = sg ? (128 << sg) : 0, rowbase = sg ? 8 + 8 * sg : 0;
  int q = p - base;
  int row = rowbase + (q >> 7) * (8 >> sg) + ((q & 7) >> sg);
  return row * 16 + ((q & 127) >> 3);
}
constexpr KIdxTab make_kidx() {
  KIdxTab t{};
  for (int w = 0; w < 1032; ++w) {
    int l0 = 2047 - 2 * w, l1 = 2046 - 2 * w;
    u32 i0 = l0 >= 0 ? kg_pos_index(kg_bf16_round(l0)) : 768, i1 = l1 >= 0 ? kg_pos_index(kg_bf16_round(l1)) : 768;
    t.v[w] = (i0 * 4) | ((i1 * 4) << 16);
  }
  return t;
}
__device__ const KIdxTab kIdxTab = make_kidx();

__global__ __launch_bounds__(256) void prep_kernel(const u16* __restrict__ x, const u16* __restrict__ A_re, const u16* __restrict__ A_im,
                                                   const u16* __restrict__ B_re, const u16* __restrict__ B_im,
                                                   const u16* __restrict__ C_re, const u16* __restrict__ C_im,
                                                   const u16* __restrict__ log_dt, char* __restrict__ ws) {
  __shared__ __attribute__((aligned(16))) char smem[24 * 1024];
  const int tid = threadIdx.x, lane = tid & 63, wid = tid >> 6;
  const int bid = blockIdx.x;
  STAMP_DECL STAMP_RT(); STAMP_NOW();
  if (bid < LN_BLOCKS) {
    u16* tile = (u16*)smem;
    const int t0 = bid * LN_ROWS;
    const int r = tid >> 4, j16 = tid & 15;
    u32x2 v[9];
#pragma unroll
    for (int i = 0; i < 9; ++i) v[i] = *(const u32x2*)(x + (size_t)(t0 + r) * D_ + (j16 + 16 * i) * 4);
    float s = 0.f;
    STAMP_VM(); STAMP_NOW();
#pragma unroll
    for (int i = 0; i < 9; ++i)
#pragma unroll
      for (int e = 0; e < 2; ++e) s += lo16(v[i][e]) + hi16(v[i][e]);
    s += __shfl_xor(s, 1); s += __shfl_xor(s, 2); s += __shfl_xor(s, 4); s += __shfl_xor(s, 8);
    const float mean = s * (1.0f / 576.0f);
    float q = 0.f;
#pragma unroll
    for (int i = 0; i < 9; ++i)
#pragma unroll
      for (int e = 0; e < 2; ++e) { float a = lo16(v[i][e]) - mean, b = hi16(v[i][e]) - mean; q += a * a + b * b; }
    q += __shfl_xor(q, 1); q += __shfl_xor(q, 2); q += __shfl_xor(q, 4); q += __shfl_xor(q, 8);
    const float var = q * (1.0f / 576.0f);
    const float mb = rbf(mean), vb = rbf(var);
    const float den = rbf(sqrtf(rbf(vb + 1e-5f)));
    const float rden = 1.0f / den;
    auto qdiv = [&](float a) { float qq = a * rden; return __builtin_fmaf(__builtin_fmaf(-qq, den, a), rden, qq); };
#pragma unroll
    for (int i = 0; i < 9; ++i) {
      u32x2 o;
#pragma unroll
      for (int e = 0; e < 2; ++e) {
        const u32 c = pack2(lo16(v[i][e]) - mb, hi16(v[i][e]) - mb);
        o[e] = pack2(qdiv(lo16(c)), qdiv(hi16(c)));
      }
      *(u32x2*)(tile + r * LN_PITCH + (j16 + 16 * i) * 4) = o;
    }
    STAMP_NOW();
    __syncthreads();
    STAMP_NOW();
    char* xn_g = ws + WS_XN + (size_t)bid * 18 * 1024;
#pragma unroll
    for (int i = 0; i < 5; ++i) {
      const int c = tid + 256 * i;
      if (c < 1152) {
        const int P = c >> 6, w = c & 63;
        const int i16 = w >> 2, kq = (w & 3) ^ blk_swz(w >> 2);
        const u16* src = tile + i16 * LN_PITCH + 32 * P + 4 * kq;
        u32x2 a = *(const u32x2*)src, b = *(const u32x2*)(src + 16);
        *(u32x4*)(xn_g + (size_t)c * 16) = u32x4{a[0], a[1], b[0], b[1]};
      }
    }
    STAMP_NOW();
    u16* xt_g = (u16*)(ws + WS_XNT);
#pragma unroll
    for (int i = 0; i < 5; ++i) {
      const int c = tid + 256 * i;
      if (c < 1152) {
        const int d = c >> 1, tq = c & 1;
        u32x4 o;
#pragma unroll
        for (int j = 0; j < 4; ++j) {
          u32 lo = tile[(tq * 8 + 2 * j) * LN_PITCH + d], hi = tile[(tq * 8 + 2 * j + 1) * LN_PITCH + d];
          o[j] = lo | (hi << 16);
        }
        *(u32x4*)(xt_g + (size_t)d * XT_PITCH + 128 + t0 + tq * 8) = o;
      }
    }
    if (tid < 72) {
      int c = bid * 72 + tid;
      *(u32x4*)(xt_g + (size_t)(c >> 4) * XT_PITCH + (c & 15) * 8) = u32x4{0u, 0u, 0u, 0u};
    }
    STAMP_NOW(); STAMP_NOW(); STAMP_RT();
    STAMP_OUT(0, ws);
  } else if (bid < LN_BLOCKS + KG_BLOCKS) {
    const int d = bid - LN_BLOCKS;
    float* red = (float*)smem;
    float* kv = red + 4 * 768;
    const int i16 = lane & 15, kq = lane >> 4;
    const size_t o = (size_t)d * N_ + wid * 32 + kq * 8;
    const u32x4 vAr = *(const u32x4*)(A_re + o), vAi = *(const u32x4*)(A_im + o);
    const u32x4 vBr = *(const u32x4*)(B_re + o), vBi = *(const u32x4*)(B_im + o);
    const u32x4 vCr = *(const u32x4*)(C_re + o), vCi = *(const u32x4*)(C_im + o);
    const u32x4 ti = *(const u32x4*)(kIdxTab.v + tid * 4);
    const u32 tt = kIdxTab.v[1024 + (tid & 7)];
    const float dt = rbf(expf(bf2f(log_dt[d])));
    f32x4 acc[3] = {f32x4{0, 0, 0, 0}, f32x4{0, 0, 0, 0}, f32x4{0, 0, 0, 0}};
    STAMP_VM(); STAMP_NOW();
    float posA[3];
#pragma unroll
    for (int t = 0; t < 3; ++t) posA[t] = (float)kg_row_pos(16 * t + i16);
    const float posB = (float)(8 * i16);
#pragma unroll
    for (int hf = 0; hf < 2; ++hf) {
      u32x4 Bh, Bl, Ah[3], Al[3];
#pragma unroll
      for (int gg = 0; gg < 4; ++gg) {
        const int g = hf * 4 + gg, e = g >> 1;
        const float lr = dt * ((g & 1) ? hi16(vAr[e]) : lo16(vAr[e])), li = dt * ((g & 1) ? hi16(vAi[e]) : lo16(vAi[e]));
        const float br = dt * ((g & 1) ? hi16(vBr[e]) : lo16(vBr[e])), bi = dt * ((g & 1) ? hi16(vBi[e]) : lo16(vBi[e]));
        const float cr = (g & 1) ? hi16(vCr[e]) : lo16(vCr[e]), ci = (g & 1) ? hi16(vCi[e]) : lo16(vCi[e]);
        const float kr = cr * br - ci * bi, ki = cr * bi + ci * br;
        const float lr2 = lr * 1.44269504088896340736f;
        const float lh = li * 0.15915494309189535f;
        float zr, zi;
        cexp_pos(lr2, lh, posB, zr, zi);
        {
          const float xr = kr * zr - ki * zi, xi = -(kr * zi + ki * zr);
          const u32 h = pack2(xr, xi);
          Bh[gg] = h; Bl[gg] = pack2(xr - lo16(h), xi - hi16(h));
        }
#pragma unroll
        for (int t = 0; t < 3; ++t) {
          float er, ei;
          cexp_pos(lr2, lh, posA[t], er, ei);
          const u32 h = pack2(er, ei);
          Ah[t][gg] = h; Al[t][gg] = pack2(er - lo16(h), ei - hi16(h));
        }
      }
#pragma unroll
      for (int t = 0; t < 3; ++t) {
        acc[t] = __builtin_amdgcn_mfma_f32_16x16x32_bf16(__builtin_bit_cast(s16x8, Al[t]), __builtin_bit_cast(s16x8, Bh), acc[t], 0, 0, 0);
        acc[t] = __builtin_amdgcn_mfma_f32_16x16x32_bf16(__builtin_bit_cast(s16x8, Ah[t]), __builtin_bit_cast(s16x8, Bl), acc[t], 0, 0, 0);
        acc[t] = __builtin_amdgcn_mfma_f32_16x16x32_bf16(__builtin_bit_cast(s16x8, Ah[t]), __builtin_bit_cast(s16x8, Bh), acc[t], 0, 0, 0);
      }
    }
    STAMP_NOW();
#pragma unroll
    for (int t = 0; t < 3; ++t)
#pragma unroll
      for (int j = 0; j < 4; ++j) red[wid * 768 + (16 * t + 4 * kq + j) * 16 + i16] = acc[t][j];
    __syncthreads();
#pragma unroll
    for (int i = 0; i < 3; ++i) { int e = tid + 256 * i; kv[e] = (red[e] + red[768 + e]) + (red[2 * 768 + e] + red[3 * 768 + e]); }
    if (tid == 0) kv[768] = 0.f;
    __syncthreads();
    STAMP_NOW();
    STAMP_NOW();
    u32* kx = (u32*)(ws + WS_KX) + (size_t)d * 2 * KX_NW;
    {
      u32x4 hi, lo;
#pragma unroll
      for (int j = 0; j < 4; ++j) {
        const float k0 = *(const float*)((const char*)kv + (ti[j] & 0xffffu)), k1 = *(const float*)((const char*)kv + (ti[j] >> 16));
        const u32 h = pack2(k0, k1);
        hi[j] = h; lo[j] = pack2(k0 - lo16(h), k1 - hi16(h));
      }
      *(u32x4*)(kx + tid * 4) = hi;
      *(u32x4*)(kx + KX_NW + tid * 4) = lo;
      if (tid < 8) {
        const float k0 = *(const float*)((const char*)kv + (tt & 0xffffu)), k1 = *(const float*)((const char*)kv + (tt >> 16));
        const u32 h = pack2(k0, k1);
        kx[1024 + tid] = h; kx[KX_NW + 1024 + tid] = pack2(k0 - lo16(h), k1 - hi16(h));
      }
    }
    STAMP_NOW(); STAMP_NOW(); STAMP_RT();
    STAMP_OUT(0, ws);
  } else {
    const int e8 = (bid - (LN_BLOCKS + KG_BLOCKS)) * 256 + tid;
    const int sign = e8 >> 12, e = e8 & 4095;
    float* lut = (float*)(ws + WS_LUT);
    int a = LUT_LO + e;
    if (a > LUT_HI) a = LUT_HI;
    float h = bf2f((u16)(a | (sign << 15)));
    float ex = rbf(expf(-fabsf(h)));
    float lp = rbf(log1pf(ex));
    float sp = rbf(lp + (h > 0.f ? h : 0.f));
    float th = tanhf(sp);
    lut[e8] = rbf(th);
  }
}

typedef __attribute__((address_space(3))) s16x4 lds_s16x4;
template <int N> DI void wait_vm() { asm volatile("s_waitcnt vmcnt(%0)" ::"n"(N) : "memory"); }
DI void raw_barrier() { asm volatile("s_waitcnt lgkmcnt(0)" ::: "memory"); __builtin_amdgcn_s_barrier(); }
#define SCHED0() __builtin_amdgcn_sched_barrier(0)

constexpr int CONV_BLOCKS = 256;
constexpr int CV_U_BYTES = 5 * 1024;
constexpr int CV_K_BYTES = 17 * 1024;
constexpr int CV_ST_BYTES = CV_U_BYTES + CV_K_BYTES;
constexpr int CV_Y_BYTES = 4 * 2048 * 4;

DI void conv_stage(char* st, int d, int g, int lane, const char* __restrict__ ws) {
  const char* ug = ws + WS_XNT + (size_t)d * XT_PITCH * 2;
  const char* kg = ws + WS_KX + (size_t)d * 2 * KX_NW * 4;
#pragma unroll
  for (int i = 0; i < 4; ++i) {
    const int ins = g + 4 * i;
    if (ins < 5)
      __builtin_amdgcn_global_load_lds((const u32*)(ug + ins * 1024 + lane * 16), (u32*)(st + ins * 1024), 16, 0, 0);
    else if (ins < 10)
      __builtin_amdgcn_global_load_lds((const u32*)(kg + (ins - 5) * 1024 + lane * 16), (u32*)(st + CV_U_BYTES + (ins - 5) * 1024), 16, 0, 0);
    else if (ins < 15)
      __builtin_amdgcn_global_load_lds((const u32*)(kg + KX_NW * 4 + (ins - 10) * 1024 + lane * 16),
                                       (u32*)(st + CV_U_BYTES + 2 * KX_NW * 4 + (ins - 10) * 1024), 16, 0, 0);
  }
}
DI void conv_odd(char* st, int t256) {
  u32* ka = (u32*)(st + CV_U_BYTES);
#pragma unroll
  for (int hl = 0; hl < 2; ++hl) {
    const u32* e = ka + hl * 2 * KX_NW;
    u32* o = ka + (hl * 2 + 1) * KX_NW;
    const int w = t256 * 4;
    const u32x4 v = *(const u32x4*)(e + w);
    const u32 nx = e[w + 4];
    *(u32x4*)(o + w) = u32x4{__builtin_amdgcn_alignbyte(v[1], v[0], 2), __builtin_amdgcn_alignbyte(v[2], v[1], 2),
                             __builtin_amdgcn_alignbyte(v[3], v[2], 2), __builtin_amdgcn_alignbyte(nx, v[3], 2)};
    if (t256 < 3) o[1024 + t256] = __builtin_amdgcn_alignbyte(e[1025 + t256], e[1024 + t256], 2);
  }
}

DI void conv_mfma(const char* st, float* ybuf, int g, int lane) {
  const int i16 = lane & 15, kq = lane >> 4;
  const char* us = st + 256;
  s16x8 F[16];
#pragma unroll
  for (int dl = 0; dl < 16; ++dl) F[dl] = *(const s16x8*)(us + (16 * dl - 4 * g + i16 - kq) * 16);
  const int rho = i16 & 7, hl = i16 >> 3;
  const u32* ks = (const u32*)(st + CV_U_BYTES) + (hl * 2 + ((1 - rho) & 1)) * KX_NW + ((2047 - 8 * (4 * g + kq) - rho) >> 1);
  f32x4 acc[16];
#pragma unroll
  for (int R = 0; R < 16; ++R) acc[R] = f32x4{0, 0, 0, 0};
#pragma unroll
  for (int q = 0; q < 16; ++q) {
    const u32* kp = ks - 64 * q;
    u32x4 v = {kp[0], kp[1], kp[2], kp[3]};
    const s16x8 B = __builtin_bit_cast(s16x8, v);
#pragma unroll
    for (int R = q; R < 16; ++R)
      acc[R] = __builtin_amdgcn_mfma_f32_16x16x32_bf16(F[R - q], B, acc[R], 0, 0, 0);
  }
#pragma unroll
  for (int R = 0; R < 16; ++R) {
    const float s0 = hl ? acc[R][0] : acc[R][2], s1 = hl ? acc[R][1] : acc[R][3];
    const float k0 = hl ? acc[R][2] : acc[R][0], k1 = hl ? acc[R][3] : acc[R][1];
    const float r0 = __builtin_bit_cast(float, __builtin_amdgcn_update_dpp(0, __builtin_bit_cast(int, s0), 0x128, 0xf, 0xf, false));
    const float r1 = __builtin_bit_cast(float, __builtin_amdgcn_update_dpp(0, __builtin_bit_cast(int, s1), 0x128, 0xf, 0xf, false));
    float* yp = ybuf + g * 2048 + 128 * R + 32 * kq + 16 * hl + rho;
    yp[0] = k0 + r0;
    yp[8] = k1 + r1;
  }
}

DI void conv_finish(const char* st, const float* ybuf, int d, int t, float dsk, char* __restrict__ ws) {
  const s16x8 uv = *(const s16x8*)(st + 256 + t * 2);
  const f32x4* yb = (const f32x4*)(ybuf + t);
  f32x4 s0 = (yb[0] + yb[512]) + (yb[1024] + yb[1536]);
  f32x4 s1 = (yb[1] + yb[513]) + (yb[1025] + yb[1537]);
  float s[8] = {s0[0], s0[1], s0[2], s0[3], s1[0], s1[1], s1[2], s1[3]};
  u32x4 o;
#pragma unroll
  for (int j = 0; j < 4; ++j) {
    float r0 = rbf(s[2 * j]) + rbf(bf2f((u16)uv[2 * j]) * dsk);
    float r1 = rbf(s[2 * j + 1]) + rbf(bf2f((u16)uv[2 * j + 1]) * dsk);
    o[j] = pack2(r0, r1);
  }
  *(u32x4*)(ws + WS_YAT + ((size_t)d * L_ + t) * 2) = o;
}

DI void conv_role(char* smem, int cb, const u16* __restrict__ Dskip, char* __restrict__ ws) {
  const int tid = threadIdx.x, lane = tid & 63, wid = __builtin_amdgcn_readfirstlane(tid >> 6);
  const int ch = wid >> 2, g = wid & 3;
  STAMP_DECL STAMP_RT(); STAMP_NOW();
  const bool three = cb < 64;
  const int d = cb * 2 + ch;
  const int d3 = 512 + cb;
  char* st = smem + ch * CV_ST_BYTES;
  char* st3 = smem + 2 * CV_ST_BYTES;
  float* ybuf = (float*)(smem + 3 * CV_ST_BYTES + ch * CV_Y_BYTES);
  conv_stage(st, d, g, lane, ws);
  const float dsk = bf2f(Dskip[d]);
  wait_vm<0>();
  if (three && ch == 0) conv_stage(st3, d3, g, lane, ws);
  raw_barrier();
  conv_odd(st, tid & 255);
  raw_barrier();
  STAMP_NOW();
  conv_mfma(st, ybuf, g, lane);
  STAMP_NOW();
  wait_vm<0>();
  raw_barrier();
  STAMP_NOW();
  conv_finish(st, ybuf, d, (tid & 255) * 8, dsk, ws);
  if (three) {
    float* yb3 = (float*)(smem + 3 * CV_ST_BYTES);
    if (ch == 1) conv_odd(st3, tid & 255);
    raw_barrier();
    if (ch == 0) conv_mfma(st3, yb3, g, lane);
    raw_barrier();
    if (ch == 0) conv_finish(st3, yb3, d3, tid * 8, bf2f(Dskip[d3]), ws);
  }
  STAMP_NOW(); STAMP_NOW(); STAMP_RT();
  STAMP_OUT(1, ws);
}

template <int MODE> struct GemmCfg {
  static constexpr int BM = MODE ? 128 : 256;
  static constexpr int NPA = MODE ? F_ / 32 : D_ / 32;
  static constexpr int LDB = MODE ? D_ : F_;
  static constexpr int A_BYTES = BM * 128;
  static constexpr int B_BYTES = 64 * 288;
  static constexpr int STAGE = A_BYTES + B_BYTES;
  static constexpr int NSTAGE = MODE ? 4 : 3;
  static constexpr int NT = MODE ? 18 : 9;
  static constexpr int A_IPW = BM / 64;
  static constexpr int SMEM = NSTAGE * STAGE;
};

constexpr int BIAS_LDS = 51200;
constexpr int LUT_LDS = 61440;
template <int NB> struct Frag { s16x8 a[4]; s16x8 b[NB]; };

template <int MODE, int NB>
DI void gemm_wave_body(char* smem, const char* __restrict__ Ag, const u16* __restrict__ Bg, const char* __restrict__ lutg,
                       const u16* __restrict__ biasg,
                       int wid, int lane, int wm, int wk, int pb0  , int sblk  ,
                       f32x4 (&acc)[4][5]) {
  using C = GemmCfg<MODE>;
  const int i16 = lane & 15, kq = lane >> 4;
  const bool extraB = wid < 2;
  int boff[3];
#pragma unroll
  for (int i = 0; i < 3; ++i) {
    int Lc = (wid + 8 * i) * 64 + lane;
    int k = Lc / 18, c8 = Lc % 18;
    boff[i] = k * C::LDB + c8 * 8;
  }
  const char* asrc = Ag + lane * 16;
  const int a_rd = (wm * 8) * 1024 + i16 * 64 + ((kq ^ blk_swz(i16)) * 16);
  const int q4 = i16 >> 2, p4 = i16 & 3;
  const int b_rd = C::A_BYTES + (4 * kq + q4) * 288 + p4 * 8 + pb0 * 32;
  const int b_rs = C::A_BYTES + (4 * kq + q4) * 288 + p4 * 8 + sblk * 32;

  auto stage = [&](int kt, int buf) {
    char* sb = smem + buf * C::STAGE;
#pragma unroll
    for (int i = 0; i < C::A_IPW; ++i) {
      const int b = wid * C::A_IPW + i;
      __builtin_amdgcn_global_load_lds((const u32*)(asrc + ((size_t)(b >> 1) * C::NPA + 2 * kt + (b & 1)) * 1024),
                                       (u32*)(sb + b * 1024), 16, 0, 0);
    }
    const u16* bk = Bg + (size_t)kt * 64 * C::LDB;
#pragma unroll
    for (int i = 0; i < 2; ++i)
      __builtin_amdgcn_global_load_lds((const u32*)(bk + boff[i]), (u32*)(sb + C::A_BYTES + (wid + 8 * i) * 1024), 16, 0, 0);
    if (extraB)
      __builtin_amdgcn_global_load_lds((const u32*)(bk + boff[2]), (u32*)(sb + C::A_BYTES + (wid + 16) * 1024), 16, 0, 0);
  };
  auto readf = [&](Frag<NB>& f, int buf, int ks) {
    const char* sb = smem + buf * C::STAGE;
#pragma unroll
    for (int nb = 0; nb < NB; ++nb) {
      const int base = (nb < 4 ? b_rd + nb * 32 : b_rs) + ks * 32 * 288;
      s16x4 lo = __builtin_amdgcn_ds_read_tr16_b64_v4i16((lds_s16x4*)(sb + base));
      s16x4 hi = __builtin_amdgcn_ds_read_tr16_b64_v4i16((lds_s16x4*)(sb + base + 16 * 288));
      f.b[nb] = s16x8{lo[0], lo[1], lo[2], lo[3], hi[0], hi[1], hi[2], hi[3]};
    }
#pragma unroll
    for (int mi = 0; mi < 4; ++mi) f.a[mi] = *(const s16x8*)(sb + a_rd + (mi * 2 + ks) * 1024);
  };
  auto mma = [&](const Frag<NB>& f) {
#pragma unroll
    for (int mi = 0; mi < 4; ++mi)
#pragma unroll
      for (int nb = 0; nb < NB; ++nb)
        acc[mi][nb] = __builtin_amdgcn_mfma_f32_16x16x32_bf16(f.b[nb], f.a[mi], acc[mi][nb], 0, 0, 0);
  };
  constexpr int NW0 = C::A_IPW + 2;
  Frag<NB> f0, f1;
  if (MODE == 0) {
    stage(0, 1); stage(1, 2);
    if (extraB) wait_vm<NW0 + 1>(); else wait_vm<NW0>();
    raw_barrier();
    readf(f0, 1, 0);
#pragma unroll
    for (int kt = 0; kt < 9; ++kt) {
      if (kt + 2 < 9) stage(kt + 2, (kt + 3) % 3);
      if (kt == 7) {
#pragma unroll
        for (int i = 0; i < 4; ++i)
          __builtin_amdgcn_global_load_lds((const u32*)(lutg + (wid * 4 + i) * 1024 + lane * 16), (u32*)(smem + LUT_LDS + (wid * 4 + i) * 1024), 16, 0, 0);
        __builtin_amdgcn_global_load_lds((const u32*)(biasg + (lane < 18 ? lane : 17) * 8), (u32*)(smem + BIAS_LDS + wid * 1024), 16, 0, 0);
      }
      readf(f1, (kt + 1) % 3, 1);
      SCHED0();
      mma(f0);
      SCHED0();
      if (kt + 1 < 9) {
        if (kt + 2 < 9) { if (extraB) wait_vm<NW0 + 1>(); else wait_vm<NW0>(); }
        else wait_vm<5>();
        raw_barrier();
        readf(f0, (kt + 2) % 3, 0);
      } else {
        wait_vm<0>();
        raw_barrier();
      }
      SCHED0();
      mma(f1);
      SCHED0();
    }
  } else {
    stage(0, 0); stage(1, 1); stage(2, 2);
    if (extraB) wait_vm<2 * (NW0 + 1)>(); else wait_vm<2 * NW0>();
    raw_barrier();
    readf(f0, 0, wk);
#pragma unroll
    for (int j = 0; j < 18; ++j) {
      if (j + 3 < 18) stage(j + 3, (j + 3) % 4);
      if (j + 1 < 18) {
        if (j + 3 < 18) { if (extraB) wait_vm<2 * (NW0 + 1)>(); else wait_vm<2 * NW0>(); }
        else if (j + 2 < 18) { if (extraB) wait_vm<NW0 + 1>(); else wait_vm<NW0>(); }
        else wait_vm<0>();
        raw_barrier();
        if (j & 1) readf(f0, (j + 1) % 4, wk); else readf(f1, (j + 1) % 4, wk);
      }
      SCHED0();
      if (j & 1) mma(f1); else mma(f0);
      SCHED0();
    }
    raw_barrier();
  }
}

__global__ __launch_bounds__(512) void gemm1_conv_kernel(const u16* __restrict__ w1, const u16* __restrict__ bias,
                                                         const u16* __restrict__ Dskip, char* __restrict__ ws) {
  using C = GemmCfg<0>;
  static_assert(3 * CV_ST_BYTES + 2 * CV_Y_BYTES <= C::SMEM, "lds");
  __shared__ __attribute__((aligned(1024))) char smem[C::SMEM];
  if (blockIdx.x < CONV_BLOCKS) { conv_role(smem, blockIdx.x, Dskip, ws); return; }
  const int tid = threadIdx.x, lane = tid & 63, wid = __builtin_amdgcn_readfirstlane(tid >> 6);
  const int bid = blockIdx.x - CONV_BLOCKS;
  const int xcd = bid & 7, idx = bid >> 3;
  const int nt = 4 * xcd + (idx & 3), m0 = (idx >> 2) * 256;
  const int n0 = nt * 144;
  const char* Ag = ws + WS_XN + (size_t)(m0 >> 4) * C::NPA * 1024;
  const u16* Bg = w1 + n0;

  const int wn = wid >> 2;
  const int wm = wid & 3;
  const int odd = nt & 1;
  const int pb0 = odd + 4 * wn;
  const int sblk = odd ? 0 : 8;
  f32x4 acc[4][5];
#pragma unroll
  for (int mi = 0; mi < 4; ++mi)
#pragma unroll
    for (int nb = 0; nb < 5; ++nb) acc[mi][nb] = f32x4{0, 0, 0, 0};
  const int i16 = lane & 15, kq = lane >> 4;
  const char* lutg = ws + WS_LUT;
  STAMP_DECL STAMP_RT(); STAMP_NOW();
  if (wn == 0) gemm_wave_body<0, 5>(smem, Ag, Bg, lutg, bias + n0, wid, lane, wm, 0, pb0, sblk, acc);
  else         gemm_wave_body<0, 4>(smem, Ag, Bg, lutg, bias + n0, wid, lane, wm, 0, pb0, sblk, acc);
  STAMP_NOW();
  f32x2 bf_[5][2];
#pragma unroll
  for (int nb = 0; nb < 5; ++nb) {
    const int blk = nb < 4 ? pb0 + nb : sblk;
    const u32x2 bb = *(const u32x2*)(smem + BIAS_LDS + wid * 1024 + (blk * 16 + 4 * kq) * 2);
#pragma unroll
    for (int e = 0; e < 2; ++e) { bf_[nb][e] = f32x2{lo16(bb[e]), hi16(bb[e])}; asm volatile("" : "+v"(bf_[nb][e])); }
  }

  const char* lut = smem + (LUT_LDS - LUT_LO * 4);
  char* act = ws + WS_ACT;
  const int inblk = i16 * 64 + ((kq ^ blk_swz(i16)) * 16);
#pragma unroll
  for (int mi = 0; mi < 4; ++mi) {
    float hf[5][4], th[5][4];
#pragma unroll
    for (int nb = 0; nb < 5; ++nb) {
      if (nb < 4 || wn == 0) {
#pragma unroll
        for (int e = 0; e < 2; ++e) {
          u32 a = pack2(acc[mi][nb][2 * e], acc[mi][nb][2 * e + 1]);
          u32 h = pack2(lo16(a) + bf_[nb][e][0], hi16(a) + bf_[nb][e][1]);
          hf[nb][2 * e] = lo16(h); hf[nb][2 * e + 1] = hi16(h);
        }
#pragma unroll
        for (int j = 0; j < 4; ++j) {
          const u32 u = __float_as_uint(hf[nb][j]);
          const float m = __builtin_amdgcn_fmed3f(__builtin_fabsf(hf[nb][j]), 0.0009765625f, 128.0f);
          u32 o;
          asm("v_lshl_add_u32 %0, %1, 14, %2" : "=v"(o) : "v"(u >> 31), "v"(__float_as_uint(m) >> 14));
          th[nb][j] = *(const float*)(lut + o);
        }
      }
    }
    const size_t mbg = (size_t)((m0 >> 4) + wm * 4 + mi);
    u32 r[5][2];
#pragma unroll
    for (int nb = 0; nb < 5; ++nb)
      if (nb < 4 || wn == 0) {
#pragma unroll
        for (int e = 0; e < 2; ++e) r[nb][e] = pack2(hf[nb][2 * e] * th[nb][2 * e], hf[nb][2 * e + 1] * th[nb][2 * e + 1]);
      }
#pragma unroll
    for (int pr = 0; pr < 2; ++pr) {
      const int P = (n0 + (pb0 + 2 * pr) * 16) >> 5;
      st16_wt(act + (mbg * 144 + P) * 1024 + inblk, u32x4{r[2 * pr][0], r[2 * pr][1], r[2 * pr + 1][0], r[2 * pr + 1][1]});
    }
    if (wn == 0) {
      const int nb16 = (n0 >> 4) + sblk;
      st8_wt(act + (mbg * 144 + (nb16 >> 1)) * 1024 + inblk + (nb16 & 1) * 8, u32x2{r[4][0], r[4][1]});
    }
  }
  STAMP_NOW(); STAMP_VM(); STAMP_NOW(); STAMP_RT();
  STAMP_OUT(2, ws);
}

__global__ __launch_bounds__(512) void gemm2_kernel(const u16* __restrict__ w2, const u16* __restrict__ x,
                                                    char* __restrict__ ws, u16* __restrict__ out) {
  using C = GemmCfg<1>;
  __shared__ __attribute__((aligned(1024))) char smem[C::SMEM];
  const int tid = threadIdx.x, lane = tid & 63, wid = __builtin_amdgcn_readfirstlane(tid >> 6);
  const int bid = blockIdx.x;
  const int xcd = bid & 7, idx = bid >> 3;
  const int ksplit = xcd >> 1, mt = (xcd & 1) * 8 + (idx >> 2), nt = idx & 3;
  const int m0 = mt * 128, n0 = nt * 144;
  const char* Ag = ws + WS_ACT + ((size_t)(m0 >> 4) * C::NPA + ksplit * 36) * 1024;
  const u16* Bg = w2 + (size_t)ksplit * 1152 * C::LDB + n0;

  const int wn = wid >> 2;
  const int wmv = wid & 3;
  const int wk = wmv >> 1, wm = wmv & 1;
  const int odd = nt & 1;
  const int pb0 = odd + 4 * wn;
  const int sblk = odd ? 0 : 8;
  f32x4 acc[4][5];
#pragma unroll
  for (int mi = 0; mi < 4; ++mi)
#pragma unroll
    for (int nb = 0; nb < 5; ++nb) acc[mi][nb] = f32x4{0, 0, 0, 0};
  if (wn == 0) gemm_wave_body<1, 5>(smem, Ag, Bg, nullptr, nullptr, wid, lane, wm, wk, pb0, sblk, acc);
  else         gemm_wave_body<1, 4>(smem, Ag, Bg, nullptr, nullptr, wid, lane, wm, wk, pb0, sblk, acc);

  const int i16 = lane & 15, kq = lane >> 4;
  f32x4* red = (f32x4*)smem;
  constexpr int YT_OFF = 80 * 1024, YT_PITCH = 288;
  constexpr int FLAG_OFF = YT_OFF + 144 * YT_PITCH;
  const int slot = wn * 2 + wm;
  if (wk == 1) {
#pragma unroll
    for (int mi = 0; mi < 4; ++mi)
#pragma unroll
      for (int nb = 0; nb < 5; ++nb)
        if (nb < 4 || wn == 0) red[(slot * 20 + mi * 5 + nb) * 64 + lane] = acc[mi][nb];
  }
  __syncthreads();
  char* partb = ws + WS_PART;
  constexpr size_t PSTRIDE = (size_t)L_ * D_ * 4;
  if (wk == 0) {
#pragma unroll
    for (int mi = 0; mi < 4; ++mi)
#pragma unroll
      for (int nb = 0; nb < 5; ++nb)
        if (nb < 4 || wn == 0) {
          acc[mi][nb] += red[(slot * 20 + mi * 5 + nb) * 64 + lane];
          const size_t mbg = (size_t)((m0 >> 4) + wm * 4 + mi);
          const int nbg = (n0 >> 4) + (nb < 4 ? pb0 + nb : sblk);
          st16_wt(partb + ksplit * PSTRIDE + (mbg * 36 + nbg) * 1024 + i16 * 64 + kq * 16, __builtin_bit_cast(u32x4, acc[mi][nb]));
        }
  }
}

__global__ __launch_bounds__(256) void final_kernel(const u16* __restrict__ x, const char* __restrict__ ws, u16* __restrict__ out) {
  __shared__ __attribute__((aligned(16))) u16 yt[64 * 72];
  const int tid = threadIdx.x;
  const int tt = blockIdx.x / 9, dt = blockIdx.x % 9;
  const int t0 = tt * 64, d0 = dt * 64;
  const u16* yat = (const u16*)(ws + WS_YAT);
  const char* part = ws + WS_PART;
  u32x4 yv[2], xv[2];
  f32x4 p[2][4][2];
#pragma unroll
  for (int i = 0; i < 2; ++i) {
    const int c = tid + 256 * i;
    const int r = c >> 3, ch = c & 7;
    yv[i] = *(const u32x4*)(yat + (size_t)(d0 + r) * L_ + t0 + ch * 8);
    xv[i] = *(const u32x4*)(x + (size_t)(t0 + r) * D_ + d0 + ch * 8);
    const int t = t0 + r, d = d0 + ch * 8;
    const size_t po = ((size_t)(t >> 4) * 36 + (d >> 4)) * 1024 + (t & 15) * 64 + ((d >> 2) & 3) * 16;
#pragma unroll
    for (int s = 0; s < 4; ++s) {
      p[i][s][0] = *(const f32x4*)(part + (size_t)s * L_ * D_ * 4 + po);
      p[i][s][1] = *(const f32x4*)(part + (size_t)s * L_ * D_ * 4 + po + 16);
    }
  }
#pragma unroll
  for (int i = 0; i < 2; ++i) {
    const int c = tid + 256 * i;
    *(u32x4*)(yt + (c >> 3) * 72 + (c & 7) * 8) = yv[i];
  }
  __syncthreads();
#pragma unroll
  for (int i = 0; i < 2; ++i) {
    const int c = tid + 256 * i;
    const int r = c >> 3, ch = c & 7;
    f32x4 s0 = (p[i][0][0] + p[i][1][0]) + (p[i][2][0] + p[i][3][0]);
    f32x4 s1 = (p[i][0][1] + p[i][1][1]) + (p[i][2][1] + p[i][3][1]);
    float s[8] = {s0[0], s0[1], s0[2], s0[3], s1[0], s1[1], s1[2], s1[3]};
    u32x4 ov;
#pragma unroll
    for (int j = 0; j < 4; ++j) {
      float r2[2];
#pragma unroll
      for (int h = 0; h < 2; ++h) {
        int e = 2 * j + h;
        float xx = h ? hi16(xv[i][j]) : lo16(xv[i][j]);
        float ya = bf2f(yt[(ch * 8 + e) * 72 + r]);
        r2[h] = rbf(xx + ya) + rbf(s[e]);
      }
      ov[j] = pack2(r2[0], r2[1]);
    }
    *(u32x4*)(out + (size_t)(t0 + r) * D_ + d0 + ch * 8) = ov;
  }
}

extern "C" void kernel_launch(void* const* d_in, const int* in_sizes, int n_in,
                              void* d_out, int out_size, void* d_ws, size_t ws_size,
                              hipStream_t stream) {
  const u16* x      = (const u16*)d_in[0];
  const u16* A_re   = (const u16*)d_in[1];
  const u16* A_im   = (const u16*)d_in[2];
  const u16* B_re   = (const u16*)d_in[3];
  const u16* B_im   = (const u16*)d_in[4];
  const u16* C_re   = (const u16*)d_in[5];
  const u16* C_im   = (const u16*)d_in[6];
  const u16* log_dt = (const u16*)d_in[7];
  const u16* Dskip  = (const u16*)d_in[8];
  const u16* w1     = (const u16*)d_in[10];
  const u16* b1     = (const u16*)d_in[11];
  const u16* w2     = (const u16*)d_in[12];
  char* ws = (char*)d_ws;
  prep_kernel<<<PREP_BLOCKS, 256, 0, stream>>>(x, A_re, A_im, B_re, B_im, C_re, C_im, log_dt, ws);
  gemm1_conv_kernel<<<CONV_BLOCKS + 256, 512, 0, stream>>>(w1, b1, Dskip, ws);
  gemm2_kernel<<<256, 512, 0, stream>>>(w2, x, ws, (u16*)d_out);
  final_kernel<<<288, 256, 0, stream>>>(x, ws, (u16*)d_out);
}
```

```cpp
#include <hip/hip_runtime.h>
#include <hip/hip_bf16.h>
#include <stdint.h>

typedef unsigned short u16;
typedef unsigned int u32;
using s16x8 = __attribute__((ext_vector_type(8))) short;
using s16x4 = __attribute__((ext_vector_type(4))) short;
using f32x4 = __attribute__((ext_vector_type(4))) float;
using u32x4 = __attribute__((ext_vector_type(4))) unsigned int;
using u32x2 = __attribute__((ext_vector_type(2))) unsigned int;
typedef u32x4 __attribute__((aligned(4))) u32x4_a4;

#define DI __device__ __forceinline__
#define STAMP_DECL
#define STAMP_NOW()
#define STAMP_RT()
#define STAMP_VM()
#define STAMP_OUT(kid, ws)

constexpr int L_ = 2048, D_ = 576, N_ = 128, F_ = 4608;
constexpr int XT_PITCH = 2048 + 128;
constexpr int KX_NW = 1032;
constexpr int LUT_LO = 0x3A80, LUT_HI = 0x4300;
constexpr int LUT_ENTRIES = 8192;

constexpr size_t WS_CNT  = 0;
constexpr size_t WS_XN   = 1024;
constexpr size_t WS_XNT  = WS_XN + (size_t)L_ * D_ * 2;
constexpr size_t WS_KX   = WS_XNT + (size_t)D_ * XT_PITCH * 2;
constexpr size_t WS_LUT  = WS_KX + (size_t)D_ * 2 * KX_NW * 4;
constexpr size_t WS_YAT  = WS_LUT + LUT_ENTRIES * 4;
constexpr size_t WS_ACT  = WS_YAT + (size_t)D_ * L_ * 2;
constexpr size_t WS_PART = WS_ACT + (size_t)L_ * F_ * 2;
constexpr size_t WS_YP   = WS_PART + (size_t)4 * L_ * D_ * 4;
constexpr size_t WS_W1P  = WS_YP + (size_t)64 * 4 * L_ * 4;
constexpr size_t WS_W2P  = WS_W1P + (size_t)288 * 18432;

DI float bf2f(u16 b) { return __uint_as_float(((u32)b) << 16); }
DI u16 f2bf(float f) {
  __hip_bfloat16 h = __float2bfloat16(f);
  return *reinterpret_cast<u16*>(&h);
}
DI float rbf(float f) { return bf2f(f2bf(f)); }
typedef __bf16 bf16x2_t __attribute__((ext_vector_type(2)));
using f32x2 = __attribute__((ext_vector_type(2))) float;
DI u32 pack2(float lo, float hi) {
  f32x2 v = {lo, hi};
  return __builtin_bit_cast(u32, __builtin_convertvector(v, bf16x2_t));
}
DI float lo16(u32 v) { return __uint_as_float(v << 16); }
DI float hi16(u32 v) { return __uint_as_float(v & 0xffff0000u); }
DI void st16_wt(void* p, u32x4 v) { asm volatile("global_store_dwordx4 %0, %1, off sc1\n\ts_nop 1" ::"v"(p), "v"(v) : "memory"); }
DI void st8_wt(void* p, u32x2 v) { asm volatile("global_store_dwordx2 %0, %1, off sc1\n\ts_nop 1" ::"v"(p), "v"(v) : "memory"); }

DI int blk_swz(int i16) { return (-(i16 >> 2)) & 3; }

DI void mfma_ip(f32x4& acc, const s16x8& a, const s16x8& b) {
  asm volatile("s_nop 1\n\tv_mfma_f32_16x16x32_bf16 %0, %1, %2, %0" : "+v"(acc) : "v"(a), "v"(b));
}
DI void mfma_drain() { asm volatile("s_nop 15\n\ts_nop 15" ::: "memory"); }

DI void cexp_pos(float lr2, float lh, float pos, float& er, float& ei) {
  const float mag = __builtin_amdgcn_exp2f(lr2 * pos);
  const float f = __builtin_amdgcn_fractf(lh * pos);
  er = mag * __builtin_amdgcn_cosf(f);
  ei = mag * __builtin_amdgcn_sinf(f);
}
DI int kg_row_pos(int row) {
  if (row < 16) return 128 * (row >> 3) + (row & 7);
  if (row < 24) { int r = row - 16; return 256 + 128 * (r >> 2) + 2 * (r & 3); }
  if (row < 32) { int r = row - 24; return 512 + 128 * (r >> 1) + 4 * (r & 1); }
  if (row < 40) return 1024 + 128 * (row - 32);
  if (row == 40) return 2048;
  return 0;
}
struct KIdxTab { u32 v[1088]; };
constexpr int kg_bf16_round(int l) {
  int sft = l >= 1024 ? 3 : l >= 512 ? 2 : l >= 256 ? 1 : 0;
  if (sft == 0) return l;
  int half = 1 << (sft - 1), q = l >> sft;
  return ((l + half - 1 + (q & 1)) >> sft) << sft;
}
constexpr int kg_pos_index(int p) {
  int sg = p >= 2048 ? 4 : p >= 1024 ? 3 : p >= 512 ? 2 : p >= 256 ? 1 : 0;
  int base = sg ? (128 << sg) : 0, rowbase = sg ? 8 + 8 * sg : 0;
  int q = p - base;
  int row = rowbase + (q >> 7) * (8 >> sg) + ((q & 7) >> sg);
  return row * 16 + ((q & 127) >> 3);
}
constexpr KIdxTab make_kidx() {
  KIdxTab t{};
  for (int w = 0; w < 1088; ++w) {
    int l0 = 2047 - 2 * w, l1 = 2046 - 2 * w;
    u32 i0 = l0 >= 0 ? kg_pos_index(kg_bf16_round(l0)) : 768, i1 = l1 >= 0 ? kg_pos_index(kg_bf16_round(l1)) : 768;
    t.v[w] = (i0 * 4) | ((i1 * 4) << 16);
  }
  return t;
}
__device__ const KIdxTab kIdxTab = make_kidx();

struct S4Params { const u16 *A_re, *A_im, *B_re, *B_im, *C_re, *C_im, *log_dt; };
struct KgFrag { u32x4 Bh, Bl, Ah[3], Al[3]; };

DI void kg_load_params(const S4Params& P, int d, int n0, u32x4 (&v)[6]) {
  const size_t o = (size_t)d * N_ + n0;
  v[0] = *(const u32x4*)(P.A_re + o); v[1] = *(const u32x4*)(P.A_im + o);
  v[2] = *(const u32x4*)(P.B_re + o); v[3] = *(const u32x4*)(P.B_im + o);
  v[4] = *(const u32x4*)(P.C_re + o); v[5] = *(const u32x4*)(P.C_im + o);
}
DI void kg_mode(const u32x4 (&v)[6], int g, float dt, const float (&posA)[3], float posB, KgFrag& f) {
  const int e = g >> 1, gg = g & 3;
  const float lr = dt * ((g & 1) ? hi16(v[0][e]) : lo16(v[0][e])), li = dt * ((g & 1) ? hi16(v[1][e]) : lo16(v[1][e]));
  const float br = dt * ((g & 1) ? hi16(v[2][e]) : lo16(v[2][e])), bi = dt * ((g & 1) ? hi16(v[3][e]) : lo16(v[3][e]));
  const float cr = (g & 1) ? hi16(v[4][e]) : lo16(v[4][e]), ci = (g & 1) ? hi16(v[5][e]) : lo16(v[5][e]);
  const float kr = cr * br - ci * bi, ki = cr * bi + ci * br;
  const float lr2 = lr * 1.44269504088896340736f;
  const float lh = li * 0.15915494309189535f;
  float zr, zi;
  cexp_pos(lr2, lh, posB, zr, zi);
  {
    const float xr = kr * zr - ki * zi, xi = -(kr * zi + ki * zr);
    const u32 h = pack2(xr, xi);
    f.Bh[gg] = h; f.Bl[gg] = pack2(xr - lo16(h), xi - hi16(h));
  }
#pragma unroll
  for (int t = 0; t < 3; ++t) {
    float er, ei;
    cexp_pos(lr2, lh, posA[t], er, ei);
    const u32 h = pack2(er, ei);
    f.Ah[t][gg] = h; f.Al[t][gg] = pack2(er - lo16(h), ei - hi16(h));
  }
}
DI void kg_mma(const KgFrag& f, f32x4 (&acc)[3]) {
#pragma unroll
  for (int t = 0; t < 3; ++t) {
    mfma_ip(acc[t], __builtin_bit_cast(s16x8, f.Al[t]), __builtin_bit_cast(s16x8, f.Bh));
    mfma_ip(acc[t], __builtin_bit_cast(s16x8, f.Ah[t]), __builtin_bit_cast(s16x8, f.Bl));
    mfma_ip(acc[t], __builtin_bit_cast(s16x8, f.Ah[t]), __builtin_bit_cast(s16x8, f.Bh));
  }
}
template <int NTHR> DI void kg_gather_store(const float* kv, int d, int t, char* __restrict__ ws) {
  u32* kx = (u32*)(ws + WS_KX) + (size_t)d * 2 * KX_NW;
#pragma unroll
  for (int i = 0; i < 256 / NTHR; ++i) {
    const int w = (i * NTHR + t) * 4;
    const u32x4 ti = *(const u32x4*)(kIdxTab.v + w);
    u32x4 hi, lo;
#pragma unroll
    for (int j = 0; j < 4; ++j) {
      const float k0 = *(const float*)((const char*)kv + (ti[j] & 0xffffu)), k1 = *(const float*)((const char*)kv + (ti[j] >> 16));
      const u32 h = pack2(k0, k1);
      hi[j] = h; lo[j] = pack2(k0 - lo16(h), k1 - hi16(h));
    }
    *(u32x4*)(kx + w) = hi;
    *(u32x4*)(kx + KX_NW + w) = lo;
  }
  if (t < 8) {
    const u32 tt = kIdxTab.v[1024 + t];
    const float k0 = *(const float*)((const char*)kv + (tt & 0xffffu)), k1 = *(const float*)((const char*)kv + (tt >> 16));
    const u32 h = pack2(k0, k1);
    kx[1024 + t] = h; kx[KX_NW + 1024 + t] = pack2(k0 - lo16(h), k1 - hi16(h));
  }
}

typedef __attribute__((address_space(3))) s16x4 lds_s16x4;
template <int N> DI void wait_vm() { asm volatile("s_waitcnt vmcnt(%0)" ::"n"(N) : "memory"); }
DI void raw_barrier() { asm volatile("s_waitcnt lgkmcnt(0)" ::: "memory"); __builtin_amdgcn_s_barrier(); }
#define SCHED0() __builtin_amdgcn_sched_barrier(0)
DI void glds16(const void* sbase, u32 voff, u32 lds_addr) {
  u32 keep;
  asm volatile("s_mov_b32 %0, m0\n\ts_mov_b32 m0, %3\n\ts_nop 0\n\tglobal_load_lds_dwordx4 %1, %2\n\ts_mov_b32 m0, %0"
               : "=&s"(keep) : "v"(voff), "s"(sbase), "s"(lds_addr) : "memory");
}
DI u32 lds_addr_of(const char* p) { return (u32)(size_t)(const __attribute__((address_space(3))) char*)p; }

constexpr int LN_ROWS = 16;
constexpr int LN_BLOCKS = L_ / LN_ROWS;
constexpr int LUT_BLOCKS = 32;
constexpr int KG_BLOCKS = D_;
constexpr int PK_BLOCKS = 576;
constexpr int PREP_BLOCKS = KG_BLOCKS + LN_BLOCKS + LUT_BLOCKS + PK_BLOCKS;
constexpr int LN_PITCH = D_ + 8;

__global__ __launch_bounds__(256) void prep_kernel(const u16* __restrict__ x, S4Params P, const u16* __restrict__ w1,
                                                   const u16* __restrict__ w2, char* __restrict__ ws) {
  __shared__ __attribute__((aligned(1024))) char smem[LN_ROWS * LN_PITCH * 2];
  const int tid = threadIdx.x;
  if (blockIdx.x >= KG_BLOCKS + LN_BLOCKS + LUT_BLOCKS) {
    const int tb = blockIdx.x - (KG_BLOCKS + LN_BLOCKS + LUT_BLOCKS);
    const int lane = tid & 63, wid = __builtin_amdgcn_readfirstlane(tid >> 6);
    const bool is2 = tb >= 288;
    const int tt = is2 ? tb - 288 : tb;
    const int nt = is2 ? tt / 72 : tt / 9, kt = is2 ? tt % 72 : tt % 9;
    const int ldb = is2 ? D_ : F_;
    const char* src = (const char*)((is2 ? w2 : w1) + (size_t)kt * 64 * ldb + nt * 144);
    const u32 l0 = __builtin_amdgcn_readfirstlane(lds_addr_of(smem));
#pragma unroll
    for (int i = 0; i < 5; ++i) {
      const int ins = wid + 4 * i;
      if (ins < 18) {
        const int Lc = ins * 64 + lane, k = Lc / 18, c8 = Lc % 18;
        glds16(src, (u32)(k * ldb + c8 * 8) * 2u, l0 + ins * 1024);
      }
    }
    wait_vm<0>();
    raw_barrier();
    const int i16 = lane & 15, kq = lane >> 4;
    const int rd = (4 * kq + (i16 >> 2)) * 288 + (i16 & 3) * 8;
    char* dst = ws + (is2 ? WS_W2P : WS_W1P) + (size_t)tt * 18432 + lane * 16;
#pragma unroll
    for (int i = 0; i < 5; ++i) {
      const int fr = wid + 4 * i;
      if (fr < 18) {
        const int ks = fr / 9, nb = fr % 9;
        s16x4 lo = __builtin_amdgcn_ds_read_tr16_b64_v4i16((lds_s16x4*)(smem + rd + ks * 32 * 288 + nb * 32));
        s16x4 hi = __builtin_amdgcn_ds_read_tr16_b64_v4i16((lds_s16x4*)(smem + rd + ks * 32 * 288 + 16 * 288 + nb * 32));
        const s16x8 v = s16x8{lo[0], lo[1], lo[2], lo[3], hi[0], hi[1], hi[2], hi[3]};
        st16_wt(dst + fr * 1024, __builtin_bit_cast(u32x4, v));
      }
    }
    return;
  }
  if (blockIdx.x >= LN_BLOCKS && blockIdx.x < LN_BLOCKS + KG_BLOCKS) {
    const int d = blockIdx.x - LN_BLOCKS, lane = tid & 63, wid = tid >> 6;
    const int i16 = lane & 15, kq = lane >> 4;
    float* red = (float*)smem;
    float* kv = red + 4 * 768;
    u32x4 v[6];
    kg_load_params(P, d, wid * 32 + kq * 8, v);
    const float dt = rbf(expf(bf2f(P.log_dt[d])));
    f32x4 acc[3] = {f32x4{0, 0, 0, 0}, f32x4{0, 0, 0, 0}, f32x4{0, 0, 0, 0}};
    float posA[3];
#pragma unroll
    for (int t = 0; t < 3; ++t) posA[t] = (float)kg_row_pos(16 * t + i16);
    const float posB = (float)(8 * i16);
    KgFrag f;
#pragma unroll
    for (int g = 0; g < 8; ++g) { kg_mode(v, g, dt, posA, posB, f); if ((g & 3) == 3) kg_mma(f, acc); }
    mfma_drain();
#pragma unroll
    for (int t = 0; t < 3; ++t)
#pragma unroll
      for (int j = 0; j < 4; ++j) red[wid * 768 + (16 * t + 4 * kq + j) * 16 + i16] = acc[t][j];
    __syncthreads();
#pragma unroll
    for (int i = 0; i < 3; ++i) { int e = tid + 256 * i; kv[e] = (red[e] + red[768 + e]) + (red[2 * 768 + e] + red[3 * 768 + e]); }
    if (tid == 0) kv[768] = 0.f;
    __syncthreads();
    kg_gather_store<256>(kv, d, tid, ws);
    return;
  }
  const int bid = blockIdx.x < LN_BLOCKS ? blockIdx.x : blockIdx.x - KG_BLOCKS;
  if (bid < LN_BLOCKS) {
    u16* tile = (u16*)smem;
    const int t0 = bid * LN_ROWS;
    const int r = tid >> 4, j16 = tid & 15;
    u32x2 v[9];
#pragma unroll
    for (int i = 0; i < 9; ++i) v[i] = *(const u32x2*)(x + (size_t)(t0 + r) * D_ + (j16 + 16 * i) * 4);
    float s = 0.f;
#pragma unroll
    for (int i = 0; i < 9; ++i)
#pragma unroll
      for (int e = 0; e < 2; ++e) s += lo16(v[i][e]) + hi16(v[i][e]);
    s += __shfl_xor(s, 1); s += __shfl_xor(s, 2); s += __shfl_xor(s, 4); s += __shfl_xor(s, 8);
    const float mean = s * (1.0f / 576.0f);
    float q = 0.f;
#pragma unroll
    for (int i = 0; i < 9; ++i)
#pragma unroll
      for (int e = 0; e < 2; ++e) { float a = lo16(v[i][e]) - mean, b = hi16(v[i][e]) - mean; q += a * a + b * b; }
    q += __shfl_xor(q, 1); q += __shfl_xor(q, 2); q += __shfl_xor(q, 4); q += __shfl_xor(q, 8);
    const float var = q * (1.0f / 576.0f);
    const float mb = rbf(mean), vb = rbf(var);
    const float den = rbf(sqrtf(rbf(vb + 1e-5f)));
    const float rden = 1.0f / den;
    auto qdiv = [&](float a) { float qq = a * rden; return __builtin_fmaf(__builtin_fmaf(-qq, den, a), rden, qq); };
#pragma unroll
    for (int i = 0; i < 9; ++i) {
      u32x2 o;
#pragma unroll
      for (int e = 0; e < 2; ++e) {
        const u32 c = pack2(lo16(v[i][e]) - mb, hi16(v[i][e]) - mb);
        o[e] = pack2(qdiv(lo16(c)), qdiv(hi16(c)));
      }
      *(u32x2*)(tile + r * LN_PITCH + (j16 + 16 * i) * 4) = o;
    }
    __syncthreads();
    char* xn_g = ws + WS_XN + (size_t)bid * 18 * 1024;
#pragma unroll
    for (int i = 0; i < 5; ++i) {
      const int c = tid + 256 * i;
      if (c < 1152) {
        const int P = c >> 6, w = c & 63;
        const int i16 = w >> 2, kq = (w & 3) ^ blk_swz(w >> 2);
        const u16* src = tile + i16 * LN_PITCH + 32 * P + 4 * kq;
        u32x2 a = *(const u32x2*)src, b = *(const u32x2*)(src + 16);
        *(u32x4*)(xn_g + (size_t)c * 16) = u32x4{a[0], a[1], b[0], b[1]};
      }
    }
    u16* xt_g = (u16*)(ws + WS_XNT);
#pragma unroll
    for (int i = 0; i < 5; ++i) {
      const int c = tid + 256 * i;
      if (c < 1152) {
        const int d = c >> 1, tq = c & 1;
        u32x4 o;
#pragma unroll
        for (int j = 0; j < 4; ++j) {
          u32 lo = tile[(tq * 8 + 2 * j) * LN_PITCH + d], hi = tile[(tq * 8 + 2 * j + 1) * LN_PITCH + d];
          o[j] = lo | (hi << 16);
        }
        *(u32x4*)(xt_g + (size_t)d * XT_PITCH + 128 + t0 + tq * 8) = o;
      }
    }
    if (tid < 72) {
      int c = bid * 72 + tid;
      *(u32x4*)(xt_g + (size_t)(c >> 4) * XT_PITCH + (c & 15) * 8) = u32x4{0u, 0u, 0u, 0u};
    }
  } else {
    const int e8 = (bid - LN_BLOCKS) * 256 + tid;
    const int sign = e8 >> 12, e = e8 & 4095;
    float* lut = (float*)(ws + WS_LUT);
    int a = LUT_LO + e;
    if (a > LUT_HI) a = LUT_HI;
    float h = bf2f((u16)(a | (sign << 15)));
    float ex = rbf(expf(-fabsf(h)));
    float lp = rbf(log1pf(ex));
    float sp = rbf(lp + (h > 0.f ? h : 0.f));
    float th = tanhf(sp);
    lut[e8] = rbf(th);
  }
}

constexpr int KG_LDS = 3200;
constexpr int CV_ST = 14848;
constexpr int CV_LDS = CV_ST + 8192;

DI void conv_stage_part(char* st, int d, int lane, int first, int step, const char* __restrict__ ws) {
  const char* ug = ws + WS_XNT + (size_t)d * XT_PITCH * 2;
  const char* kg = ws + WS_KX + (size_t)d * 2 * KX_NW * 4;
  const u32 l0 = __builtin_amdgcn_readfirstlane(lds_addr_of(st));
#pragma unroll
  for (int i = 0; i < 14; ++i) {
    if (i % step == first) {
      if (i < 5) glds16(ug + i * 1024, (u32)lane * 16u, l0 + i * 1024);
      else if (i < 13) glds16(kg + (i - 5) * 1024, (u32)lane * 16u, l0 + 5120 + (i - 5) * 1024);
      else glds16(kg + 8192, (u32)(lane & 3) * 16u, l0 + 5120 + 8192);
    }
  }
}
DI s16x8 conv_bfrag(const u32* kp, u32 sh) {
  const u32 e0 = kp[0], e1 = kp[1], e2 = kp[2], e3 = kp[3], e4 = kp[4];
  const u32x4 v = {__builtin_amdgcn_alignbyte(e1, e0, sh), __builtin_amdgcn_alignbyte(e2, e1, sh),
                   __builtin_amdgcn_alignbyte(e3, e2, sh), __builtin_amdgcn_alignbyte(e4, e3, sh)};
  return __builtin_bit_cast(s16x8, v);
}
DI void conv_hlsum(const f32x4& a, int hl, float& o0, float& o1) {
  const float s0 = hl ? a[0] : a[2], s1 = hl ? a[1] : a[3];
  const float k0 = hl ? a[2] : a[0], k1 = hl ? a[3] : a[1];
  o0 = k0 + __builtin_bit_cast(float, __builtin_amdgcn_update_dpp(0, __builtin_bit_cast(int, s0), 0x128, 0xf, 0xf, false));
  o1 = k1 + __builtin_bit_cast(float, __builtin_amdgcn_update_dpp(0, __builtin_bit_cast(int, s1), 0x128, 0xf, 0xf, false));
}

DI void s4_side_gemm1(char* smem, int bid, int j, int lane, char* __restrict__ ws) {
  const int i16 = lane & 15, kq = lane >> 4;
#pragma unroll
  for (int i = 0; i < 10; ++i) __builtin_amdgcn_s_barrier();
  const int dx = 512 + (bid >> 2), g = bid & 3;
  char* st = smem + (j < 3 ? j * CV_ST : 102400);
  conv_stage_part(st, dx, lane, 0, 1, ws);
  wait_vm<0>();
  {
    const int rho = i16 & 7, hl = i16 >> 3;
    const char* us = st + 256;
    const u32 sh = ((1 - rho) & 1) * 2;
    const u32* ks = (const u32*)(st + 5120) + hl * KX_NW + ((2047 - 8 * (4 * g + kq) - rho) >> 1);
    f32x4 ya[4] = {f32x4{0, 0, 0, 0}, f32x4{0, 0, 0, 0}, f32x4{0, 0, 0, 0}, f32x4{0, 0, 0, 0}};
    const char* fb = us + (16 * j - 4 * g + i16 - kq) * 16;
#pragma unroll
    for (int q = 0; q < 16; ++q) {
      const s16x8 B = conv_bfrag(ks - 64 * q, sh);
#pragma unroll
      for (int r = 0; r < 4; ++r) {
        if (4 * r + 3 >= q) {
          if (j + 4 * r >= q) {
            const s16x8 F = *(const s16x8*)(fb + (4 * r - q) * 256);
            mfma_ip(ya[r], F, B);
          }
        }
      }
    }
    mfma_drain();
    float* yp = (float*)(ws + WS_YP) + ((size_t)(dx - 512) * 4 + g) * L_;
#pragma unroll
    for (int r = 0; r < 4; ++r) {
      float o0, o1;
      conv_hlsum(ya[r], hl, o0, o1);
      float* q = yp + 128 * (j + 4 * r) + 32 * kq + 16 * hl + rho;
      q[0] = o0; q[8] = o1;
    }
  }
}

DI void s4_side_gemm2(char* scratch, int bid, int j, int lane, const u16* __restrict__ Dskip, char* __restrict__ ws) {
  const int d = 2 * bid + (j >> 1), gh = j & 1;
  char* st = scratch + (j >> 1) * CV_LDS;
  conv_stage_part(st, d, lane, gh, 2, ws);
  const float dsk = bf2f(Dskip[d]);
  __builtin_amdgcn_s_barrier(); __builtin_amdgcn_s_barrier(); __builtin_amdgcn_s_barrier(); __builtin_amdgcn_s_barrier();
  wait_vm<0>();
  __builtin_amdgcn_s_barrier();
  const int i16 = lane & 15, kq = lane >> 4;
  const int rho = i16 & 7, hl = i16 >> 3;
  const char* us = st + 256;
  const u32 sh = ((1 - rho) & 1) * 2;
  f32x4 acc[16];
#pragma unroll
  for (int R = 0; R < 16; ++R) acc[R] = f32x4{0, 0, 0, 0};
#pragma unroll
  for (int gi = 0; gi < 2; ++gi) {
    const int g = 2 * gh + gi;
    s16x8 F[16];
#pragma unroll
    for (int dl = 0; dl < 16; ++dl) F[dl] = *(const s16x8*)(us + (16 * dl - 4 * g + i16 - kq) * 16);
    const u32* ks = (const u32*)(st + 5120) + hl * KX_NW + ((2047 - 8 * (4 * g + kq) - rho) >> 1);
#pragma unroll
    for (int q = 0; q < 16; ++q) {
      const s16x8 B = conv_bfrag(ks - 64 * q, sh);
#pragma unroll
      for (int R = q; R < 16; ++R) mfma_ip(acc[R], F[R - q], B);
      if (q == 0 || q == 2 || q == 3 || q == 5 || q == 7 || q == 10 || (q == 15 && gi == 0)) { SCHED0(); __builtin_amdgcn_s_barrier(); }
    }
  }
  mfma_drain();
  float* ybuf = (float*)(st + CV_ST);
  float o[16][2];
#pragma unroll
  for (int R = 0; R < 16; ++R) conv_hlsum(acc[R], hl, o[R][0], o[R][1]);
  float* yl = ybuf + 32 * kq + 16 * hl + rho;
  if (gh) {
#pragma unroll
    for (int R = 0; R < 8; ++R) { yl[128 * R] = o[R][0]; yl[128 * R + 8] = o[R][1]; }
  } else {
#pragma unroll
    for (int R = 8; R < 16; ++R) { yl[128 * R] = o[R][0]; yl[128 * R + 8] = o[R][1]; }
  }
  raw_barrier();
  __builtin_amdgcn_s_barrier();
  if (gh) {
#pragma unroll
    for (int R = 8; R < 16; ++R) { yl[128 * R] += o[R][0]; yl[128 * R + 8] += o[R][1]; }
  } else {
#pragma unroll
    for (int R = 0; R < 8; ++R) { yl[128 * R] = o[R][0] + yl[128 * R]; yl[128 * R + 8] = o[R][1] + yl[128 * R + 8]; }
  }
  asm volatile("s_waitcnt lgkmcnt(0)" ::: "memory");
#pragma unroll
  for (int i = 0; i < 2; ++i) {
    const int t = gh * 1024 + (i * 64 + lane) * 8;
    const s16x8 uv = *(const s16x8*)(us + t * 2);
    const f32x4 s0 = *(const f32x4*)(ybuf + t), s1 = *(const f32x4*)(ybuf + t + 4);
    float sv[8] = {s0[0], s0[1], s0[2], s0[3], s1[0], s1[1], s1[2], s1[3]};
    u32x4 ov;
#pragma unroll
    for (int jj = 0; jj < 4; ++jj) {
      float r0 = rbf(sv[2 * jj]) + rbf(bf2f((u16)uv[2 * jj]) * dsk);
      float r1 = rbf(sv[2 * jj + 1]) + rbf(bf2f((u16)uv[2 * jj + 1]) * dsk);
      ov[jj] = pack2(r0, r1);
    }
    *(u32x4*)(ws + WS_YAT + ((size_t)d * L_ + t) * 2) = ov;
  }
}

template <int MODE> struct GemmCfg {
  static constexpr int BM = MODE ? 128 : 256;
  static constexpr int NPA = MODE ? F_ / 32 : D_ / 32;
  static constexpr int LDB = MODE ? D_ : F_;
  static constexpr int A_BYTES = BM * 128;
  static constexpr int B_BYTES = 64 * 288;
  static constexpr int STAGE = A_BYTES + B_BYTES;
  static constexpr int A_IPW = BM / 64;
  static constexpr int S4_OFF = 3 * STAGE;
  static constexpr int SMEM = 3 * STAGE + (MODE ? 2 * CV_LDS : 0);
};
#define NPRE 2
constexpr int BIAS_LDS = 51200;
constexpr int LUT_LDS = 61440;

template <int MODE, int NB>
DI void gemm_wave_body(char* smem, const char* __restrict__ Ag, const char* __restrict__ Bg, const char* __restrict__ lutg,
                       const u16* __restrict__ biasg,
                       int wid, int lane, int wm, int wk, int pb0  , int sblk  ,
                       f32x4 (&acc)[4][5]) {
  using C = GemmCfg<MODE>;
  const int i16 = lane & 15, kq = lane >> 4;
  const bool extraB = wid < 2;
  const u32 aoffl = (u32)lane * 16u;
  const int a_rd = (wm * 8) * 1024 + i16 * 64 + ((kq ^ blk_swz(i16)) * 16);
  const int b_rd = C::A_BYTES + lane * 16 + pb0 * 1024;
  const int b_rs = C::A_BYTES + lane * 16 + sblk * 1024;

  const u32 lds0 = __builtin_amdgcn_readfirstlane(lds_addr_of(smem));
  auto stage = [&](int kt, int buf) {
    const u32 sb = lds0 + buf * C::STAGE;
#pragma unroll
    for (int i = 0; i < C::A_IPW; ++i) {
      const int b = wid * C::A_IPW + i;
      glds16(Ag + ((size_t)(b >> 1) * C::NPA + 2 * kt + (b & 1)) * 1024, aoffl, sb + b * 1024);
    }
    const char* bk = Bg + (size_t)kt * 18432;
#pragma unroll
    for (int i = 0; i < 2; ++i) glds16(bk + (wid + 8 * i) * 1024, aoffl, sb + C::A_BYTES + (wid + 8 * i) * 1024);
    if (extraB) glds16(bk + (wid + 16) * 1024, aoffl, sb + C::A_BYTES + (wid + 16) * 1024);
  };
  auto readA = [&](s16x8 (&a)[4], int buf, int ks) {
    const char* sb = smem + buf * C::STAGE;
#pragma unroll
    for (int mi = 0; mi < 4; ++mi) a[mi] = *(const s16x8*)(sb + a_rd + (mi * 2 + ks) * 1024);
  };
  auto readB = [&](int nb, int buf, int ks) -> s16x8 {
    const char* sb = smem + buf * C::STAGE;
    return *(const s16x8*)(sb + (nb < 4 ? b_rd + nb * 1024 : b_rs) + ks * 9 * 1024);
  };
  s16x8 a0[4], a1[4], b[NB];
  auto mm = [&](s16x8 (&ac)[4], int nb) {
#pragma unroll
    for (int mi = 0; mi < 4; ++mi)
      asm volatile("v_mfma_f32_16x16x32_bf16 %0, %1, %2, %0" : "+v"(acc[mi][nb]) : "v"(b[nb]), "v"(ac[mi]));
  };
  auto sub_pre = [&](s16x8 (&ac)[4]) {
#pragma unroll
    for (int nb = 0; nb < NPRE; ++nb) mm(ac, nb);
    SCHED0();
  };
  auto sub_post = [&](s16x8 (&ac)[4], s16x8 (&an)[4], bool ld, int nbuf, int nks) {
    if (ld) {
      readA(an, nbuf, nks);
#pragma unroll
      for (int nb = 0; nb < NPRE; ++nb) b[nb] = readB(nb, nbuf, nks);
    }
    SCHED0();
#pragma unroll
    for (int nb = NPRE; nb < NB; ++nb) {
      mm(ac, nb);
      if (ld) b[nb] = readB(nb, nbuf, nks);
      SCHED0();
    }
  };
  constexpr int NW0 = C::A_IPW + 2;
#pragma unroll
  for (int mi = 0; mi < 4; ++mi)
#pragma unroll
    for (int nb = 0; nb < NB; ++nb) asm volatile("" : "+v"(acc[mi][nb]));
  if (MODE == 0) {
    stage(0, 1); stage(1, 2);
    if (extraB) wait_vm<NW0 + 1>(); else wait_vm<NW0>();
    raw_barrier();
    readA(a0, 1, 0);
#pragma unroll
    for (int nb = 0; nb < NB; ++nb) b[nb] = readB(nb, 1, 0);
#pragma unroll
    for (int kt = 0; kt < 9; ++kt) {
      if (kt + 2 < 9) stage(kt + 2, (kt + 3) % 3);
      if (kt == 7) {
#pragma unroll
        for (int i = 0; i < 4; ++i)
          glds16(lutg + (wid * 4 + i) * 1024, aoffl, lds0 + LUT_LDS + (wid * 4 + i) * 1024);
        glds16(biasg, aoffl < 288u ? aoffl : 272u, lds0 + BIAS_LDS + wid * 1024);
      }
      SCHED0();
      sub_pre(a0); sub_post(a0, a1, true, (kt + 1) % 3, 1);
      sub_pre(a1);
      if (kt + 2 < 9) { if (extraB) wait_vm<NW0 + 1>(); else wait_vm<NW0>(); }
      else if (kt + 1 < 9) wait_vm<5>();
      else wait_vm<0>();
      raw_barrier();
      sub_post(a1, a0, kt + 1 < 9, (kt + 2) % 3, 0);
    }
  } else {
    stage(0, 0); stage(1, 1);
    if (extraB) wait_vm<NW0 + 1>(); else wait_vm<NW0>();
    raw_barrier();
    readA(a0, 0, wk);
#pragma unroll
    for (int nb = 0; nb < NB; ++nb) b[nb] = readB(nb, 0, wk);
#pragma unroll
    for (int j = 0; j < 18; ++j) {
      if (j + 2 < 18) stage(j + 2, (j + 2) % 3);
      SCHED0();
      if (j & 1) sub_pre(a1); else sub_pre(a0);
      if (j + 1 < 18) {
        if (j + 2 < 18) { if (extraB) wait_vm<NW0 + 1>(); else wait_vm<NW0>(); }
        else wait_vm<0>();
        raw_barrier();
      }
      SCHED0();
      if (j & 1) sub_post(a1, a0, j + 1 < 18, (j + 1) % 3, wk);
      else       sub_post(a0, a1, j + 1 < 18, (j + 1) % 3, wk);
    }
    raw_barrier();
  }
  asm volatile("s_nop 15\n\ts_nop 15" ::: "memory");
#pragma unroll
  for (int mi = 0; mi < 4; ++mi)
#pragma unroll
    for (int nb = 0; nb < NB; ++nb) asm volatile("" : "+v"(acc[mi][nb]));
}

__global__ __launch_bounds__(768) void gemm1_kernel(const u16* __restrict__ w1, const u16* __restrict__ bias, char* __restrict__ ws) {
  using C = GemmCfg<0>;
  __shared__ __attribute__((aligned(1024))) char smem[C::SMEM];
  const int tid = threadIdx.x, lane = tid & 63, wid = __builtin_amdgcn_readfirstlane(tid >> 6);
  const int bid = blockIdx.x;
  if (wid >= 8) { s4_side_gemm1(smem, bid, wid - 8, lane, ws); return; }
  __builtin_amdgcn_s_setprio(1);
  const int xcd = bid & 7, idx = bid >> 3;
  const int nt = 4 * xcd + (idx & 3), m0 = (idx >> 2) * 256;
  const int n0 = nt * 144;
  const char* Ag = ws + WS_XN + (size_t)(m0 >> 4) * C::NPA * 1024;
  const char* Bg = ws + WS_W1P + (size_t)nt * 9 * 18432;

  const int wn = wid >> 2;
  const int wm = wid & 3;
  const int odd = nt & 1;
  const int pb0 = odd + 4 * wn;
  const int sblk = odd ? 0 : 8;
  f32x4 acc[4][5];
#pragma unroll
  for (int mi = 0; mi < 4; ++mi)
#pragma unroll
    for (int nb = 0; nb < 5; ++nb) acc[mi][nb] = f32x4{0, 0, 0, 0};
  const int i16 = lane & 15, kq = lane >> 4;
  const char* lutg = ws + WS_LUT;
  STAMP_DECL STAMP_RT(); STAMP_NOW();
  if (wn == 0) gemm_wave_body<0, 5>(smem, Ag, Bg, lutg, bias + n0, wid, lane, wm, 0, pb0, sblk, acc);
  else         gemm_wave_body<0, 4>(smem, Ag, Bg, lutg, bias + n0, wid, lane, wm, 0, pb0, sblk, acc);
  STAMP_NOW();
  f32x2 bf_[5][2];
#pragma unroll
  for (int nb = 0; nb < 5; ++nb) {
    const int blk = nb < 4 ? pb0 + nb : sblk;
    const u32x2 bb = *(const u32x2*)(smem + BIAS_LDS + wid * 1024 + (blk * 16 + 4 * kq) * 2);
#pragma unroll
    for (int e = 0; e < 2; ++e) { bf_[nb][e] = f32x2{lo16(bb[e]), hi16(bb[e])}; asm volatile("" : "+v"(bf_[nb][e])); }
  }
  const char* lut = smem + (LUT_LDS - LUT_LO * 4);
  char* act = ws + WS_ACT;
  const int inblk = i16 * 64 + ((kq ^ blk_swz(i16)) * 16);
#pragma unroll
  for (int mi = 0; mi < 4; ++mi) {
    float hf[5][4], th[5][4];
#pragma unroll
    for (int nb = 0; nb < 5; ++nb) {
      if (nb < 4 || wn == 0) {
#pragma unroll
        for (int e = 0; e < 2; ++e) {
          u32 a = pack2(acc[mi][nb][2 * e], acc[mi][nb][2 * e + 1]);
          u32 h = pack2(lo16(a) + bf_[nb][e][0], hi16(a) + bf_[nb][e][1]);
          hf[nb][2 * e] = lo16(h); hf[nb][2 * e + 1] = hi16(h);
        }
#pragma unroll
        for (int j = 0; j < 4; ++j) {
          const u32 u = __float_as_uint(hf[nb][j]);
          const float m = __builtin_amdgcn_fmed3f(__builtin_fabsf(hf[nb][j]), 0.0009765625f, 128.0f);
          u32 o;
          asm("v_lshl_add_u32 %0, %1, 14, %2" : "=v"(o) : "v"(u >> 31), "v"(__float_as_uint(m) >> 14));
          th[nb][j] = *(const float*)(lut + o);
        }
      }
    }
    const size_t mbg = (size_t)((m0 >> 4) + wm * 4 + mi);
    u32 r[5][2];
#pragma unroll
    for (int nb = 0; nb < 5; ++nb)
      if (nb < 4 || wn == 0) {
#pragma unroll
        for (int e = 0; e < 2; ++e) r[nb][e] = pack2(hf[nb][2 * e] * th[nb][2 * e], hf[nb][2 * e + 1] * th[nb][2 * e + 1]);
      }
#pragma unroll
    for (int pr = 0; pr < 2; ++pr) {
      const int Pb = (n0 + (pb0 + 2 * pr) * 16) >> 5;
      st16_wt(act + (mbg * 144 + Pb) * 1024 + inblk, u32x4{r[2 * pr][0], r[2 * pr][1], r[2 * pr + 1][0], r[2 * pr + 1][1]});
    }
    if (wn == 0) {
      const int nb16 = (n0 >> 4) + sblk;
      st8_wt(act + (mbg * 144 + (nb16 >> 1)) * 1024 + inblk + (nb16 & 1) * 8, u32x2{r[4][0], r[4][1]});
    }
  }
  STAMP_NOW(); STAMP_VM(); STAMP_NOW(); STAMP_RT();
  STAMP_OUT(2, ws);
}

__global__ __launch_bounds__(768) void gemm2_kernel(const u16* __restrict__ w2, const u16* __restrict__ Dskip, char* __restrict__ ws) {
  using C = GemmCfg<1>;
  __shared__ __attribute__((aligned(1024))) char smem[C::SMEM];
  const int tid = threadIdx.x, lane = tid & 63, wid = __builtin_amdgcn_readfirstlane(tid >> 6);
  const int bid = blockIdx.x;
  if (wid >= 8) {
    STAMP_DECL STAMP_RT(); STAMP_NOW();
    __builtin_amdgcn_s_setprio(2);
    s4_side_gemm2(smem + C::S4_OFF, bid, wid - 8, lane, Dskip, ws);
    STAMP_NOW(); STAMP_NOW(); STAMP_NOW(); STAMP_RT();
    STAMP_OUT(3, ws);
    return;
  }
  __builtin_amdgcn_s_setprio(1);
  const int xcd = bid & 7, idx = bid >> 3;
  const int ksplit = xcd >> 1, mt = (xcd & 1) * 8 + (idx >> 2), nt = idx & 3;
  const int m0 = mt * 128, n0 = nt * 144;
  const char* Ag = ws + WS_ACT + ((size_t)(m0 >> 4) * C::NPA + ksplit * 36) * 1024;
  const char* Bg = ws + WS_W2P + ((size_t)nt * 72 + ksplit * 18) * 18432;

  const int wn = wid >> 2;
  const int wmv = wid & 3;
  const int wk = wmv >> 1, wm = wmv & 1;
  const int odd = nt & 1;
  const int pb0 = odd + 4 * wn;
  const int sblk = odd ? 0 : 8;
  f32x4 acc[4][5];
#pragma unroll
  for (int mi = 0; mi < 4; ++mi)
#pragma unroll
    for (int nb = 0; nb < 5; ++nb) acc[mi][nb] = f32x4{0, 0, 0, 0};
  STAMP_DECL STAMP_RT(); STAMP_NOW();
  if (wn == 0) gemm_wave_body<1, 5>(smem, Ag, Bg, nullptr, nullptr, wid, lane, wm, wk, pb0, sblk, acc);
  else         gemm_wave_body<1, 4>(smem, Ag, Bg, nullptr, nullptr, wid, lane, wm, wk, pb0, sblk, acc);
  STAMP_NOW();

  const int i16 = lane & 15, kq = lane >> 4;
  f32x4* red = (f32x4*)smem;
  const int slot = wn * 2 + wm;
  if (wk == 1) {
#pragma unroll
    for (int mi = 0; mi < 4; ++mi)
#pragma unroll
      for (int nb = 0; nb < 5; ++nb)
        if (nb < 4 || wn == 0) red[(slot * 20 + mi * 5 + nb) * 64 + lane] = acc[mi][nb];
  }
  raw_barrier();
  char* partb = ws + WS_PART;
  constexpr size_t PSTRIDE = (size_t)L_ * D_ * 4;
  if (wk == 0) {
#pragma unroll
    for (int mi = 0; mi < 4; ++mi)
#pragma unroll
      for (int nb = 0; nb < 5; ++nb)
        if (nb < 4 || wn == 0) {
          acc[mi][nb] += red[(slot * 20 + mi * 5 + nb) * 64 + lane];
          const size_t mbg = (size_t)((m0 >> 4) + wm * 4 + mi);
          const int nbg = (n0 >> 4) + (nb < 4 ? pb0 + nb : sblk);
          st16_wt(partb + ksplit * PSTRIDE + (mbg * 36 + nbg) * 1024 + i16 * 64 + kq * 16, __builtin_bit_cast(u32x4, acc[mi][nb]));
        }
  }
  STAMP_NOW(); STAMP_VM(); STAMP_NOW(); STAMP_RT();
  STAMP_OUT(3, ws);
}

__global__ __launch_bounds__(256) void final_kernel(const u16* __restrict__ x, const u16* __restrict__ Dskip,
                                                    const char* __restrict__ ws, u16* __restrict__ out) {
  __shared__ __attribute__((aligned(16))) u16 yt[64 * 72];
  const int tid = threadIdx.x;
  const int tt = blockIdx.x / 9, dt = blockIdx.x % 9;
  const int t0 = tt * 64, d0 = dt * 64;
  const u16* yat = (const u16*)(ws + WS_YAT);
  const char* part = ws + WS_PART;
  u32x4 yv[2], xv[2];
  f32x4 p[2][4][2];
#pragma unroll
  for (int i = 0; i < 2; ++i) {
    const int c = tid + 256 * i;
    const int r = c >> 3, ch = c & 7;
    if (dt < 8) yv[i] = *(const u32x4*)(yat + (size_t)(d0 + r) * L_ + t0 + ch * 8);
    else {
      const float* yp = (const float*)(ws + WS_YP) + (size_t)r * 4 * L_ + t0 + ch * 8;
      f32x4 a0 = (*(const f32x4*)(yp) + *(const f32x4*)(yp + L_)) + (*(const f32x4*)(yp + 2 * L_) + *(const f32x4*)(yp + 3 * L_));
      f32x4 a1 = (*(const f32x4*)(yp + 4) + *(const f32x4*)(yp + L_ + 4)) + (*(const f32x4*)(yp + 2 * L_ + 4) + *(const f32x4*)(yp + 3 * L_ + 4));
      const u32x4 uv = *(const u32x4*)((const u16*)(ws + WS_XNT) + (size_t)(d0 + r) * XT_PITCH + 128 + t0 + ch * 8);
      const float dsk = bf2f(Dskip[d0 + r]);
      float sv[8] = {a0[0], a0[1], a0[2], a0[3], a1[0], a1[1], a1[2], a1[3]};
#pragma unroll
      for (int jj = 0; jj < 4; ++jj)
        yv[i][jj] = pack2(rbf(sv[2 * jj]) + rbf(lo16(uv[jj]) * dsk), rbf(sv[2 * jj + 1]) + rbf(hi16(uv[jj]) * dsk));
    }
    xv[i] = *(const u32x4*)(x + (size_t)(t0 + r) * D_ + d0 + ch * 8);
    const int t = t0 + r, d = d0 + ch * 8;
    const size_t po = ((size_t)(t >> 4) * 36 + (d >> 4)) * 1024 + (t & 15) * 64 + ((d >> 2) & 3) * 16;
#pragma unroll
    for (int s = 0; s < 4; ++s) {
      p[i][s][0] = *(const f32x4*)(part + (size_t)s * L_ * D_ * 4 + po);
      p[i][s][1] = *(const f32x4*)(part + (size_t)s * L_ * D_ * 4 + po + 16);
    }
  }
#pragma unroll
  for (int i = 0; i < 2; ++i) {
    const int c = tid + 256 * i;
    *(u32x4*)(yt + (c >> 3) * 72 + (c & 7) * 8) = yv[i];
  }
  __syncthreads();
#pragma unroll
  for (int i = 0; i < 2; ++i) {
    const int c = tid + 256 * i;
    const int r = c >> 3, ch = c & 7;
    f32x4 s0 = (p[i][0][0] + p[i][1][0]) + (p[i][2][0] + p[i][3][0]);
    f32x4 s1 = (p[i][0][1] + p[i][1][1]) + (p[i][2][1] + p[i][3][1]);
    float s[8] = {s0[0], s0[1], s0[2], s0[3], s1[0], s1[1], s1[2], s1[3]};
    u32x4 ov;
#pragma unroll
    for (int j = 0; j < 4; ++j) {
      float r2[2];
#pragma unroll
      for (int h = 0; h < 2; ++h) {
        int e = 2 * j + h;
        float xx = h ? hi16(xv[i][j]) : lo16(xv[i][j]);
        float ya = bf2f(yt[(ch * 8 + e) * 72 + r]);
        r2[h] = rbf(xx + ya) + rbf(s[e]);
      }
      ov[j] = pack2(r2[0], r2[1]);
    }
    *(u32x4*)(out + (size_t)(t0 + r) * D_ + d0 + ch * 8) = ov;
  }
}

extern "C" void kernel_launch(void* const* d_in, const int* in_sizes, int n_in,
                              void* d_out, int out_size, void* d_ws, size_t ws_size,
                              hipStream_t stream) {
  const u16* x      = (const u16*)d_in[0];
  const u16* A_re   = (const u16*)d_in[1];
  const u16* A_im   = (const u16*)d_in[2];
  const u16* B_re   = (const u16*)d_in[3];
  const u16* B_im   = (const u16*)d_in[4];
  const u16* C_re   = (const u16*)d_in[5];
  const u16* C_im   = (const u16*)d_in[6];
  const u16* log_dt = (const u16*)d_in[7];
  const u16* Dskip  = (const u16*)d_in[8];
  const u16* w1     = (const u16*)d_in[10];
  const u16* b1     = (const u16*)d_in[11];
  const u16* w2     = (const u16*)d_in[12];
  char* ws = (char*)d_ws;
  S4Params P{A_re, A_im, B_re, B_im, C_re, C_im, log_dt};
  prep_kernel<<<PREP_BLOCKS, 256, 0, stream>>>(x, P, w1, w2, ws);
  gemm1_kernel<<<256, 768, 0, stream>>>(w1, b1, ws);
  gemm2_kernel<<<256, 768, 0, stream>>>(w2, Dskip, ws);
  final_kernel<<<288, 256, 0, stream>>>(x, Dskip, ws, (u16*)d_out);
}
```

```cpp
#include <hip/hip_runtime.h>
#include <hip/hip_bf16.h>
#include <stdint.h>

typedef unsigned short u16;
typedef unsigned int u32;
using s16x8 = __attribute__((ext_vector_type(8))) short;
using s16x4 = __attribute__((ext_vector_type(4))) short;
using f32x4 = __attribute__((ext_vector_type(4))) float;
using u32x4 = __attribute__((ext_vector_type(4))) unsigned int;
using u32x2 = __attribute__((ext_vector_type(2))) unsigned int;
typedef u32x4 __attribute__((aligned(4))) u32x4_a4;

#define DI __device__ __forceinline__
#define STAMP_DECL
#define STAMP_NOW()
#define STAMP_RT()
#define STAMP_VM()
#define STAMP_OUT(kid, ws)

constexpr int L_ = 2048, D_ = 576, N_ = 128, F_ = 4608;
constexpr int XT_PITCH = 2048 + 128;
constexpr int KX_NW = 1032;
constexpr int LUT_LO = 0x3A80, LUT_HI = 0x4300;
constexpr int LUT_ENTRIES = 8192;

constexpr size_t WS_CNT  = 0;
constexpr size_t WS_XN   = 1024;
constexpr size_t WS_XNT  = WS_XN + (size_t)L_ * D_ * 2;
constexpr size_t WS_KX   = WS_XNT + (size_t)D_ * XT_PITCH * 2;
constexpr size_t WS_LUT  = WS_KX + (size_t)D_ * 2 * KX_NW * 4;
constexpr size_t WS_YAT  = WS_LUT + LUT_ENTRIES * 4;
constexpr size_t WS_ACT  = WS_YAT + (size_t)D_ * L_ * 2;
constexpr size_t WS_PART = WS_ACT + (size_t)L_ * F_ * 2;
constexpr size_t WS_YP   = WS_PART + (size_t)4 * L_ * D_ * 4;
constexpr size_t WS_W1P  = WS_YP + (size_t)64 * 4 * L_ * 4;
constexpr size_t WS_W2P  = WS_W1P + (size_t)288 * 18432;

DI float bf2f(u16 b) { return __uint_as_float(((u32)b) << 16); }
DI u16 f2bf(float f) {
  __hip_bfloat16 h = __float2bfloat16(f);
  return *reinterpret_cast<u16*>(&h);
}
DI float rbf(float f) { return bf2f(f2bf(f)); }
typedef __bf16 bf16x2_t __attribute__((ext_vector_type(2)));
using f32x2 = __attribute__((ext_vector_type(2))) float;
DI u32 pack2(float lo, float hi) {
  f32x2 v = {lo, hi};
  return __builtin_bit_cast(u32, __builtin_convertvector(v, bf16x2_t));
}
DI float lo16(u32 v) { return __uint_as_float(v << 16); }
DI float hi16(u32 v) { return __uint_as_float(v & 0xffff0000u); }
DI void st16_wt(void* p, u32x4 v) { asm volatile("global_store_dwordx4 %0, %1, off sc1\n\ts_nop 1" ::"v"(p), "v"(v) : "memory"); }
DI void st8_wt(void* p, u32x2 v) { asm volatile("global_store_dwordx2 %0, %1, off sc1\n\ts_nop 1" ::"v"(p), "v"(v) : "memory"); }

DI int blk_swz(int i16) { return (-(i16 >> 2)) & 3; }

DI void mfma_ip(f32x4& acc, const s16x8& a, const s16x8& b) {
  asm volatile("s_nop 1\n\tv_mfma_f32_16x16x32_bf16 %0, %1, %2, %0" : "+v"(acc) : "v"(a), "v"(b));
}
DI void mfma_drain() { asm volatile("s_nop 15\n\ts_nop 15" ::: "memory"); }

DI void cexp_pos(float lr2, float lh, float pos, float& er, float& ei) {
  const float mag = __builtin_amdgcn_exp2f(lr2 * pos);
  const float f = __builtin_amdgcn_fractf(lh * pos);
  er = mag * __builtin_amdgcn_cosf(f);
  ei = mag * __builtin_amdgcn_sinf(f);
}
DI int kg_row_pos(int row) {
  if (row < 16) return 128 * (row >> 3) + (row & 7);
  if (row < 24) { int r = row - 16; return 256 + 128 * (r >> 2) + 2 * (r & 3); }
  if (row < 32) { int r = row - 24; return 512 + 128 * (r >> 1) + 4 * (r & 1); }
  if (row < 40) return 1024 + 128 * (row - 32);
  if (row == 40) return 2048;
  return 0;
}
struct KIdxTab { u32 v[1088]; };
constexpr int kg_bf16_round(int l) {
  int sft = l >= 1024 ? 3 : l >= 512 ? 2 : l >= 256 ? 1 : 0;
  if (sft == 0) return l;
  int half = 1 << (sft - 1), q = l >> sft;
  return ((l + half - 1 + (q & 1)) >> sft) << sft;
}
constexpr int kg_pos_index(int p) {
  int sg = p >= 2048 ? 4 : p >= 1024 ? 3 : p >= 512 ? 2 : p >= 256 ? 1 : 0;
  int base = sg ? (128 << sg) : 0, rowbase = sg ? 8 + 8 * sg : 0;
  int q = p - base;
  int row = rowbase + (q >> 7) * (8 >> sg) + ((q & 7) >> sg);
  return row * 16 + ((q & 127) >> 3);
}
constexpr KIdxTab make_kidx() {
  KIdxTab t{};
  for (int w = 0; w < 1088; ++w) {
    int l0 = 2047 - 2 * w, l1 = 2046 - 2 * w;
    u32 i0 = l0 >= 0 ? kg_pos_index(kg_bf16_round(l0)) : 768, i1 = l1 >= 0 ? kg_pos_index(kg_bf16_round(l1)) : 768;
    t.v[w] = (i0 * 4) | ((i1 * 4) << 16);
  }
  return t;
}
__device__ const KIdxTab kIdxTab = make_kidx();

struct S4Params { const u16 *A_re, *A_im, *B_re, *B_im, *C_re, *C_im, *log_dt; };
struct KgFrag { u32x4 Bh, Bl, Ah[3], Al[3]; };

DI void kg_load_params(const S4Params& P, int d, int n0, u32x4 (&v)[6]) {
  const size_t o = (size_t)d * N_ + n0;
  v[0] = *(const u32x4*)(P.A_re + o); v[1] = *(const u32x4*)(P.A_im + o);
  v[2] = *(const u32x4*)(P.B_re + o); v[3] = *(const u32x4*)(P.B_im + o);
  v[4] = *(const u32x4*)(P.C_re + o); v[5] = *(const u32x4*)(P.C_im + o);
}
DI void kg_mode(const u32x4 (&v)[6], int g, float dt, const float (&posA)[3], float posB, KgFrag& f) {
  const int e = g >> 1, gg = g & 3;
  const float lr = dt * ((g & 1) ? hi16(v[0][e]) : lo16(v[0][e])), li = dt * ((g & 1) ? hi16(v[1][e]) : lo16(v[1][e]));
  const float br = dt * ((g & 1) ? hi16(v[2][e]) : lo16(v[2][e])), bi = dt * ((g & 1) ? hi16(v[3][e]) : lo16(v[3][e]));
  const float cr = (g & 1) ? hi16(v[4][e]) : lo16(v[4][e]), ci = (g & 1) ? hi16(v[5][e]) : lo16(v[5][e]);
  const float kr = cr * br - ci * bi, ki = cr * bi + ci * br;
  const float lr2 = lr * 1.44269504088896340736f;
  const float lh = li * 0.15915494309189535f;
  float zr, zi;
  cexp_pos(lr2, lh, posB, zr, zi);
  {
    const float xr = kr * zr - ki * zi, xi = -(kr * zi + ki * zr);
    const u32 h = pack2(xr, xi);
    f.Bh[gg] = h; f.Bl[gg] = pack2(xr - lo16(h), xi - hi16(h));
  }
#pragma unroll
  for (int t = 0; t < 3; ++t) {
    float er, ei;
    cexp_pos(lr2, lh, posA[t], er, ei);
    const u32 h = pack2(er, ei);
    f.Ah[t][gg] = h; f.Al[t][gg] = pack2(er - lo16(h), ei - hi16(h));
  }
}
DI void kg_mma(const KgFrag& f, f32x4 (&acc)[3]) {
#pragma unroll
  for (int t = 0; t < 3; ++t) {
    mfma_ip(acc[t], __builtin_bit_cast(s16x8, f.Al[t]), __builtin_bit_cast(s16x8, f.Bh));
    mfma_ip(acc[t], __builtin_bit_cast(s16x8, f.Ah[t]), __builtin_bit_cast(s16x8, f.Bl));
    mfma_ip(acc[t], __builtin_bit_cast(s16x8, f.Ah[t]), __builtin_bit_cast(s16x8, f.Bh));
  }
}
template <int NTHR> DI void kg_gather_store(const float* kv, int d, int t, char* __restrict__ ws) {
  u32* kx = (u32*)(ws + WS_KX) + (size_t)d * 2 * KX_NW;
#pragma unroll
  for (int i = 0; i < 256 / NTHR; ++i) {
    const int w = (i * NTHR + t) * 4;
    const u32x4 ti = *(const u32x4*)(kIdxTab.v + w);
    u32x4 hi, lo;
#pragma unroll
    for (int j = 0; j < 4; ++j) {
      const float k0 = *(const float*)((const char*)kv + (ti[j] & 0xffffu)), k1 = *(const float*)((const char*)kv + (ti[j] >> 16));
      const u32 h = pack2(k0, k1);
      hi[j] = h; lo[j] = pack2(k0 - lo16(h), k1 - hi16(h));
    }
    *(u32x4*)(kx + w) = hi;
    *(u32x4*)(kx + KX_NW + w) = lo;
  }
  if (t < 8) {
    const u32 tt = kIdxTab.v[1024 + t];
    const float k0 = *(const float*)((const char*)kv + (tt & 0xffffu)), k1 = *(const float*)((const char*)kv + (tt >> 16));
    const u32 h = pack2(k0, k1);
    kx[1024 + t] = h; kx[KX_NW + 1024 + t] = pack2(k0 - lo16(h), k1 - hi16(h));
  }
}

typedef __attribute__((address_space(3))) s16x4 lds_s16x4;
template <int N> DI void wait_vm() { asm volatile("s_waitcnt vmcnt(%0)" ::"n"(N) : "memory"); }
DI void raw_barrier() { asm volatile("s_waitcnt lgkmcnt(0)" ::: "memory"); __builtin_amdgcn_s_barrier(); }
#define SCHED0() __builtin_amdgcn_sched_barrier(0)
DI void glds16(const void* sbase, u32 voff, u32 lds_addr) {
  u32 keep;
  asm volatile("s_mov_b32 %0, m0\n\ts_mov_b32 m0, %3\n\ts_nop 0\n\tglobal_load_lds_dwordx4 %1, %2\n\ts_mov_b32 m0, %0"
               : "=&s"(keep) : "v"(voff), "s"(sbase), "s"(lds_addr) : "memory");
}
DI u32 lds_addr_of(const char* p) { return (u32)(size_t)(const __attribute__((address_space(3))) char*)p; }

constexpr int LN_ROWS = 16;
constexpr int LN_BLOCKS = L_ / LN_ROWS;
constexpr int LUT_BLOCKS = 32;
constexpr int KG_BLOCKS = D_;
constexpr int PK_BLOCKS = 576;
constexpr int PREP_BLOCKS = KG_BLOCKS + LN_BLOCKS + LUT_BLOCKS + PK_BLOCKS;
constexpr int LN_PITCH = D_ + 8;

__global__ __launch_bounds__(256) void prep_kernel(const u16* __restrict__ x, S4Params P, const u16* __restrict__ w1,
                                                   const u16* __restrict__ w2, char* __restrict__ ws) {
  __shared__ __attribute__((aligned(1024))) char smem[LN_ROWS * LN_PITCH * 2];
  const int tid = threadIdx.x;
  if (blockIdx.x >= KG_BLOCKS + LN_BLOCKS + LUT_BLOCKS) {
    const int tb = blockIdx.x - (KG_BLOCKS + LN_BLOCKS + LUT_BLOCKS);
    const int lane = tid & 63, wid = __builtin_amdgcn_readfirstlane(tid >> 6);
    const bool is2 = tb >= 288;
    const int tt = is2 ? tb - 288 : tb;
    const int nt = is2 ? tt / 72 : tt / 9, kt = is2 ? tt % 72 : tt % 9;
    const int ldb = is2 ? D_ : F_;
    const char* src = (const char*)((is2 ? w2 : w1) + (size_t)kt * 64 * ldb + nt * 144);
    const u32 l0 = __builtin_amdgcn_readfirstlane(lds_addr_of(smem));
#pragma unroll
    for (int i = 0; i < 5; ++i) {
      const int ins = wid + 4 * i;
      if (ins < 18) {
        const int Lc = ins * 64 + lane, k = Lc / 18, c8 = Lc % 18;
        glds16(src, (u32)(k * ldb + c8 * 8) * 2u, l0 + ins * 1024);
      }
    }
    wait_vm<0>();
    raw_barrier();
    const int i16 = lane & 15, kq = lane >> 4;
    const int rd = (4 * kq + (i16 >> 2)) * 288 + (i16 & 3) * 8;
    char* dst = ws + (is2 ? WS_W2P : WS_W1P) + (size_t)tt * 18432 + lane * 16;
#pragma unroll
    for (int i = 0; i < 5; ++i) {
      const int fr = wid + 4 * i;
      if (fr < 18) {
        const int ks = fr / 9, nb = fr % 9;
        s16x4 lo = __builtin_amdgcn_ds_read_tr16_b64_v4i16((lds_s16x4*)(smem + rd + ks * 32 * 288 + nb * 32));
        s16x4 hi = __builtin_amdgcn_ds_read_tr16_b64_v4i16((lds_s16x4*)(smem + rd + ks * 32 * 288 + 16 * 288 + nb * 32));
        const s16x8 v = s16x8{lo[0], lo[1], lo[2], lo[3], hi[0], hi[1], hi[2], hi[3]};
        st16_wt(dst + fr * 1024, __builtin_bit_cast(u32x4, v));
      }
    }
    return;
  }
  if (blockIdx.x >= LN_BLOCKS && blockIdx.x < LN_BLOCKS + KG_BLOCKS) {
    const int d = blockIdx.x - LN_BLOCKS, lane = tid & 63, wid = tid >> 6;
    const int i16 = lane & 15, kq = lane >> 4;
    float* red = (float*)smem;
    float* kv = red + 4 * 768;
    u32x4 v[6];
    kg_load_params(P, d, wid * 32 + kq * 8, v);
    const float dt = rbf(expf(bf2f(P.log_dt[d])));
    f32x4 acc[3] = {f32x4{0, 0, 0, 0}, f32x4{0, 0, 0, 0}, f32x4{0, 0, 0, 0}};
    float posA[3];
#pragma unroll
    for (int t = 0; t < 3; ++t) posA[t] = (float)kg_row_pos(16 * t + i16);
    const float posB = (float)(8 * i16);
    KgFrag f;
#pragma unroll
    for (int g = 0; g < 8; ++g) { kg_mode(v, g, dt, posA, posB, f); if ((g & 3) == 3) kg_mma(f, acc); }
    mfma_drain();
#pragma unroll
    for (int t = 0; t < 3; ++t)
#pragma unroll
      for (int j = 0; j < 4; ++j) red[wid * 768 + (16 * t + 4 * kq + j) * 16 + i16] = acc[t][j];
    __syncthreads();
#pragma unroll
    for (int i = 0; i < 3; ++i) { int e = tid + 256 * i; kv[e] = (red[e] + red[768 + e]) + (red[2 * 768 + e] + red[3 * 768 + e]); }
    if (tid == 0) kv[768] = 0.f;
    __syncthreads();
    kg_gather_store<256>(kv, d, tid, ws);
    return;
  }
  const int bid = blockIdx.x < LN_BLOCKS ? blockIdx.x : blockIdx.x - KG_BLOCKS;
  if (bid < LN_BLOCKS) {
    u16* tile = (u16*)smem;
    const int t0 = bid * LN_ROWS;
    const int r = tid >> 4, j16 = tid & 15;
    u32x2 v[9];
#pragma unroll
    for (int i = 0; i < 9; ++i) v[i] = *(const u32x2*)(x + (size_t)(t0 + r) * D_ + (j16 + 16 * i) * 4);
    float s = 0.f;
#pragma unroll
    for (int i = 0; i < 9; ++i)
#pragma unroll
      for (int e = 0; e < 2; ++e) s += lo16(v[i][e]) + hi16(v[i][e]);
    s += __shfl_xor(s, 1); s += __shfl_xor(s, 2); s += __shfl_xor(s, 4); s += __shfl_xor(s, 8);
    const float mean = s * (1.0f / 576.0f);
    float q = 0.f;
#pragma unroll
    for (int i = 0; i < 9; ++i)
#pragma unroll
      for (int e = 0; e < 2; ++e) { float a = lo16(v[i][e]) - mean, b = hi16(v[i][e]) - mean; q += a * a + b * b; }
    q += __shfl_xor(q, 1); q += __shfl_xor(q, 2); q += __shfl_xor(q, 4); q += __shfl_xor(q, 8);
    const float var = q * (1.0f / 576.0f);
    const float mb = rbf(mean), vb = rbf(var);
    const float den = rbf(sqrtf(rbf(vb + 1e-5f)));
    const float rden = 1.0f / den;
    auto qdiv = [&](float a) { float qq = a * rden; return __builtin_fmaf(__builtin_fmaf(-qq, den, a), rden, qq); };
#pragma unroll
    for (int i = 0; i < 9; ++i) {
      u32x2 o;
#pragma unroll
      for (int e = 0; e < 2; ++e) {
        const u32 c = pack2(lo16(v[i][e]) - mb, hi16(v[i][e]) - mb);
        o[e] = pack2(qdiv(lo16(c)), qdiv(hi16(c)));
      }
      *(u32x2*)(tile + r * LN_PITCH + (j16 + 16 * i) * 4) = o;
    }
    __syncthreads();
    char* xn_g = ws + WS_XN + (size_t)bid * 18 * 1024;
#pragma unroll
    for (int i = 0; i < 5; ++i) {
      const int c = tid + 256 * i;
      if (c < 1152) {
        const int P = c >> 6, w = c & 63;
        const int i16 = w >> 2, kq = (w & 3) ^ blk_swz(w >> 2);
        const u16* src = tile + i16 * LN_PITCH + 32 * P + 4 * kq;
        u32x2 a = *(const u32x2*)src, b = *(const u32x2*)(src + 16);
        *(u32x4*)(xn_g + (size_t)c * 16) = u32x4{a[0], a[1], b[0], b[1]};
      }
    }
    u16* xt_g = (u16*)(ws + WS_XNT);
#pragma unroll
    for (int i = 0; i < 5; ++i) {
      const int c = tid + 256 * i;
      if (c < 1152) {
        const int d = c >> 1, tq = c & 1;
        u32x4 o;
#pragma unroll
        for (int j = 0; j < 4; ++j) {
          u32 lo = tile[(tq * 8 + 2 * j) * LN_PITCH + d], hi = tile[(tq * 8 + 2 * j + 1) * LN_PITCH + d];
          o[j] = lo | (hi << 16);
        }
        *(u32x4*)(xt_g + (size_t)d * XT_PITCH + 128 + t0 + tq * 8) = o;
      }
    }
    if (tid < 72) {
      int c = bid * 72 + tid;
      *(u32x4*)(xt_g + (size_t)(c >> 4) * XT_PITCH + (c & 15) * 8) = u32x4{0u, 0u, 0u, 0u};
    }
  } else {
    const int e8 = (bid - LN_BLOCKS) * 256 + tid;
    const int sign = e8 >> 12, e = e8 & 4095;
    float* lut = (float*)(ws + WS_LUT);
    int a = LUT_LO + e;
    if (a > LUT_HI) a = LUT_HI;
    float h = bf2f((u16)(a | (sign << 15)));
    float ex = rbf(expf(-fabsf(h)));
    float lp = rbf(log1pf(ex));
    float sp = rbf(lp + (h > 0.f ? h : 0.f));
    float th = tanhf(sp);
    lut[e8] = rbf(th);
  }
}

constexpr int BIAS_LDS = 51200;
constexpr int LUT_LDS = 61440;
constexpr int KG_LDS = 3200;
constexpr int CV_ST = 14848;
constexpr int CV_LDS = CV_ST + 8192;

DI void conv_stage_part(char* st, int d, int lane, int first, int step, const char* __restrict__ ws) {
  const char* ug = ws + WS_XNT + (size_t)d * XT_PITCH * 2;
  const char* kg = ws + WS_KX + (size_t)d * 2 * KX_NW * 4;
  const u32 l0 = __builtin_amdgcn_readfirstlane(lds_addr_of(st));
#pragma unroll
  for (int i = 0; i < 14; ++i) {
    if (i % step == first) {
      if (i < 5) glds16(ug + i * 1024, (u32)lane * 16u, l0 + i * 1024);
      else if (i < 13) glds16(kg + (i - 5) * 1024, (u32)lane * 16u, l0 + 5120 + (i - 5) * 1024);
      else glds16(kg + 8192, (u32)(lane & 3) * 16u, l0 + 5120 + 8192);
    }
  }
}
DI s16x8 conv_bfrag(const u32* kp, u32 sh) {
  const u32 e0 = kp[0], e1 = kp[1], e2 = kp[2], e3 = kp[3], e4 = kp[4];
  const u32x4 v = {__builtin_amdgcn_alignbyte(e1, e0, sh), __builtin_amdgcn_alignbyte(e2, e1, sh),
                   __builtin_amdgcn_alignbyte(e3, e2, sh), __builtin_amdgcn_alignbyte(e4, e3, sh)};
  return __builtin_bit_cast(s16x8, v);
}
DI void conv_hlsum(const f32x4& a, int hl, float& o0, float& o1) {
  const float s0 = hl ? a[0] : a[2], s1 = hl ? a[1] : a[3];
  const float k0 = hl ? a[2] : a[0], k1 = hl ? a[3] : a[1];
  o0 = k0 + __builtin_bit_cast(float, __builtin_amdgcn_update_dpp(0, __builtin_bit_cast(int, s0), 0x128, 0xf, 0xf, false));
  o1 = k1 + __builtin_bit_cast(float, __builtin_amdgcn_update_dpp(0, __builtin_bit_cast(int, s1), 0x128, 0xf, 0xf, false));
}

DI void s4_side_gemm1(char* smem, int bid, int j, int lane, const char* __restrict__ Ag, const char* __restrict__ Bg,
                      const u16* __restrict__ biasg, char* __restrict__ ws) {
  const int i16 = lane & 15, kq = lane >> 4;
  {
    constexpr int STAGE = 51200;
    const u32 lds0 = __builtin_amdgcn_readfirstlane(lds_addr_of(smem));
    const u32 lo = (u32)lane * 16u;
    auto stage = [&](int kt, int buf) {
      const u32 sb = lds0 + buf * STAGE;
#pragma unroll
      for (int i = 0; i < 8; ++i) {
        const int bb = j + 4 * i;
        glds16(Ag + ((size_t)(bb >> 1) * 18 + 2 * kt + (bb & 1)) * 1024, lo, sb + bb * 1024);
      }
      const char* bk = Bg + (size_t)kt * 18432;
#pragma unroll
      for (int i = 0; i < 4; ++i) glds16(bk + (j + 4 * i) * 1024, lo, sb + 32768 + (j + 4 * i) * 1024);
      if (j < 2) glds16(bk + (j + 16) * 1024, lo, sb + 32768 + (j + 16) * 1024);
    };
    stage(0, 1); stage(1, 2);
    if (j < 2) wait_vm<13>(); else wait_vm<12>();
    __builtin_amdgcn_s_barrier();
#pragma unroll
    for (int kt = 0; kt < 9; ++kt) {
      if (kt + 2 < 9) stage(kt + 2, (kt + 3) % 3);
      if (kt == 7) {
#pragma unroll
        for (int i = 0; i < 8; ++i) glds16(ws + WS_LUT + (j * 8 + i) * 1024, lo, lds0 + LUT_LDS + (j * 8 + i) * 1024);
#pragma unroll
        for (int i = 0; i < 2; ++i) glds16(biasg, lo < 288u ? lo : 272u, lds0 + BIAS_LDS + (j * 2 + i) * 1024);
      }
      if (kt + 2 < 9) { if (j < 2) wait_vm<13>(); else wait_vm<12>(); }
      else if (kt + 1 < 9) wait_vm<10>();
      else wait_vm<0>();
      __builtin_amdgcn_s_barrier();
    }
  }
  const int dx = 512 + (bid >> 2), g = bid & 3;
  char* st = smem + (j < 3 ? j * CV_ST : 102400);
  conv_stage_part(st, dx, lane, 0, 1, ws);
  wait_vm<0>();
  {
    const int rho = i16 & 7, hl = i16 >> 3;
    const char* us = st + 256;
    const u32 sh = ((1 - rho) & 1) * 2;
    const u32* ks = (const u32*)(st + 5120) + hl * KX_NW + ((2047 - 8 * (4 * g + kq) - rho) >> 1);
    f32x4 ya[4] = {f32x4{0, 0, 0, 0}, f32x4{0, 0, 0, 0}, f32x4{0, 0, 0, 0}, f32x4{0, 0, 0, 0}};
    const char* fb = us + (16 * j - 4 * g + i16 - kq) * 16;
#pragma unroll
    for (int q = 0; q < 16; ++q) {
      const s16x8 B = conv_bfrag(ks - 64 * q, sh);
#pragma unroll
      for (int r = 0; r < 4; ++r) {
        if (4 * r + 3 >= q) {
          if (j + 4 * r >= q) {
            const s16x8 F = *(const s16x8*)(fb + (4 * r - q) * 256);
            mfma_ip(ya[r], F, B);
          }
        }
      }
    }
    mfma_drain();
    float* yp = (float*)(ws + WS_YP) + ((size_t)(dx - 512) * 4 + g) * L_;
#pragma unroll
    for (int r = 0; r < 4; ++r) {
      float o0, o1;
      conv_hlsum(ya[r], hl, o0, o1);
      float* q = yp + 128 * (j + 4 * r) + 32 * kq + 16 * hl + rho;
      q[0] = o0; q[8] = o1;
    }
  }
}

DI void s4_side_gemm2(char* scratch, int bid, int j, int lane, const u16* __restrict__ Dskip, char* __restrict__ ws) {
  const int d = 2 * bid + (j >> 1), gh = j & 1;
  char* st = scratch + (j >> 1) * CV_LDS;
  conv_stage_part(st, d, lane, gh, 2, ws);
  const float dsk = bf2f(Dskip[d]);
  __builtin_amdgcn_s_barrier(); __builtin_amdgcn_s_barrier(); __builtin_amdgcn_s_barrier(); __builtin_amdgcn_s_barrier();
  wait_vm<0>();
  __builtin_amdgcn_s_barrier();
  const int i16 = lane & 15, kq = lane >> 4;
  const int rho = i16 & 7, hl = i16 >> 3;
  const char* us = st + 256;
  const u32 sh = ((1 - rho) & 1) * 2;
  f32x4 acc[16];
#pragma unroll
  for (int R = 0; R < 16; ++R) acc[R] = f32x4{0, 0, 0, 0};
#pragma unroll
  for (int gi = 0; gi < 2; ++gi) {
    const int g = 2 * gh + gi;
    s16x8 F[16];
#pragma unroll
    for (int dl = 0; dl < 16; ++dl) F[dl] = *(const s16x8*)(us + (16 * dl - 4 * g + i16 - kq) * 16);
    const u32* ks = (const u32*)(st + 5120) + hl * KX_NW + ((2047 - 8 * (4 * g + kq) - rho) >> 1);
#pragma unroll
    for (int q = 0; q < 16; ++q) {
      const s16x8 B = conv_bfrag(ks - 64 * q, sh);
#pragma unroll
      for (int R = q; R < 16; ++R) mfma_ip(acc[R], F[R - q], B);
      if (q == 0 || q == 2 || q == 3 || q == 5 || q == 7 || q == 10 || (q == 15 && gi == 0)) { SCHED0(); __builtin_amdgcn_s_barrier(); }
    }
  }
  mfma_drain();
  float* ybuf = (float*)(st + CV_ST);
  float o[16][2];
#pragma unroll
  for (int R = 0; R < 16; ++R) conv_hlsum(acc[R], hl, o[R][0], o[R][1]);
  float* yl = ybuf + 32 * kq + 16 * hl + rho;
  if (gh) {
#pragma unroll
    for (int R = 0; R < 8; ++R) { yl[128 * R] = o[R][0]; yl[128 * R + 8] = o[R][1]; }
  } else {
#pragma unroll
    for (int R = 8; R < 16; ++R) { yl[128 * R] = o[R][0]; yl[128 * R + 8] = o[R][1]; }
  }
  raw_barrier();
  __builtin_amdgcn_s_barrier();
  if (gh) {
#pragma unroll
    for (int R = 8; R < 16; ++R) { yl[128 * R] += o[R][0]; yl[128 * R + 8] += o[R][1]; }
  } else {
#pragma unroll
    for (int R = 0; R < 8; ++R) { yl[128 * R] = o[R][0] + yl[128 * R]; yl[128 * R + 8] = o[R][1] + yl[128 * R + 8]; }
  }
  asm volatile("s_waitcnt lgkmcnt(0)" ::: "memory");
#pragma unroll
  for (int i = 0; i < 2; ++i) {
    const int t = gh * 1024 + (i * 64 + lane) * 8;
    const s16x8 uv = *(const s16x8*)(us + t * 2);
    const f32x4 s0 = *(const f32x4*)(ybuf + t), s1 = *(const f32x4*)(ybuf + t + 4);
    float sv[8] = {s0[0], s0[1], s0[2], s0[3], s1[0], s1[1], s1[2], s1[3]};
    u32x4 ov;
#pragma unroll
    for (int jj = 0; jj < 4; ++jj) {
      float r0 = rbf(sv[2 * jj]) + rbf(bf2f((u16)uv[2 * jj]) * dsk);
      float r1 = rbf(sv[2 * jj + 1]) + rbf(bf2f((u16)uv[2 * jj + 1]) * dsk);
      ov[jj] = pack2(r0, r1);
    }
    *(u32x4*)(ws + WS_YAT + ((size_t)d * L_ + t) * 2) = ov;
  }
}

template <int MODE> struct GemmCfg {
  static constexpr int BM = MODE ? 128 : 256;
  static constexpr int NPA = MODE ? F_ / 32 : D_ / 32;
  static constexpr int LDB = MODE ? D_ : F_;
  static constexpr int A_BYTES = BM * 128;
  static constexpr int B_BYTES = 64 * 288;
  static constexpr int STAGE = A_BYTES + B_BYTES;
  static constexpr int A_IPW = BM / 64;
  static constexpr int S4_OFF = 3 * STAGE;
  static constexpr int SMEM = 3 * STAGE + (MODE ? 2 * CV_LDS : 0);
};
#define NPRE 2

template <int MODE, int NB>
DI void gemm_wave_body(char* smem, const char* __restrict__ Ag, const char* __restrict__ Bg, const char* __restrict__ lutg,
                       const u16* __restrict__ biasg,
                       int wid, int lane, int wm, int wk, int pb0  , int sblk  ,
                       f32x4 (&acc)[4][5]) {
  using C = GemmCfg<MODE>;
  const int i16 = lane & 15, kq = lane >> 4;
  const bool extraB = wid < 2;
  const u32 aoffl = (u32)lane * 16u;
  const int a_rd = (wm * 8) * 1024 + i16 * 64 + ((kq ^ blk_swz(i16)) * 16);
  const int b_rd = C::A_BYTES + lane * 16 + pb0 * 1024;
  const int b_rs = C::A_BYTES + lane * 16 + sblk * 1024;

  const u32 lds0 = __builtin_amdgcn_readfirstlane(lds_addr_of(smem));
  auto stage = [&](int kt, int buf) {
    const u32 sb = lds0 + buf * C::STAGE;
#pragma unroll
    for (int i = 0; i < C::A_IPW; ++i) {
      const int b = wid * C::A_IPW + i;
      glds16(Ag + ((size_t)(b >> 1) * C::NPA + 2 * kt + (b & 1)) * 1024, aoffl, sb + b * 1024);
    }
    const char* bk = Bg + (size_t)kt * 18432;
#pragma unroll
    for (int i = 0; i < 2; ++i) glds16(bk + (wid + 8 * i) * 1024, aoffl, sb + C::A_BYTES + (wid + 8 * i) * 1024);
    if (extraB) glds16(bk + (wid + 16) * 1024, aoffl, sb + C::A_BYTES + (wid + 16) * 1024);
  };
  auto readA = [&](s16x8 (&a)[4], int buf, int ks) {
    const char* sb = smem + buf * C::STAGE;
#pragma unroll
    for (int mi = 0; mi < 4; ++mi) a[mi] = *(const s16x8*)(sb + a_rd + (mi * 2 + ks) * 1024);
  };
  auto readB = [&](int nb, int buf, int ks) -> s16x8 {
    const char* sb = smem + buf * C::STAGE;
    return *(const s16x8*)(sb + (nb < 4 ? b_rd + nb * 1024 : b_rs) + ks * 9 * 1024);
  };
  s16x8 a0[4], a1[4], b[NB];
  auto mm = [&](s16x8 (&ac)[4], int nb) {
#pragma unroll
    for (int mi = 0; mi < 4; ++mi)
      asm volatile("v_mfma_f32_16x16x32_bf16 %0, %1, %2, %0" : "+v"(acc[mi][nb]) : "v"(b[nb]), "v"(ac[mi]));
  };
  auto sub_pre = [&](s16x8 (&ac)[4]) {
#pragma unroll
    for (int nb = 0; nb < NPRE; ++nb) mm(ac, nb);
    SCHED0();
  };
  auto sub_post = [&](s16x8 (&ac)[4], s16x8 (&an)[4], bool ld, int nbuf, int nks) {
    if (ld) {
      readA(an, nbuf, nks);
#pragma unroll
      for (int nb = 0; nb < NPRE; ++nb) b[nb] = readB(nb, nbuf, nks);
    }
    SCHED0();
#pragma unroll
    for (int nb = NPRE; nb < NB; ++nb) {
      mm(ac, nb);
      if (ld) b[nb] = readB(nb, nbuf, nks);
      SCHED0();
    }
  };
  constexpr int NW0 = C::A_IPW + 2;
#pragma unroll
  for (int mi = 0; mi < 4; ++mi)
#pragma unroll
    for (int nb = 0; nb < NB; ++nb) asm volatile("" : "+v"(acc[mi][nb]));
  if (MODE == 0) {
    raw_barrier();
    readA(a0, 1, 0);
#pragma unroll
    for (int nb = 0; nb < NB; ++nb) b[nb] = readB(nb, 1, 0);
#pragma unroll
    for (int kt = 0; kt < 9; ++kt) {
      SCHED0();
      sub_pre(a0); sub_post(a0, a1, true, (kt + 1) % 3, 1);
      sub_pre(a1);
      raw_barrier();
      sub_post(a1, a0, kt + 1 < 9, (kt + 2) % 3, 0);
    }
  } else {
    stage(0, 0); stage(1, 1);
    if (extraB) wait_vm<NW0 + 1>(); else wait_vm<NW0>();
    raw_barrier();
    readA(a0, 0, wk);
#pragma unroll
    for (int nb = 0; nb < NB; ++nb) b[nb] = readB(nb, 0, wk);
#pragma unroll
    for (int j = 0; j < 18; ++j) {
      if (j + 2 < 18) stage(j + 2, (j + 2) % 3);
      SCHED0();
      if (j & 1) sub_pre(a1); else sub_pre(a0);
      if (j + 1 < 18) {
        if (j + 2 < 18) { if (extraB) wait_vm<NW0 + 1>(); else wait_vm<NW0>(); }
        else wait_vm<0>();
        raw_barrier();
      }
      SCHED0();
      if (j & 1) sub_post(a1, a0, j + 1 < 18, (j + 1) % 3, wk);
      else       sub_post(a0, a1, j + 1 < 18, (j + 1) % 3, wk);
    }
    raw_barrier();
  }
  asm volatile("s_nop 15\n\ts_nop 15" ::: "memory");
#pragma unroll
  for (int mi = 0; mi < 4; ++mi)
#pragma unroll
    for (int nb = 0; nb < NB; ++nb) asm volatile("" : "+v"(acc[mi][nb]));
}

__global__ __launch_bounds__(768) void gemm1_kernel(const u16* __restrict__ w1, const u16* __restrict__ bias, char* __restrict__ ws) {
  using C = GemmCfg<0>;
  __shared__ __attribute__((aligned(1024))) char smem[C::SMEM];
  const int tid = threadIdx.x, lane = tid & 63, wid = __builtin_amdgcn_readfirstlane(tid >> 6);
  const int bid = blockIdx.x;
  const int xcd = bid & 7, idx = bid >> 3;
  const int nt = 4 * xcd + (idx & 3), m0 = (idx >> 2) * 256;
  const int n0 = nt * 144;
  const char* Ag = ws + WS_XN + (size_t)(m0 >> 4) * C::NPA * 1024;
  const char* Bg = ws + WS_W1P + (size_t)nt * 9 * 18432;
  if (wid >= 8) { s4_side_gemm1(smem, bid, wid - 8, lane, Ag, Bg, bias + n0, ws); return; }
  __builtin_amdgcn_s_setprio(1);

  const int wn = wid >> 2;
  const int wm = wid & 3;
  const int odd = nt & 1;
  const int pb0 = odd + 4 * wn;
  const int sblk = odd ? 0 : 8;
  f32x4 acc[4][5];
#pragma unroll
  for (int mi = 0; mi < 4; ++mi)
#pragma unroll
    for (int nb = 0; nb < 5; ++nb) acc[mi][nb] = f32x4{0, 0, 0, 0};
  const int i16 = lane & 15, kq = lane >> 4;
  const char* lutg = ws + WS_LUT;
  STAMP_DECL STAMP_RT(); STAMP_NOW();
  if (wn == 0) gemm_wave_body<0, 5>(smem, Ag, Bg, lutg, bias + n0, wid, lane, wm, 0, pb0, sblk, acc);
  else         gemm_wave_body<0, 4>(smem, Ag, Bg, lutg, bias + n0, wid, lane, wm, 0, pb0, sblk, acc);
  STAMP_NOW();
  f32x2 bf_[5][2];
#pragma unroll
  for (int nb = 0; nb < 5; ++nb) {
    const int blk = nb < 4 ? pb0 + nb : sblk;
    const u32x2 bb = *(const u32x2*)(smem + BIAS_LDS + wid * 1024 + (blk * 16 + 4 * kq) * 2);
#pragma unroll
    for (int e = 0; e < 2; ++e) { bf_[nb][e] = f32x2{lo16(bb[e]), hi16(bb[e])}; asm volatile("" : "+v"(bf_[nb][e])); }
  }
  const char* lut = smem + (LUT_LDS - LUT_LO * 4);
  char* act = ws + WS_ACT;
  const int inblk = i16 * 64 + ((kq ^ blk_swz(i16)) * 16);
#pragma unroll
  for (int mi = 0; mi < 4; ++mi) {
    float hf[5][4], th[5][4];
#pragma unroll
    for (int nb = 0; nb < 5; ++nb) {
      if (nb < 4 || wn == 0) {
#pragma unroll
        for (int e = 0; e < 2; ++e) {
          u32 a = pack2(acc[mi][nb][2 * e], acc[mi][nb][2 * e + 1]);
          u32 h = pack2(lo16(a) + bf_[nb][e][0], hi16(a) + bf_[nb][e][1]);
          hf[nb][2 * e] = lo16(h); hf[nb][2 * e + 1] = hi16(h);
        }
#pragma unroll
        for (int j = 0; j < 4; ++j) {
          const u32 u = __float_as_uint(hf[nb][j]);
          const float m = __builtin_amdgcn_fmed3f(__builtin_fabsf(hf[nb][j]), 0.0009765625f, 128.0f);
          u32 o;
          asm("v_lshl_add_u32 %0, %1, 14, %2" : "=v"(o) : "v"(u >> 31), "v"(__float_as_uint(m) >> 14));
          th[nb][j] = *(const float*)(lut + o);
        }
      }
    }
    const size_t mbg = (size_t)((m0 >> 4) + wm * 4 + mi);
    u32 r[5][2];
#pragma unroll
    for (int nb = 0; nb < 5; ++nb)
      if (nb < 4 || wn == 0) {
#pragma unroll
        for (int e = 0; e < 2; ++e) r[nb][e] = pack2(hf[nb][2 * e] * th[nb][2 * e], hf[nb][2 * e + 1] * th[nb][2 * e + 1]);
      }
#pragma unroll
    for (int pr = 0; pr < 2; ++pr) {
      const int Pb = (n0 + (pb0 + 2 * pr) * 16) >> 5;
      st16_wt(act + (mbg * 144 + Pb) * 1024 + inblk, u32x4{r[2 * pr][0], r[2 * pr][1], r[2 * pr + 1][0], r[2 * pr + 1][1]});
    }
    if (wn == 0) {
      const int nb16 = (n0 >> 4) + sblk;
      st8_wt(act + (mbg * 144 + (nb16 >> 1)) * 1024 + inblk + (nb16 & 1) * 8, u32x2{r[4][0], r[4][1]});
    }
  }
  STAMP_NOW(); STAMP_VM(); STAMP_NOW(); STAMP_RT();
  STAMP_OUT(2, ws);
}

__global__ __launch_bounds__(768) void gemm2_kernel(const u16* __restrict__ w2, const u16* __restrict__ Dskip, char* __restrict__ ws) {
  using C = GemmCfg<1>;
  __shared__ __attribute__((aligned(1024))) char smem[C::SMEM];
  const int tid = threadIdx.x, lane = tid & 63, wid = __builtin_amdgcn_readfirstlane(tid >> 6);
  const int bid = blockIdx.x;
  if (wid >= 8) {
    STAMP_DECL STAMP_RT(); STAMP_NOW();
    __builtin_amdgcn_s_setprio(2);
    s4_side_gemm2(smem + C::S4_OFF, bid, wid - 8, lane, Dskip, ws);
    STAMP_NOW(); STAMP_NOW(); STAMP_NOW(); STAMP_RT();
    STAMP_OUT(3, ws);
    return;
  }
  __builtin_amdgcn_s_setprio(1);
  const int xcd = bid & 7, idx = bid >> 3;
  const int ksplit = xcd >> 1, mt = (xcd & 1) * 8 + (idx >> 2), nt = idx & 3;
  const int m0 = mt * 128, n0 = nt * 144;
  const char* Ag = ws + WS_ACT + ((size_t)(m0 >> 4) * C::NPA + ksplit * 36) * 1024;
  const char* Bg = ws + WS_W2P + ((size_t)nt * 72 + ksplit * 18) * 18432;

  const int wn = wid >> 2;
  const int wmv = wid & 3;
  const int wk = wmv >> 1, wm = wmv & 1;
  const int odd = nt & 1;
  const int pb0 = odd + 4 * wn;
  const int sblk = odd ? 0 : 8;
  f32x4 acc[4][5];
#pragma unroll
  for (int mi = 0; mi < 4; ++mi)
#pragma unroll
    for (int nb = 0; nb < 5; ++nb) acc[mi][nb] = f32x4{0, 0, 0, 0};
  STAMP_DECL STAMP_RT(); STAMP_NOW();
  if (wn == 0) gemm_wave_body<1, 5>(smem, Ag, Bg, nullptr, nullptr, wid, lane, wm, wk, pb0, sblk, acc);
  else         gemm_wave_body<1, 4>(smem, Ag, Bg, nullptr, nullptr, wid, lane, wm, wk, pb0, sblk, acc);
  STAMP_NOW();

  const int i16 = lane & 15, kq = lane >> 4;
  f32x4* red = (f32x4*)smem;
  const int slot = wn * 2 + wm;
  if (wk == 1) {
#pragma unroll
    for (int mi = 0; mi < 4; ++mi)
#pragma unroll
      for (int nb = 0; nb < 5; ++nb)
        if (nb < 4 || wn == 0) red[(slot * 20 + mi * 5 + nb) * 64 + lane] = acc[mi][nb];
  }
  raw_barrier();
  char* partb = ws + WS_PART;
  constexpr size_t PSTRIDE = (size_t)L_ * D_ * 4;
  if (wk == 0) {
#pragma unroll
    for (int mi = 0; mi < 4; ++mi)
#pragma unroll
      for (int nb = 0; nb < 5; ++nb)
        if (nb < 4 || wn == 0) {
          acc[mi][nb] += red[(slot * 20 + mi * 5 + nb) * 64 + lane];
          const size_t mbg = (size_t)((m0 >> 4) + wm * 4 + mi);
          const int nbg = (n0 >> 4) + (nb < 4 ? pb0 + nb : sblk);
          st16_wt(partb + ksplit * PSTRIDE + (mbg * 36 + nbg) * 1024 + i16 * 64 + kq * 16, __builtin_bit_cast(u32x4, acc[mi][nb]));
        }
  }
  STAMP_NOW(); STAMP_VM(); STAMP_NOW(); STAMP_RT();
  STAMP_OUT(3, ws);
}

__global__ __launch_bounds__(256) void final_kernel(const u16* __restrict__ x, const u16* __restrict__ Dskip,
                                                    const char* __restrict__ ws, u16* __restrict__ out) {
  __shared__ __attribute__((aligned(16))) u16 yt[64 * 72];
  const int tid = threadIdx.x;
  const int tt = blockIdx.x / 9, dt = blockIdx.x % 9;
  const int t0 = tt * 64, d0 = dt * 64;
  const u16* yat = (const u16*)(ws + WS_YAT);
  const char* part = ws + WS_PART;
  u32x4 yv[2], xv[2];
  f32x4 p[2][4][2];
#pragma unroll
  for (int i = 0; i < 2; ++i) {
    const int c = tid + 256 * i;
    const int r = c >> 3, ch = c & 7;
    if (dt < 8) yv[i] = *(const u32x4*)(yat + (size_t)(d0 + r) * L_ + t0 + ch * 8);
    else {
      const float* yp = (const float*)(ws + WS_YP) + (size_t)r * 4 * L_ + t0 + ch * 8;
      f32x4 a0 = (*(const f32x4*)(yp) + *(const f32x4*)(yp + L_)) + (*(const f32x4*)(yp + 2 * L_) + *(const f32x4*)(yp + 3 * L_));
      f32x4 a1 = (*(const f32x4*)(yp + 4) + *(const f32x4*)(yp + L_ + 4)) + (*(const f32x4*)(yp + 2 * L_ + 4) + *(const f32x4*)(yp + 3 * L_ + 4));
      const u32x4 uv = *(const u32x4*)((const u16*)(ws + WS_XNT) + (size_t)(d0 + r) * XT_PITCH + 128 + t0 + ch * 8);
      const float dsk = bf2f(Dskip[d0 + r]);
      float sv[8] = {a0[0], a0[1], a0[2], a0[3], a1[0], a1[1], a1[2], a1[3]};
#pragma unroll
      for (int jj = 0; jj < 4; ++jj)
        yv[i][jj] = pack2(rbf(sv[2 * jj]) + rbf(lo16(uv[jj]) * dsk), rbf(sv[2 * jj + 1]) + rbf(hi16(uv[jj]) * dsk));
    }
    xv[i] = *(const u32x4*)(x + (size_t)(t0 + r) * D_ + d0 + ch * 8);
    const int t = t0 + r, d = d0 + ch * 8;
    const size_t po = ((size_t)(t >> 4) * 36 + (d >> 4)) * 1024 + (t & 15) * 64 + ((d >> 2) & 3) * 16;
#pragma unroll
    for (int s = 0; s < 4; ++s) {
      p[i][s][0] = *(const f32x4*)(part + (size_t)s * L_ * D_ * 4 + po);
      p[i][s][1] = *(const f32x4*)(part + (size_t)s * L_ * D_ * 4 + po + 16);
    }
  }
#pragma unroll
  for (int i = 0; i < 2; ++i) {
    const int c = tid + 256 * i;
    *(u32x4*)(yt + (c >> 3) * 72 + (c & 7) * 8) = yv[i];
  }
  __syncthreads();
#pragma unroll
  for (int i = 0; i < 2; ++i) {
    const int c = tid + 256 * i;
    const int r = c >> 3, ch = c & 7;
    f32x4 s0 = (p[i][0][0] + p[i][1][0]) + (p[i][2][0] + p[i][3][0]);
    f32x4 s1 = (p[i][0][1] + p[i][1][1]) + (p[i][2][1] + p[i][3][1]);
    float s[8] = {s0[0], s0[1], s0[2], s0[3], s1[0], s1[1], s1[2], s1[3]};
    u32x4 ov;
#pragma unroll
    for (int j = 0; j < 4; ++j) {
      float r2[2];
#pragma unroll
      for (int h = 0; h < 2; ++h) {
        int e = 2 * j + h;
        float xx = h ? hi16(xv[i][j]) : lo16(xv[i][j]);
        float ya = bf2f(yt[(ch * 8 + e) * 72 + r]);
        r2[h] = rbf(xx + ya) + rbf(s[e]);
      }
      ov[j] = pack2(r2[0], r2[1]);
    }
    *(u32x4*)(out + (size_t)(t0 + r) * D_ + d0 + ch * 8) = ov;
  }
}

extern "C" void kernel_launch(void* const* d_in, const int* in_sizes, int n_in,
                              void* d_out, int out_size, void* d_ws, size_t ws_size,
                              hipStream_t stream) {
  const u16* x      = (const u16*)d_in[0];
  const u16* A_re   = (const u16*)d_in[1];
  const u16* A_im   = (const u16*)d_in[2];
  const u16* B_re   = (const u16*)d_in[3];
  const u16* B_im   = (const u16*)d_in[4];
  const u16* C_re   = (const u16*)d_in[5];
  const u16* C_im   = (const u16*)d_in[6];
  const u16* log_dt = (const u16*)d_in[7];
  const u16* Dskip  = (const u16*)d_in[8];
  const u16* w1     = (const u16*)d_in[10];
  const u16* b1     = (const u16*)d_in[11];
  const u16* w2     = (const u16*)d_in[12];
  char* ws = (char*)d_ws;
  S4Params P{A_re, A_im, B_re, B_im, C_re, C_im, log_dt};
  prep_kernel<<<PREP_BLOCKS, 256, 0, stream>>>(x, P, w1, w2, ws);
  gemm1_kernel<<<256, 768, 0, stream>>>(w1, b1, ws);
  gemm2_kernel<<<256, 768, 0, stream>>>(w2, Dskip, ws);
  final_kernel<<<288, 256, 0, stream>>>(x, Dskip, ws, (u16*)d_out);
}
```

```cpp
#include <hip/hip_runtime.h>
#include <hip/hip_bf16.h>
#include <stdint.h>

typedef unsigned short u16;
typedef unsigned int u32;
using s16x8 = __attribute__((ext_vector_type(8))) short;
using s16x4 = __attribute__((ext_vector_type(4))) short;
using f32x4 = __attribute__((ext_vector_type(4))) float;
using u32x4 = __attribute__((ext_vector_type(4))) unsigned int;
using u32x2 = __attribute__((ext_vector_type(2))) unsigned int;
typedef u32x4 __attribute__((aligned(4))) u32x4_a4;

#define DI __device__ __forceinline__
#define STAMP_DECL
#define STAMP_NOW()
#define STAMP_RT()
#define STAMP_VM()
#define STAMP_OUT(kid, ws)

constexpr int L_ = 2048, D_ = 576, N_ = 128, F_ = 4608;
constexpr int XT_PITCH = 2048 + 128;
constexpr int KX_NW = 1032;
constexpr int LUT_LO = 0x3A80, LUT_HI = 0x4300;
constexpr int LUT_ENTRIES = 8192;

constexpr size_t WS_CNT  = 0;
constexpr size_t WS_XN   = 1024;
constexpr size_t WS_XNT  = WS_XN + (size_t)L_ * D_ * 2;
constexpr size_t WS_KX   = WS_XNT + (size_t)D_ * XT_PITCH * 2;
constexpr size_t WS_LUT  = WS_KX + (size_t)D_ * 2 * KX_NW * 4;
constexpr size_t WS_YAT  = WS_LUT + LUT_ENTRIES * 4;
constexpr size_t WS_ACT  = WS_YAT + (size_t)D_ * L_ * 2;
constexpr size_t WS_PART = WS_ACT + (size_t)L_ * F_ * 2;
constexpr size_t WS_YP   = WS_PART + (size_t)4 * L_ * D_ * 4;
constexpr size_t WS_W1P  = WS_YP + (size_t)64 * 4 * L_ * 4;
constexpr size_t WS_W2P  = WS_W1P + (size_t)288 * 18432;

DI float bf2f(u16 b) { return __uint_as_float(((u32)b) << 16); }
DI u16 f2bf(float f) {
  __hip_bfloat16 h = __float2bfloat16(f);
  return *reinterpret_cast<u16*>(&h);
}
DI float rbf(float f) { return bf2f(f2bf(f)); }
typedef __bf16 bf16x2_t __attribute__((ext_vector_type(2)));
using f32x2 = __attribute__((ext_vector_type(2))) float;
DI u32 pack2(float lo, float hi) {
  f32x2 v = {lo, hi};
  return __builtin_bit_cast(u32, __builtin_convertvector(v, bf16x2_t));
}
DI float lo16(u32 v) { return __uint_as_float(v << 16); }
DI float hi16(u32 v) { return __uint_as_float(v & 0xffff0000u); }
DI void st16_wt(void* p, u32x4 v) { asm volatile("global_store_dwordx4 %0, %1, off sc1\n\ts_nop 1" ::"v"(p), "v"(v) : "memory"); }
DI void st8_wt(void* p, u32x2 v) { asm volatile("global_store_dwordx2 %0, %1, off sc1\n\ts_nop 1" ::"v"(p), "v"(v) : "memory"); }

DI int blk_swz(int i16) { return (-(i16 >> 2)) & 3; }

DI void mfma_ip(f32x4& acc, const s16x8& a, const s16x8& b) {
  asm volatile("s_nop 1\n\tv_mfma_f32_16x16x32_bf16 %0, %1, %2, %0" : "+v"(acc) : "v"(a), "v"(b));
}
DI void mfma_drain() { asm volatile("s_nop 15\n\ts_nop 15" ::: "memory"); }

DI void cexp_pos(float lr2, float lh, float pos, float& er, float& ei) {
  const float mag = __builtin_amdgcn_exp2f(lr2 * pos);
  const float f = __builtin_amdgcn_fractf(lh * pos);
  er = mag * __builtin_amdgcn_cosf(f);
  ei = mag * __builtin_amdgcn_sinf(f);
}
DI int kg_row_pos(int row) {
  if (row < 16) return 128 * (row >> 3) + (row & 7);
  if (row < 24) { int r = row - 16; return 256 + 128 * (r >> 2) + 2 * (r & 3); }
  if (row < 32) { int r = row - 24; return 512 + 128 * (r >> 1) + 4 * (r & 1); }
  if (row < 40) return 1024 + 128 * (row - 32);
  if (row == 40) return 2048;
  return 0;
}
struct KIdxTab { u32 v[1088]; };
constexpr int kg_bf16_round(int l) {
  int sft = l >= 1024 ? 3 : l >= 512 ? 2 : l >= 256 ? 1 : 0;
  if (sft == 0) return l;
  int half = 1 << (sft - 1), q = l >> sft;
  return ((l + half - 1 + (q & 1)) >> sft) << sft;
}
constexpr int kg_pos_index(int p) {
  int sg = p >= 2048 ? 4 : p >= 1024 ? 3 : p >= 512 ? 2 : p >= 256 ? 1 : 0;
  int base = sg ? (128 << sg) : 0, rowbase = sg ? 8 + 8 * sg : 0;
  int q = p - base;
  int row = rowbase + (q >> 7) * (8 >> sg) + ((q & 7) >> sg);
  return row * 16 + ((q & 127) >> 3);
}
constexpr KIdxTab make_kidx() {
  KIdxTab t{};
  for (int w = 0; w < 1088; ++w) {
    int l0 = 2047 - 2 * w, l1 = 2046 - 2 * w;
    u32 i0 = l0 >= 0 ? kg_pos_index(kg_bf16_round(l0)) : 768, i1 = l1 >= 0 ? kg_pos_index(kg_bf16_round(l1)) : 768;
    t.v[w] = (i0 * 4) | ((i1 * 4) << 16);
  }
  return t;
}
__device__ const KIdxTab kIdxTab = make_kidx();

struct S4Params { const u16 *A_re, *A_im, *B_re, *B_im, *C_re, *C_im, *log_dt; };
struct KgFrag { u32x4 Bh, Bl, Ah[3], Al[3]; };

DI void kg_load_params(const S4Params& P, int d, int n0, u32x4 (&v)[6]) {
  const size_t o = (size_t)d * N_ + n0;
  v[0] = *(const u32x4*)(P.A_re + o); v[1] = *(const u32x4*)(P.A_im + o);
  v[2] = *(const u32x4*)(P.B_re + o); v[3] = *(const u32x4*)(P.B_im + o);
  v[4] = *(const u32x4*)(P.C_re + o); v[5] = *(const u32x4*)(P.C_im + o);
}
DI void kg_mode(const u32x4 (&v)[6], int g, float dt, const float (&posA)[3], float posB, KgFrag& f) {
  const int e = g >> 1, gg = g & 3;
  const float lr = dt * ((g & 1) ? hi16(v[0][e]) : lo16(v[0][e])), li = dt * ((g & 1) ? hi16(v[1][e]) : lo16(v[1][e]));
  const float br = dt * ((g & 1) ? hi16(v[2][e]) : lo16(v[2][e])), bi = dt * ((g & 1) ? hi16(v[3][e]) : lo16(v[3][e]));
  const float cr = (g & 1) ? hi16(v[4][e]) : lo16(v[4][e]), ci = (g & 1) ? hi16(v[5][e]) : lo16(v[5][e]);
  const float kr = cr * br - ci * bi, ki = cr * bi + ci * br;
  const float lr2 = lr * 1.44269504088896340736f;
  const float lh = li * 0.15915494309189535f;
  float zr, zi;
  cexp_pos(lr2, lh, posB, zr, zi);
  {
    const float xr = kr * zr - ki * zi, xi = -(kr * zi + ki * zr);
    const u32 h = pack2(xr, xi);
    f.Bh[gg] = h; f.Bl[gg] = pack2(xr - lo16(h), xi - hi16(h));
  }
#pragma unroll
  for (int t = 0; t < 3; ++t) {
    float er, ei;
    cexp_pos(lr2, lh, posA[t], er, ei);
    const u32 h = pack2(er, ei);
    f.Ah[t][gg] = h; f.Al[t][gg] = pack2(er - lo16(h), ei - hi16(h));
  }
}
DI void kg_mma(const KgFrag& f, f32x4 (&acc)[3]) {
#pragma unroll
  for (int t = 0; t < 3; ++t) {
    mfma_ip(acc[t], __builtin_bit_cast(s16x8, f.Al[t]), __builtin_bit_cast(s16x8, f.Bh));
    mfma_ip(acc[t], __builtin_bit_cast(s16x8, f.Ah[t]), __builtin_bit_cast(s16x8, f.Bl));
    mfma_ip(acc[t], __builtin_bit_cast(s16x8, f.Ah[t]), __builtin_bit_cast(s16x8, f.Bh));
  }
}
template <int NTHR> DI void kg_gather_store(const float* kv, int d, int t, char* __restrict__ ws) {
  u32* kx = (u32*)(ws + WS_KX) + (size_t)d * 2 * KX_NW;
#pragma unroll
  for (int i = 0; i < 256 / NTHR; ++i) {
    const int w = (i * NTHR + t) * 4;
    const u32x4 ti = *(const u32x4*)(kIdxTab.v + w);
    u32x4 hi, lo;
#pragma unroll
    for (int j = 0; j < 4; ++j) {
      const float k0 = *(const float*)((const char*)kv + (ti[j] & 0xffffu)), k1 = *(const float*)((const char*)kv + (ti[j] >> 16));
      const u32 h = pack2(k0, k1);
      hi[j] = h; lo[j] = pack2(k0 - lo16(h), k1 - hi16(h));
    }
    *(u32x4*)(kx + w) = hi;
    *(u32x4*)(kx + KX_NW + w) = lo;
  }
  if (t < 8) {
    const u32 tt = kIdxTab.v[1024 + t];
    const float k0 = *(const float*)((const char*)kv + (tt & 0xffffu)), k1 = *(const float*)((const char*)kv + (tt >> 16));
    const u32 h = pack2(k0, k1);
    kx[1024 + t] = h; kx[KX_NW + 1024 + t] = pack2(k0 - lo16(h), k1 - hi16(h));
  }
}

typedef __attribute__((address_space(3))) s16x4 lds_s16x4;
template <int N> DI void wait_vm() { asm volatile("s_waitcnt vmcnt(%0)" ::"n"(N) : "memory"); }
DI void raw_barrier() { asm volatile("s_waitcnt lgkmcnt(0)" ::: "memory"); __builtin_amdgcn_s_barrier(); }
#define SCHED0() __builtin_amdgcn_sched_barrier(0)
DI void glds16(const void* sbase, u32 voff, u32 lds_addr) {
  u32 keep;
  asm volatile("s_mov_b32 %0, m0\n\ts_mov_b32 m0, %3\n\ts_nop 0\n\tglobal_load_lds_dwordx4 %1, %2\n\ts_mov_b32 m0, %0"
               : "=&s"(keep) : "v"(voff), "s"(sbase), "s"(lds_addr) : "memory");
}
DI u32 lds_addr_of(const char* p) { return (u32)(size_t)(const __attribute__((address_space(3))) char*)p; }

constexpr int LN_ROWS = 16;
constexpr int LN_BLOCKS = L_ / LN_ROWS;
constexpr int LUT_BLOCKS = 32;
constexpr int KG_BLOCKS = D_;
constexpr int PK_BLOCKS = 576;
constexpr int PREP_BLOCKS = KG_BLOCKS + LN_BLOCKS + LUT_BLOCKS + PK_BLOCKS;
constexpr int LN_PITCH = D_ + 8;

__global__ __launch_bounds__(256) void prep_kernel(const u16* __restrict__ x, S4Params P, const u16* __restrict__ w1,
                                                   const u16* __restrict__ w2, char* __restrict__ ws) {
  __shared__ __attribute__((aligned(1024))) char smem[LN_ROWS * LN_PITCH * 2];
  const int tid = threadIdx.x;
  if (blockIdx.x >= KG_BLOCKS + LN_BLOCKS + LUT_BLOCKS) {
    const int tb = blockIdx.x - (KG_BLOCKS + LN_BLOCKS + LUT_BLOCKS);
    const int lane = tid & 63, wid = __builtin_amdgcn_readfirstlane(tid >> 6);
    const bool is2 = tb >= 288;
    const int tt = is2 ? tb - 288 : tb;
    const int nt = is2 ? tt / 72 : tt / 9, kt = is2 ? tt % 72 : tt % 9;
    const int ldb = is2 ? D_ : F_;
    const char* src = (const char*)((is2 ? w2 : w1) + (size_t)kt * 64 * ldb + nt * 144);
    const u32 l0 = __builtin_amdgcn_readfirstlane(lds_addr_of(smem));
#pragma unroll
    for (int i = 0; i < 5; ++i) {
      const int ins = wid + 4 * i;
      if (ins < 18) {
        const int Lc = ins * 64 + lane, k = Lc / 18, c8 = Lc % 18;
        glds16(src, (u32)(k * ldb + c8 * 8) * 2u, l0 + ins * 1024);
      }
    }
    wait_vm<0>();
    raw_barrier();
    const int i16 = lane & 15, kq = lane >> 4;
    const int rd = (4 * kq + (i16 >> 2)) * 288 + (i16 & 3) * 8;
    char* dst = ws + (is2 ? WS_W2P : WS_W1P) + (size_t)tt * 18432 + lane * 16;
#pragma unroll
    for (int i = 0; i < 5; ++i) {
      const int fr = wid + 4 * i;
      if (fr < 18) {
        const int ks = fr / 9, nb = fr % 9;
        s16x4 lo = __builtin_amdgcn_ds_read_tr16_b64_v4i16((lds_s16x4*)(smem + rd + ks * 32 * 288 + nb * 32));
        s16x4 hi = __builtin_amdgcn_ds_read_tr16_b64_v4i16((lds_s16x4*)(smem + rd + ks * 32 * 288 + 16 * 288 + nb * 32));
        const s16x8 v = s16x8{lo[0], lo[1], lo[2], lo[3], hi[0], hi[1], hi[2], hi[3]};
        st16_wt(dst + fr * 1024, __builtin_bit_cast(u32x4, v));
      }
    }
    return;
  }
  if (blockIdx.x >= LN_BLOCKS && blockIdx.x < LN_BLOCKS + KG_BLOCKS) {
    const int d = blockIdx.x - LN_BLOCKS, lane = tid & 63, wid = tid >> 6;
    const int i16 = lane & 15, kq = lane >> 4;
    float* red = (float*)smem;
    float* kv = red + 4 * 768;
    u32x4 v[6];
    kg_load_params(P, d, wid * 32 + kq * 8, v);
    const float dt = rbf(expf(bf2f(P.log_dt[d])));
    f32x4 acc[3] = {f32x4{0, 0, 0, 0}, f32x4{0, 0, 0, 0}, f32x4{0, 0, 0, 0}};
    float posA[3];
#pragma unroll
    for (int t = 0; t < 3; ++t) posA[t] = (float)kg_row_pos(16 * t + i16);
    const float posB = (float)(8 * i16);
    KgFrag f;
#pragma unroll
    for (int g = 0; g < 8; ++g) { kg_mode(v, g, dt, posA, posB, f); if ((g & 3) == 3) kg_mma(f, acc); }
    mfma_drain();
#pragma unroll
    for (int t = 0; t < 3; ++t)
#pragma unroll
      for (int j = 0; j < 4; ++j) red[wid * 768 + (16 * t + 4 * kq + j) * 16 + i16] = acc[t][j];
    __syncthreads();
#pragma unroll
    for (int i = 0; i < 3; ++i) { int e = tid + 256 * i; kv[e] = (red[e] + red[768 + e]) + (red[2 * 768 + e] + red[3 * 768 + e]); }
    if (tid == 0) kv[768] = 0.f;
    __syncthreads();
    kg_gather_store<256>(kv, d, tid, ws);
    return;
  }
  const int bid = blockIdx.x < LN_BLOCKS ? blockIdx.x : blockIdx.x - KG_BLOCKS;
  if (bid < LN_BLOCKS) {
    u16* tile = (u16*)smem;
    const int t0 = bid * LN_ROWS;
    const int r = tid >> 4, j16 = tid & 15;
    u32x2 v[9];
#pragma unroll
    for (int i = 0; i < 9; ++i) v[i] = *(const u32x2*)(x + (size_t)(t0 + r) * D_ + (j16 + 16 * i) * 4);
    float s = 0.f;
#pragma unroll
    for (int i = 0; i < 9; ++i)
#pragma unroll
      for (int e = 0; e < 2; ++e) s += lo16(v[i][e]) + hi16(v[i][e]);
    s += __shfl_xor(s, 1); s += __shfl_xor(s, 2); s += __shfl_xor(s, 4); s += __shfl_xor(s, 8);
    const float mean = s * (1.0f / 576.0f);
    float q = 0.f;
#pragma unroll
    for (int i = 0; i < 9; ++i)
#pragma unroll
      for (int e = 0; e < 2; ++e) { float a = lo16(v[i][e]) - mean, b = hi16(v[i][e]) - mean; q += a * a + b * b; }
    q += __shfl_xor(q, 1); q += __shfl_xor(q, 2); q += __shfl_xor(q, 4); q += __shfl_xor(q, 8);
    const float var = q * (1.0f / 576.0f);
    const float mb = rbf(mean), vb = rbf(var);
    const float den = rbf(sqrtf(rbf(vb + 1e-5f)));
    const float rden = 1.0f / den;
    auto qdiv = [&](float a) { float qq = a * rden; return __builtin_fmaf(__builtin_fmaf(-qq, den, a), rden, qq); };
#pragma unroll
    for (int i = 0; i < 9; ++i) {
      u32x2 o;
#pragma unroll
      for (int e = 0; e < 2; ++e) {
        const u32 c = pack2(lo16(v[i][e]) - mb, hi16(v[i][e]) - mb);
        o[e] = pack2(qdiv(lo16(c)), qdiv(hi16(c)));
      }
      *(u32x2*)(tile + r * LN_PITCH + (j16 + 16 * i) * 4) = o;
    }
    __syncthreads();
    char* xn_g = ws + WS_XN + (size_t)bid * 18 * 1024;
#pragma unroll
    for (int i = 0; i < 5; ++i) {
      const int c = tid + 256 * i;
      if (c < 1152) {
        const int P = c >> 6, w = c & 63;
        const int i16 = w >> 2, kq = (w & 3) ^ blk_swz(w >> 2);
        const u16* src = tile + i16 * LN_PITCH + 32 * P + 4 * kq;
        u32x2 a = *(const u32x2*)src, b = *(const u32x2*)(src + 16);
        *(u32x4*)(xn_g + (size_t)c * 16) = u32x4{a[0], a[1], b[0], b[1]};
      }
    }
    u16* xt_g = (u16*)(ws + WS_XNT);
#pragma unroll
    for (int i = 0; i < 5; ++i) {
      const int c = tid + 256 * i;
      if (c < 1152) {
        const int d = c >> 1, tq = c & 1;
        u32x4 o;
#pragma unroll
        for (int j = 0; j < 4; ++j) {
          u32 lo = tile[(tq * 8 + 2 * j) * LN_PITCH + d], hi = tile[(tq * 8 + 2 * j + 1) * LN_PITCH + d];
          o[j] = lo | (hi << 16);
        }
        *(u32x4*)(xt_g + (size_t)d * XT_PITCH + 128 + t0 + tq * 8) = o;
      }
    }
    if (tid < 72) {
      int c = bid * 72 + tid;
      *(u32x4*)(xt_g + (size_t)(c >> 4) * XT_PITCH + (c & 15) * 8) = u32x4{0u, 0u, 0u, 0u};
    }
  } else {
    const int e8 = (bid - LN_BLOCKS) * 256 + tid;
    const int sign = e8 >> 12, e = e8 & 4095;
    float* lut = (float*)(ws + WS_LUT);
    int a = LUT_LO + e;
    if (a > LUT_HI) a = LUT_HI;
    float h = bf2f((u16)(a | (sign << 15)));
    float ex = rbf(expf(-fabsf(h)));
    float lp = rbf(log1pf(ex));
    float sp = rbf(lp + (h > 0.f ? h : 0.f));
    float th = tanhf(sp);
    lut[e8] = rbf(th);
  }
}

constexpr int BIAS_LDS = 51200;
constexpr int LUT_LDS = 61440;
constexpr int KG_LDS = 3200;
constexpr int CV_ST = 14848;
constexpr int CV_LDS = CV_ST + 8192;

DI void conv_stage_part(char* st, int d, int lane, int first, int step, const char* __restrict__ ws) {
  const char* ug = ws + WS_XNT + (size_t)d * XT_PITCH * 2;
  const char* kg = ws + WS_KX + (size_t)d * 2 * KX_NW * 4;
  const u32 l0 = __builtin_amdgcn_readfirstlane(lds_addr_of(st));
#pragma unroll
  for (int i = 0; i < 14; ++i) {
    if (i % step == first) {
      if (i < 5) glds16(ug + i * 1024, (u32)lane * 16u, l0 + i * 1024);
      else if (i < 13) glds16(kg + (i - 5) * 1024, (u32)lane * 16u, l0 + 5120 + (i - 5) * 1024);
      else glds16(kg + 8192, (u32)(lane & 3) * 16u, l0 + 5120 + 8192);
    }
  }
}
DI s16x8 conv_bfrag(const u32* kp, u32 sh) {
  const u32 e0 = kp[0], e1 = kp[1], e2 = kp[2], e3 = kp[3], e4 = kp[4];
  const u32x4 v = {__builtin_amdgcn_alignbyte(e1, e0, sh), __builtin_amdgcn_alignbyte(e2, e1, sh),
                   __builtin_amdgcn_alignbyte(e3, e2, sh), __builtin_amdgcn_alignbyte(e4, e3, sh)};
  return __builtin_bit_cast(s16x8, v);
}
DI void conv_hlsum(const f32x4& a, int hl, float& o0, float& o1) {
  const float s0 = hl ? a[0] : a[2], s1 = hl ? a[1] : a[3];
  const float k0 = hl ? a[2] : a[0], k1 = hl ? a[3] : a[1];
  o0 = k0 + __builtin_bit_cast(float, __builtin_amdgcn_update_dpp(0, __builtin_bit_cast(int, s0), 0x128, 0xf, 0xf, false));
  o1 = k1 + __builtin_bit_cast(float, __builtin_amdgcn_update_dpp(0, __builtin_bit_cast(int, s1), 0x128, 0xf, 0xf, false));
}

DI void s4_side_gemm1(char* smem, int bid, int j, int lane, const char* __restrict__ Ag, const char* __restrict__ Bg,
                      const u16* __restrict__ biasg, char* __restrict__ ws) {
  const int i16 = lane & 15, kq = lane >> 4;
  {
    constexpr int STAGE = 51200;
    const u32 lds0 = __builtin_amdgcn_readfirstlane(lds_addr_of(smem));
    const u32 lo = (u32)lane * 16u;
    auto stage = [&](int kt, int buf) {
      const u32 sb = lds0 + buf * STAGE;
#pragma unroll
      for (int i = 0; i < 8; ++i) {
        const int bb = j + 4 * i;
        glds16(Ag + ((size_t)(bb >> 1) * 18 + 2 * kt + (bb & 1)) * 1024, lo, sb + bb * 1024);
      }
      const char* bk = Bg + (size_t)kt * 18432;
#pragma unroll
      for (int i = 0; i < 4; ++i) glds16(bk + (j + 4 * i) * 1024, lo, sb + 32768 + (j + 4 * i) * 1024);
      if (j < 2) glds16(bk + (j + 16) * 1024, lo, sb + 32768 + (j + 16) * 1024);
    };
    stage(0, 1); stage(1, 2);
    if (j < 2) wait_vm<13>(); else wait_vm<12>();
    __builtin_amdgcn_s_barrier();
#pragma unroll 1
    for (int kt = 0; kt < 7; ++kt) {
      stage(kt + 2, kt % 3);
      if (j < 2) wait_vm<13>(); else wait_vm<12>();
      __builtin_amdgcn_s_barrier();
    }
#pragma unroll
    for (int i = 0; i < 8; ++i) glds16(ws + WS_LUT + (j * 8 + i) * 1024, lo, lds0 + LUT_LDS + (j * 8 + i) * 1024);
#pragma unroll
    for (int i = 0; i < 2; ++i) glds16(biasg, lo < 288u ? lo : 272u, lds0 + BIAS_LDS + (j * 2 + i) * 1024);
    wait_vm<10>();
    __builtin_amdgcn_s_barrier();
    wait_vm<0>();
    __builtin_amdgcn_s_barrier();
  }
  const int dx = 512 + (bid >> 2), g = bid & 3;
  char* st = smem + (j < 3 ? j * CV_ST : 102400);
  conv_stage_part(st, dx, lane, 0, 1, ws);
  wait_vm<0>();
  {
    const int rho = i16 & 7, hl = i16 >> 3;
    const char* us = st + 256;
    const u32 sh = ((1 - rho) & 1) * 2;
    const u32* ks = (const u32*)(st + 5120) + hl * KX_NW + ((2047 - 8 * (4 * g + kq) - rho) >> 1);
    f32x4 ya[4] = {f32x4{0, 0, 0, 0}, f32x4{0, 0, 0, 0}, f32x4{0, 0, 0, 0}, f32x4{0, 0, 0, 0}};
    const char* fb = us + (16 * j - 4 * g + i16 - kq) * 16;
#pragma unroll
    for (int q = 0; q < 16; ++q) {
      const s16x8 B = conv_bfrag(ks - 64 * q, sh);
#pragma unroll
      for (int r = 0; r < 4; ++r) {
        if (4 * r + 3 >= q) {
          if (j + 4 * r >= q) {
            const s16x8 F = *(const s16x8*)(fb + (4 * r - q) * 256);
            mfma_ip(ya[r], F, B);
          }
        }
      }
    }
    mfma_drain();
    float* yp = (float*)(ws + WS_YP) + ((size_t)(dx - 512) * 4 + g) * L_;
#pragma unroll
    for (int r = 0; r < 4; ++r) {
      float o0, o1;
      conv_hlsum(ya[r], hl, o0, o1);
      float* q = yp + 128 * (j + 4 * r) + 32 * kq + 16 * hl + rho;
      q[0] = o0; q[8] = o1;
    }
  }
}

DI void s4_side_gemm2(char* scratch, int bid, int j, int lane, const u16* __restrict__ Dskip, char* __restrict__ ws) {
  const int d = 2 * bid + (j >> 1), gh = j & 1;
  char* st = scratch + (j >> 1) * CV_LDS;
  conv_stage_part(st, d, lane, gh, 2, ws);
  const float dsk = bf2f(Dskip[d]);
  __builtin_amdgcn_s_barrier(); __builtin_amdgcn_s_barrier(); __builtin_amdgcn_s_barrier(); __builtin_amdgcn_s_barrier();
  wait_vm<0>();
  __builtin_amdgcn_s_barrier();
  const int i16 = lane & 15, kq = lane >> 4;
  const int rho = i16 & 7, hl = i16 >> 3;
  const char* us = st + 256;
  const u32 sh = ((1 - rho) & 1) * 2;
  f32x4 acc[16];
#pragma unroll
  for (int R = 0; R < 16; ++R) acc[R] = f32x4{0, 0, 0, 0};
#pragma unroll
  for (int gi = 0; gi < 2; ++gi) {
    const int g = 2 * gh + gi;
    s16x8 F[16];
#pragma unroll
    for (int dl = 0; dl < 16; ++dl) F[dl] = *(const s16x8*)(us + (16 * dl - 4 * g + i16 - kq) * 16);
    const u32* ks = (const u32*)(st + 5120) + hl * KX_NW + ((2047 - 8 * (4 * g + kq) - rho) >> 1);
#pragma unroll
    for (int q = 0; q < 16; ++q) {
      const s16x8 B = conv_bfrag(ks - 64 * q, sh);
#pragma unroll
      for (int R = q; R < 16; ++R) mfma_ip(acc[R], F[R - q], B);
      if (q == 0 || q == 2 || q == 3 || q == 5 || q == 7 || q == 10 || (q == 15 && gi == 0)) { SCHED0(); __builtin_amdgcn_s_barrier(); }
    }
  }
  mfma_drain();
  float* ybuf = (float*)(st + CV_ST);
  float o[16][2];
#pragma unroll
  for (int R = 0; R < 16; ++R) conv_hlsum(acc[R], hl, o[R][0], o[R][1]);
  float* yl = ybuf + 32 * kq + 16 * hl + rho;
  if (gh) {
#pragma unroll
    for (int R = 0; R < 8; ++R) { yl[128 * R] = o[R][0]; yl[128 * R + 8] = o[R][1]; }
  } else {
#pragma unroll
    for (int R = 8; R < 16; ++R) { yl[128 * R] = o[R][0]; yl[128 * R + 8] = o[R][1]; }
  }
  raw_barrier();
  __builtin_amdgcn_s_barrier();
  if (gh) {
#pragma unroll
    for (int R = 8; R < 16; ++R) { yl[128 * R] += o[R][0]; yl[128 * R + 8] += o[R][1]; }
  } else {
#pragma unroll
    for (int R = 0; R < 8; ++R) { yl[128 * R] = o[R][0] + yl[128 * R]; yl[128 * R + 8] = o[R][1] + yl[128 * R + 8]; }
  }
  asm volatile("s_waitcnt lgkmcnt(0)" ::: "memory");
#pragma unroll
  for (int i = 0; i < 2; ++i) {
    const int t = gh * 1024 + (i * 64 + lane) * 8;
    const s16x8 uv = *(const s16x8*)(us + t * 2);
    const f32x4 s0 = *(const f32x4*)(ybuf + t), s1 = *(const f32x4*)(ybuf + t + 4);
    float sv[8] = {s0[0], s0[1], s0[2], s0[3], s1[0], s1[1], s1[2], s1[3]};
    u32x4 ov;
#pragma unroll
    for (int jj = 0; jj < 4; ++jj) {
      float r0 = rbf(sv[2 * jj]) + rbf(bf2f((u16)uv[2 * jj]) * dsk);
      float r1 = rbf(sv[2 * jj + 1]) + rbf(bf2f((u16)uv[2 * jj + 1]) * dsk);
      ov[jj] = pack2(r0, r1);
    }
    *(u32x4*)(ws + WS_YAT + ((size_t)d * L_ + t) * 2) = ov;
  }
}

template <int MODE> struct GemmCfg {
  static constexpr int BM = MODE ? 128 : 256;
  static constexpr int NPA = MODE ? F_ / 32 : D_ / 32;
  static constexpr int LDB = MODE ? D_ : F_;
  static constexpr int A_BYTES = BM * 128;
  static constexpr int B_BYTES = 64 * 288;
  static constexpr int STAGE = A_BYTES + B_BYTES;
  static constexpr int A_IPW = BM / 64;
  static constexpr int S4_OFF = 3 * STAGE;
  static constexpr int SMEM = 3 * STAGE + (MODE ? 2 * CV_LDS : 0);
};
#define NPRE 2

template <int MODE, int NB>
DI void gemm_wave_body(char* smem, const char* __restrict__ Ag, const char* __restrict__ Bg, const char* __restrict__ lutg,
                       const u16* __restrict__ biasg,
                       int wid, int lane, int wm, int wk, int pb0  , int sblk  ,
                       f32x4 (&acc)[4][5]) {
  using C = GemmCfg<MODE>;
  const int i16 = lane & 15, kq = lane >> 4;
  const bool extraB = wid < 2;
  const u32 aoffl = (u32)lane * 16u;
  const int a_rd = (wm * 8) * 1024 + i16 * 64 + ((kq ^ blk_swz(i16)) * 16);
  const int b_rd = C::A_BYTES + lane * 16 + pb0 * 1024;
  const int b_rs = C::A_BYTES + lane * 16 + sblk * 1024;

  const u32 lds0 = __builtin_amdgcn_readfirstlane(lds_addr_of(smem));
  auto stage = [&](int kt, int buf) {
    const u32 sb = lds0 + buf * C::STAGE;
#pragma unroll
    for (int i = 0; i < C::A_IPW; ++i) {
      const int b = wid * C::A_IPW + i;
      glds16(Ag + ((size_t)(b >> 1) * C::NPA + 2 * kt + (b & 1)) * 1024, aoffl, sb + b * 1024);
    }
    const char* bk = Bg + (size_t)kt * 18432;
#pragma unroll
    for (int i = 0; i < 2; ++i) glds16(bk + (wid + 8 * i) * 1024, aoffl, sb + C::A_BYTES + (wid + 8 * i) * 1024);
    if (extraB) glds16(bk + (wid + 16) * 1024, aoffl, sb + C::A_BYTES + (wid + 16) * 1024);
  };
  auto readA = [&](s16x8 (&a)[4], int buf, int ks) {
    const char* sb = smem + buf * C::STAGE;
#pragma unroll
    for (int mi = 0; mi < 4; ++mi) a[mi] = *(const s16x8*)(sb + a_rd + (mi * 2 + ks) * 1024);
  };
  auto readB = [&](int nb, int buf, int ks) -> s16x8 {
    const char* sb = smem + buf * C::STAGE;
    return *(const s16x8*)(sb + (nb < 4 ? b_rd + nb * 1024 : b_rs) + ks * 9 * 1024);
  };
  s16x8 a0[4], a1[4], b[NB];
  auto mm = [&](s16x8 (&ac)[4], int nb) {
#pragma unroll
    for (int mi = 0; mi < 4; ++mi)
      asm volatile("v_mfma_f32_16x16x32_bf16 %0, %1, %2, %0" : "+v"(acc[mi][nb]) : "v"(b[nb]), "v"(ac[mi]));
  };
  auto sub_pre = [&](s16x8 (&ac)[4]) {
#pragma unroll
    for (int nb = 0; nb < NPRE; ++nb) mm(ac, nb);
    SCHED0();
  };
  auto sub_post = [&](s16x8 (&ac)[4], s16x8 (&an)[4], bool ld, int nbuf, int nks) {
    if (ld) {
      readA(an, nbuf, nks);
#pragma unroll
      for (int nb = 0; nb < NPRE; ++nb) b[nb] = readB(nb, nbuf, nks);
    }
    SCHED0();
#pragma unroll
    for (int nb = NPRE; nb < NB; ++nb) {
      mm(ac, nb);
      if (ld) b[nb] = readB(nb, nbuf, nks);
      SCHED0();
    }
  };
  constexpr int NW0 = C::A_IPW + 2;
#pragma unroll
  for (int mi = 0; mi < 4; ++mi)
#pragma unroll
    for (int nb = 0; nb < NB; ++nb) asm volatile("" : "+v"(acc[mi][nb]));
  if (MODE == 0) {
    raw_barrier();
    readA(a0, 1, 0);
#pragma unroll
    for (int nb = 0; nb < NB; ++nb) b[nb] = readB(nb, 1, 0);
#pragma unroll 1
    for (int o = 0; o < 3; ++o) {
#pragma unroll
      for (int u = 0; u < 3; ++u) {
        SCHED0();
        sub_pre(a0); sub_post(a0, a1, true, (u + 1) % 3, 1);
        sub_pre(a1);
        raw_barrier();
        sub_post(a1, a0, true, (u + 2) % 3, 0);
      }
    }
  } else {
    stage(0, 0); stage(1, 1);
    if (extraB) wait_vm<NW0 + 1>(); else wait_vm<NW0>();
    raw_barrier();
    readA(a0, 0, wk);
#pragma unroll
    for (int nb = 0; nb < NB; ++nb) b[nb] = readB(nb, 0, wk);
#pragma unroll 1
    for (int o = 0; o < 3; ++o) {
#pragma unroll
      for (int u = 0; u < 6; ++u) {
        const int j = 6 * o + u;
        if (j + 2 < 18) stage(j + 2, (u + 2) % 3);
        SCHED0();
        if (u & 1) sub_pre(a1); else sub_pre(a0);
        if (j + 1 < 18) {
          if (j + 2 < 18) { if (extraB) wait_vm<NW0 + 1>(); else wait_vm<NW0>(); }
          else wait_vm<0>();
          raw_barrier();
        }
        SCHED0();
        if (u & 1) sub_post(a1, a0, true, (u + 1) % 3, wk);
        else       sub_post(a0, a1, true, (u + 1) % 3, wk);
      }
    }
    raw_barrier();
  }
  asm volatile("s_nop 15\n\ts_nop 15" ::: "memory");
#pragma unroll
  for (int mi = 0; mi < 4; ++mi)
#pragma unroll
    for (int nb = 0; nb < NB; ++nb) asm volatile("" : "+v"(acc[mi][nb]));
}

__global__ __launch_bounds__(768) void gemm1_kernel(const u16* __restrict__ w1, const u16* __restrict__ bias, char* __restrict__ ws) {
  using C = GemmCfg<0>;
  __shared__ __attribute__((aligned(1024))) char smem[C::SMEM];
  const int tid = threadIdx.x, lane = tid & 63, wid = __builtin_amdgcn_readfirstlane(tid >> 6);
  const int bid = blockIdx.x;
  const int xcd = bid & 7, idx = bid >> 3;
  const int nt = 4 * xcd + (idx & 3), m0 = (idx >> 2) * 256;
  const int n0 = nt * 144;
  const char* Ag = ws + WS_XN + (size_t)(m0 >> 4) * C::NPA * 1024;
  const char* Bg = ws + WS_W1P + (size_t)nt * 9 * 18432;
  if (wid >= 8) { s4_side_gemm1(smem, bid, wid - 8, lane, Ag, Bg, bias + n0, ws); return; }
  __builtin_amdgcn_s_setprio(1);

  const int wn = wid >> 2;
  const int wm = wid & 3;
  const int odd = nt & 1;
  const int pb0 = odd + 4 * wn;
  const int sblk = odd ? 0 : 8;
  f32x4 acc[4][5];
#pragma unroll
  for (int mi = 0; mi < 4; ++mi)
#pragma unroll
    for (int nb = 0; nb < 5; ++nb) acc[mi][nb] = f32x4{0, 0, 0, 0};
  const int i16 = lane & 15, kq = lane >> 4;
  const char* lutg = ws + WS_LUT;
  STAMP_DECL STAMP_RT(); STAMP_NOW();
  if (wn == 0) gemm_wave_body<0, 5>(smem, Ag, Bg, lutg, bias + n0, wid, lane, wm, 0, pb0, sblk, acc);
  else         gemm_wave_body<0, 4>(smem, Ag, Bg, lutg, bias + n0, wid, lane, wm, 0, pb0, sblk, acc);
  STAMP_NOW();
  f32x2 bf_[5][2];
#pragma unroll
  for (int nb = 0; nb < 5; ++nb) {
    const int blk = nb < 4 ? pb0 + nb : sblk;
    const u32x2 bb = *(const u32x2*)(smem + BIAS_LDS + wid * 1024 + (blk * 16 + 4 * kq) * 2);
#pragma unroll
    for (int e = 0; e < 2; ++e) { bf_[nb][e] = f32x2{lo16(bb[e]), hi16(bb[e])}; asm volatile("" : "+v"(bf_[nb][e])); }
  }
  const char* lut = smem + (LUT_LDS - LUT_LO * 4);
  char* act = ws + WS_ACT;
  const int inblk = i16 * 64 + ((kq ^ blk_swz(i16)) * 16);
#pragma unroll
  for (int mi = 0; mi < 4; ++mi) {
    float hf[5][4], th[5][4];
#pragma unroll
    for (int nb = 0; nb < 5; ++nb) {
      if (nb < 4 || wn == 0) {
#pragma unroll
        for (int e = 0; e < 2; ++e) {
          u32 a = pack2(acc[mi][nb][2 * e], acc[mi][nb][2 * e + 1]);
          u32 h = pack2(lo16(a) + bf_[nb][e][0], hi16(a) + bf_[nb][e][1]);
          hf[nb][2 * e] = lo16(h); hf[nb][2 * e + 1] = hi16(h);
        }
#pragma unroll
        for (int j = 0; j < 4; ++j) {
          const u32 u = __float_as_uint(hf[nb][j]);
          const float m = __builtin_amdgcn_fmed3f(__builtin_fabsf(hf[nb][j]), 0.0009765625f, 128.0f);
          u32 o;
          asm("v_lshl_add_u32 %0, %1, 14, %2" : "=v"(o) : "v"(u >> 31), "v"(__float_as_uint(m) >> 14));
          th[nb][j] = *(const float*)(lut + o);
        }
      }
    }
    const size_t mbg = (size_t)((m0 >> 4) + wm * 4 + mi);
    u32 r[5][2];
#pragma unroll
    for (int nb = 0; nb < 5; ++nb)
      if (nb < 4 || wn == 0) {
#pragma unroll
        for (int e = 0; e < 2; ++e) r[nb][e] = pack2(hf[nb][2 * e] * th[nb][2 * e], hf[nb][2 * e + 1] * th[nb][2 * e + 1]);
      }
#pragma unroll
    for (int pr = 0; pr < 2; ++pr) {
      const int Pb = (n0 + (pb0 + 2 * pr) * 16) >> 5;
      st16_wt(act + (mbg * 144 + Pb) * 1024 + inblk, u32x4{r[2 * pr][0], r[2 * pr][1], r[2 * pr + 1][0], r[2 * pr + 1][1]});
    }
    if (wn == 0) {
      const int nb16 = (n0 >> 4) + sblk;
      st8_wt(act + (mbg * 144 + (nb16 >> 1)) * 1024 + inblk + (nb16 & 1) * 8, u32x2{r[4][0], r[4][1]});
    }
  }
  STAMP_NOW(); STAMP_VM(); STAMP_NOW(); STAMP_RT();
  STAMP_OUT(2, ws);
}

__global__ __launch_bounds__(768) void gemm2_kernel(const u16* __restrict__ w2, const u16* __restrict__ Dskip, char* __restrict__ ws) {
  using C = GemmCfg<1>;
  __shared__ __attribute__((aligned(1024))) char smem[C::SMEM];
  const int tid = threadIdx.x, lane = tid & 63, wid = __builtin_amdgcn_readfirstlane(tid >> 6);
  const int bid = blockIdx.x;
  if (wid >= 8) {
    STAMP_DECL STAMP_RT(); STAMP_NOW();
    __builtin_amdgcn_s_setprio(2);
    s4_side_gemm2(smem + C::S4_OFF, bid, wid - 8, lane, Dskip, ws);
    STAMP_NOW(); STAMP_NOW(); STAMP_NOW(); STAMP_RT();
    STAMP_OUT(3, ws);
    return;
  }
  __builtin_amdgcn_s_setprio(1);
  const int xcd = bid & 7, idx = bid >> 3;
  const int ksplit = xcd >> 1, mt = (xcd & 1) * 8 + (idx >> 2), nt = idx & 3;
  const int m0 = mt * 128, n0 = nt * 144;
  const char* Ag = ws + WS_ACT + ((size_t)(m0 >> 4) * C::NPA + ksplit * 36) * 1024;
  const char* Bg = ws + WS_W2P + ((size_t)nt * 72 + ksplit * 18) * 18432;

  const int wn = wid >> 2;
  const int wmv = wid & 3;
  const int wk = wmv >> 1, wm = wmv & 1;
  const int odd = nt & 1;
  const int pb0 = odd + 4 * wn;
  const int sblk = odd ? 0 : 8;
  f32x4 acc[4][5];
#pragma unroll
  for (int mi = 0; mi < 4; ++mi)
#pragma unroll
    for (int nb = 0; nb < 5; ++nb) acc[mi][nb] = f32x4{0, 0, 0, 0};
  STAMP_DECL STAMP_RT(); STAMP_NOW();
  if (wn == 0) gemm_wave_body<1, 5>(smem, Ag, Bg, nullptr, nullptr, wid, lane, wm, wk, pb0, sblk, acc);
  else         gemm_wave_body<1, 4>(smem, Ag, Bg, nullptr, nullptr, wid, lane, wm, wk, pb0, sblk, acc);
  STAMP_NOW();

  const int i16 = lane & 15, kq = lane >> 4;
  f32x4* red = (f32x4*)smem;
  const int slot = wn * 2 + wm;
  if (wk == 1) {
#pragma unroll
    for (int mi = 0; mi < 4; ++mi)
#pragma unroll
      for (int nb = 0; nb < 5; ++nb)
        if (nb < 4 || wn == 0) red[(slot * 20 + mi * 5 + nb) * 64 + lane] = acc[mi][nb];
  }
  raw_barrier();
  char* partb = ws + WS_PART;
  constexpr size_t PSTRIDE = (size_t)L_ * D_ * 4;
  if (wk == 0) {
#pragma unroll
    for (int mi = 0; mi < 4; ++mi)
#pragma unroll
      for (int nb = 0; nb < 5; ++nb)
        if (nb < 4 || wn == 0) {
          acc[mi][nb] += red[(slot * 20 + mi * 5 + nb) * 64 + lane];
          const size_t mbg = (size_t)((m0 >> 4) + wm * 4 + mi);
          const int nbg = (n0 >> 4) + (nb < 4 ? pb0 + nb : sblk);
          st16_wt(partb + ksplit * PSTRIDE + (mbg * 36 + nbg) * 1024 + i16 * 64 + kq * 16, __builtin_bit_cast(u32x4, acc[mi][nb]));
        }
  }
  STAMP_NOW(); STAMP_VM(); STAMP_NOW(); STAMP_RT();
  STAMP_OUT(3, ws);
}

__global__ __launch_bounds__(256) void final_kernel(const u16* __restrict__ x, const u16* __restrict__ Dskip,
                                                    const char* __restrict__ ws, u16* __restrict__ out) {
  __shared__ __attribute__((aligned(16))) u16 yt[64 * 72];
  const int tid = threadIdx.x;
  const int tt = blockIdx.x / 9, dt = blockIdx.x % 9;
  const int t0 = tt * 64, d0 = dt * 64;
  const u16* yat = (const u16*)(ws + WS_YAT);
  const char* part = ws + WS_PART;
  u32x4 yv[2], xv[2];
  f32x4 p[2][4][2];
#pragma unroll
  for (int i = 0; i < 2; ++i) {
    const int c = tid + 256 * i;
    const int r = c >> 3, ch = c & 7;
    if (dt < 8) yv[i] = *(const u32x4*)(yat + (size_t)(d0 + r) * L_ + t0 + ch * 8);
    else {
      const float* yp = (const float*)(ws + WS_YP) + (size_t)r * 4 * L_ + t0 + ch * 8;
      f32x4 a0 = (*(const f32x4*)(yp) + *(const f32x4*)(yp + L_)) + (*(const f32x4*)(yp + 2 * L_) + *(const f32x4*)(yp + 3 * L_));
      f32x4 a1 = (*(const f32x4*)(yp + 4) + *(const f32x4*)(yp + L_ + 4)) + (*(const f32x4*)(yp + 2 * L_ + 4) + *(const f32x4*)(yp + 3 * L_ + 4));
      const u32x4 uv = *(const u32x4*)((const u16*)(ws + WS_XNT) + (size_t)(d0 + r) * XT_PITCH + 128 + t0 + ch * 8);
      const float dsk = bf2f(Dskip[d0 + r]);
      float sv[8] = {a0[0], a0[1], a0[2], a0[3], a1[0], a1[1], a1[2], a1[3]};
#pragma unroll
      for (int jj = 0; jj < 4; ++jj)
        yv[i][jj] = pack2(rbf(sv[2 * jj]) + rbf(lo16(uv[jj]) * dsk), rbf(sv[2 * jj + 1]) + rbf(hi16(uv[jj]) * dsk));
    }
    xv[i] = *(const u32x4*)(x + (size_t)(t0 + r) * D_ + d0 + ch * 8);
    const int t = t0 + r, d = d0 + ch * 8;
    const size_t po = ((size_t)(t >> 4) * 36 + (d >> 4)) * 1024 + (t & 15) * 64 + ((d >> 2) & 3) * 16;
#pragma unroll
    for (int s = 0; s < 4; ++s) {
      p[i][s][0] = *(const f32x4*)(part + (size_t)s * L_ * D_ * 4 + po);
      p[i][s][1] = *(const f32x4*)(part + (size_t)s * L_ * D_ * 4 + po + 16);
    }
  }
#pragma unroll
  for (int i = 0; i < 2; ++i) {
    const int c = tid + 256 * i;
    *(u32x4*)(yt + (c >> 3) * 72 + (c & 7) * 8) = yv[i];
  }
  __syncthreads();
#pragma unroll
  for (int i = 0; i < 2; ++i) {
    const int c = tid + 256 * i;
    const int r = c >> 3, ch = c & 7;
    f32x4 s0 = (p[i][0][0] + p[i][1][0]) + (p[i][2][0] + p[i][3][0]);
    f32x4 s1 = (p[i][0][1] + p[i][1][1]) + (p[i][2][1] + p[i][3][1]);
    float s[8] = {s0[0], s0[1], s0[2], s0[3], s1[0], s1[1], s1[2], s1[3]};
    u32x4 ov;
#pragma unroll
    for (int j = 0; j < 4; ++j) {
      float r2[2];
#pragma unroll
      for (int h = 0; h < 2; ++h) {
        int e = 2 * j + h;
        float xx = h ? hi16(xv[i][j]) : lo16(xv[i][j]);
        float ya = bf2f(yt[(ch * 8 + e) * 72 + r]);
        r2[h] = rbf(xx + ya) + rbf(s[e]);
      }
      ov[j] = pack2(r2[0], r2[1]);
    }
    *(u32x4*)(out + (size_t)(t0 + r) * D_ + d0 + ch * 8) = ov;
  }
}

extern "C" void kernel_launch(void* const* d_in, const int* in_sizes, int n_in,
                              void* d_out, int out_size, void* d_ws, size_t ws_size,
                              hipStream_t stream) {
  const u16* x      = (const u16*)d_in[0];
  const u16* A_re   = (const u16*)d_in[1];
  const u16* A_im   = (const u16*)d_in[2];
  const u16* B_re   = (const u16*)d_in[3];
  const u16* B_im   = (const u16*)d_in[4];
  const u16* C_re   = (const u16*)d_in[5];
  const u16* C_im   = (const u16*)d_in[6];
  const u16* log_dt = (const u16*)d_in[7];
  const u16* Dskip  = (const u16*)d_in[8];
  const u16* w1     = (const u16*)d_in[10];
  const u16* b1     = (const u16*)d_in[11];
  const u16* w2     = (const u16*)d_in[12];
  char* ws = (char*)d_ws;
  S4Params P{A_re, A_im, B_re, B_im, C_re, C_im, log_dt};
  prep_kernel<<<PREP_BLOCKS, 256, 0, stream>>>(x, P, w1, w2, ws);
  gemm1_kernel<<<256, 768, 0, stream>>>(w1, b1, ws);
  gemm2_kernel<<<256, 768, 0, stream>>>(w2, Dskip, ws);
  final_kernel<<<288, 256, 0, stream>>>(x, Dskip, ws, (u16*)d_out);
}
```

```cpp
#include <hip/hip_runtime.h>
#include <hip/hip_bf16.h>
#include <stdint.h>

typedef unsigned short u16;
typedef unsigned int u32;
using s16x8 = __attribute__((ext_vector_type(8))) short;
using s16x4 = __attribute__((ext_vector_type(4))) short;
using f32x4 = __attribute__((ext_vector_type(4))) float;
using u32x4 = __attribute__((ext_vector_type(4))) unsigned int;
using u32x2 = __attribute__((ext_vector_type(2))) unsigned int;
typedef u32x4 __attribute__((aligned(4))) u32x4_a4;

#define DI __device__ __forceinline__
#define STAMP_DECL
#define STAMP_NOW()
#define STAMP_RT()
#define STAMP_VM()
#define STAMP_OUT(kid, ws)

constexpr int L_ = 2048, D_ = 576, N_ = 128, F_ = 4608;
constexpr int XT_PITCH = 2048 + 128;
constexpr int KX_NW = 1032;
constexpr int LUT_LO = 0x3A80, LUT_HI = 0x4300;
constexpr int LUT_ENTRIES = 8192;

constexpr size_t WS_CNT  = 0;
constexpr size_t WS_XN   = 1024;
constexpr size_t WS_XNT  = WS_XN + (size_t)L_ * D_ * 2;
constexpr size_t WS_KX   = WS_XNT + (size_t)D_ * XT_PITCH * 2;
constexpr size_t WS_LUT  = WS_KX + (size_t)D_ * 2 * KX_NW * 4;
constexpr size_t WS_YAT  = WS_LUT + LUT_ENTRIES * 4;
constexpr size_t WS_ACT  = WS_YAT + (size_t)D_ * L_ * 2;
constexpr size_t WS_PART = WS_ACT + (size_t)L_ * F_ * 2;
constexpr size_t WS_YP   = WS_PART + (size_t)4 * L_ * D_ * 4;
constexpr size_t WS_W1P  = WS_YP + (size_t)64 * 4 * L_ * 4;
constexpr size_t WS_W2P  = WS_W1P + (size_t)288 * 18432;

DI float bf2f(u16 b) { return __uint_as_float(((u32)b) << 16); }
DI u16 f2bf(float f) {
  __hip_bfloat16 h = __float2bfloat16(f);
  return *reinterpret_cast<u16*>(&h);
}
DI float rbf(float f) { return bf2f(f2bf(f)); }
typedef __bf16 bf16x2_t __attribute__((ext_vector_type(2)));
using f32x2 = __attribute__((ext_vector_type(2))) float;
DI u32 pack2(float lo, float hi) {
  f32x2 v = {lo, hi};
  return __builtin_bit_cast(u32, __builtin_convertvector(v, bf16x2_t));
}
DI float lo16(u32 v) { return __uint_as_float(v << 16); }
DI float hi16(u32 v) { return __uint_as_float(v & 0xffff0000u); }
DI void st16_wt(void* p, u32x4 v) { asm volatile("global_store_dwordx4 %0, %1, off sc1\n\ts_nop 1" ::"v"(p), "v"(v) : "memory"); }
DI void st8_wt(void* p, u32x2 v) { asm volatile("global_store_dwordx2 %0, %1, off sc1\n\ts_nop 1" ::"v"(p), "v"(v) : "memory"); }

DI int blk_swz(int i16) { return (-(i16 >> 2)) & 3; }

DI void mfma_ip(f32x4& acc, const s16x8& a, const s16x8& b) {
  asm volatile("s_nop 1\n\tv_mfma_f32_16x16x32_bf16 %0, %1, %2, %0" : "+v"(acc) : "v"(a), "v"(b));
}
DI void mfma_drain() { asm volatile("s_nop 15\n\ts_nop 15" ::: "memory"); }

DI void cexp_pos(float lr2, float lh, float pos, float& er, float& ei) {
  const float mag = __builtin_amdgcn_exp2f(lr2 * pos);
  const float f = __builtin_amdgcn_fractf(lh * pos);
  er = mag * __builtin_amdgcn_cosf(f);
  ei = mag * __builtin_amdgcn_sinf(f);
}
DI int kg_row_pos(int row) {
  if (row < 16) return 128 * (row >> 3) + (row & 7);
  if (row < 24) { int r = row - 16; return 256 + 128 * (r >> 2) + 2 * (r & 3); }
  if (row < 32) { int r = row - 24; return 512 + 128 * (r >> 1) + 4 * (r & 1); }
  if (row < 40) return 1024 + 128 * (row - 32);
  if (row == 40) return 2048;
  return 0;
}
struct KIdxTab { u32 v[1088]; };
constexpr int kg_bf16_round(int l) {
  int sft = l >= 1024 ? 3 : l >= 512 ? 2 : l >= 256 ? 1 : 0;
  if (sft == 0) return l;
  int half = 1 << (sft - 1), q = l >> sft;
  return ((l + half - 1 + (q & 1)) >> sft) << sft;
}
constexpr int kg_pos_index(int p) {
  int sg = p >= 2048 ? 4 : p >= 1024 ? 3 : p >= 512 ? 2 : p >= 256 ? 1 : 0;
  int base = sg ? (128 << sg) : 0, rowbase = sg ? 8 + 8 * sg : 0;
  int q = p - base;
  int row = rowbase + (q >> 7) * (8 >> sg) + ((q & 7) >> sg);
  return row * 16 + ((q & 127) >> 3);
}
constexpr KIdxTab make_kidx() {
  KIdxTab t{};
  for (int w = 0; w < 1088; ++w) {
    int l0 = 2047 - 2 * w, l1 = 2046 - 2 * w;
    u32 i0 = l0 >= 0 ? kg_pos_index(kg_bf16_round(l0)) : 768, i1 = l1 >= 0 ? kg_pos_index(kg_bf16_round(l1)) : 768;
    t.v[w] = (i0 * 4) | ((i1 * 4) << 16);
  }
  return t;
}
__device__ const KIdxTab kIdxTab = make_kidx();

struct S4Params { const u16 *A_re, *A_im, *B_re, *B_im, *C_re, *C_im, *log_dt; };
struct KgFrag { u32x4 Bh, Bl, Ah[3], Al[3]; };

DI void kg_load_params(const S4Params& P, int d, int n0, u32x4 (&v)[6]) {
  const size_t o = (size_t)d * N_ + n0;
  v[0] = *(const u32x4*)(P.A_re + o); v[1] = *(const u32x4*)(P.A_im + o);
  v[2] = *(const u32x4*)(P.B_re + o); v[3] = *(const u32x4*)(P.B_im + o);
  v[4] = *(const u32x4*)(P.C_re + o); v[5] = *(const u32x4*)(P.C_im + o);
}
DI void kg_mode(const u32x4 (&v)[6], int g, float dt, const float (&posA)[3], float posB, KgFrag& f) {
  const int e = g >> 1, gg = g & 3;
  const float lr = dt * ((g & 1) ? hi16(v[0][e]) : lo16(v[0][e])), li = dt * ((g & 1) ? hi16(v[1][e]) : lo16(v[1][e]));
  const float br = dt * ((g & 1) ? hi16(v[2][e]) : lo16(v[2][e])), bi = dt * ((g & 1) ? hi16(v[3][e]) : lo16(v[3][e]));
  const float cr = (g & 1) ? hi16(v[4][e]) : lo16(v[4][e]), ci = (g & 1) ? hi16(v[5][e]) : lo16(v[5][e]);
  const float kr = cr * br - ci * bi, ki = cr * bi + ci * br;
  const float lr2 = lr * 1.44269504088896340736f;
  const float lh = li * 0.15915494309189535f;
  float zr, zi;
  cexp_pos(lr2, lh, posB, zr, zi);
  {
    const float xr = kr * zr - ki * zi, xi = -(kr * zi + ki * zr);
    const u32 h = pack2(xr, xi);
    f.Bh[gg] = h; f.Bl[gg] = pack2(xr - lo16(h), xi - hi16(h));
  }
#pragma unroll
  for (int t = 0; t < 3; ++t) {
    float er, ei;
    cexp_pos(lr2, lh, posA[t], er, ei);
    const u32 h = pack2(er, ei);
    f.Ah[t][gg] = h; f.Al[t][gg] = pack2(er - lo16(h), ei - hi16(h));
  }
}
DI void kg_mma(const KgFrag& f, f32x4 (&acc)[3]) {
#pragma unroll
  for (int t = 0; t < 3; ++t) {
    mfma_ip(acc[t], __builtin_bit_cast(s16x8, f.Al[t]), __builtin_bit_cast(s16x8, f.Bh));
    mfma_ip(acc[t], __builtin_bit_cast(s16x8, f.Ah[t]), __builtin_bit_cast(s16x8, f.Bl));
    mfma_ip(acc[t], __builtin_bit_cast(s16x8, f.Ah[t]), __builtin_bit_cast(s16x8, f.Bh));
  }
}
template <int NTHR> DI void kg_gather_store(const float* kv, int d, int t, char* __restrict__ ws) {
  u32* kx = (u32*)(ws + WS_KX) + (size_t)d * 2 * KX_NW;
#pragma unroll
  for (int i = 0; i < 256 / NTHR; ++i) {
    const int w = (i * NTHR + t) * 4;
    const u32x4 ti = *(const u32x4*)(kIdxTab.v + w);
    u32x4 hi, lo;
#pragma unroll
    for (int j = 0; j < 4; ++j) {
      const float k0 = *(const float*)((const char*)kv + (ti[j] & 0xffffu)), k1 = *(const float*)((const char*)kv + (ti[j] >> 16));
      const u32 h = pack2(k0, k1);
      hi[j] = h; lo[j] = pack2(k0 - lo16(h), k1 - hi16(h));
    }
    *(u32x4*)(kx + w) = hi;
    *(u32x4*)(kx + KX_NW + w) = lo;
  }
  if (t < 8) {
    const u32 tt = kIdxTab.v[1024 + t];
    const float k0 = *(const float*)((const char*)kv + (tt & 0xffffu)), k1 = *(const float*)((const char*)kv + (tt >> 16));
    const u32 h = pack2(k0, k1);
    kx[1024 + t] = h; kx[KX_NW + 1024 + t] = pack2(k0 - lo16(h), k1 - hi16(h));
  }
}

typedef __attribute__((address_space(3))) s16x4 lds_s16x4;
template <int N> DI void wait_vm() { asm volatile("s_waitcnt vmcnt(%0)" ::"n"(N) : "memory"); }
DI void raw_barrier() { asm volatile("s_waitcnt lgkmcnt(0)" ::: "memory"); __builtin_amdgcn_s_barrier(); }
#define SCHED0() __builtin_amdgcn_sched_barrier(0)
DI void glds16(const void* sbase, u32 voff, u32 lds_addr) {
  u32 keep;
  asm volatile("s_mov_b32 %0, m0\n\ts_mov_b32 m0, %3\n\ts_nop 0\n\tglobal_load_lds_dwordx4 %1, %2\n\ts_mov_b32 m0, %0"
               : "=&s"(keep) : "v"(voff), "s"(sbase), "s"(lds_addr) : "memory");
}
DI u32 lds_addr_of(const char* p) { return (u32)(size_t)(const __attribute__((address_space(3))) char*)p; }

constexpr int LN_ROWS = 16;
constexpr int LN_BLOCKS = L_ / LN_ROWS;
constexpr int LUT_BLOCKS = 32;
constexpr int KG_BLOCKS = D_;
constexpr int PK_BLOCKS = 576;
constexpr int PREP_BLOCKS = KG_BLOCKS + LN_BLOCKS + LUT_BLOCKS + PK_BLOCKS;
constexpr int LN_PITCH = D_ + 8;

__global__ __launch_bounds__(256) void prep_kernel(const u16* __restrict__ x, S4Params P, const u16* __restrict__ w1,
                                                   const u16* __restrict__ w2, char* __restrict__ ws) {
  __shared__ __attribute__((aligned(1024))) char smem[LN_ROWS * LN_PITCH * 2];
  const int tid = threadIdx.x;
  const int vb = blockIdx.x < LN_BLOCKS ? blockIdx.x
               : blockIdx.x < LN_BLOCKS + PK_BLOCKS ? blockIdx.x + KG_BLOCKS + LUT_BLOCKS
               : blockIdx.x - PK_BLOCKS;
  if (vb >= KG_BLOCKS + LN_BLOCKS + LUT_BLOCKS) {
    const int tb = vb - (KG_BLOCKS + LN_BLOCKS + LUT_BLOCKS);
    const int lane = tid & 63, wid = __builtin_amdgcn_readfirstlane(tid >> 6);
    const bool is2 = tb >= 288;
    const int tt = is2 ? tb - 288 : tb;
    const int nt = is2 ? tt / 72 : tt / 9, kt = is2 ? tt % 72 : tt % 9;
    const int ldb = is2 ? D_ : F_;
    const char* src = (const char*)((is2 ? w2 : w1) + (size_t)kt * 64 * ldb + nt * 144);
    const u32 l0 = __builtin_amdgcn_readfirstlane(lds_addr_of(smem));
#pragma unroll
    for (int i = 0; i < 5; ++i) {
      const int ins = wid + 4 * i;
      if (ins < 18) {
        const int Lc = ins * 64 + lane, k = Lc / 18, c8 = Lc % 18;
        glds16(src, (u32)(k * ldb + c8 * 8) * 2u, l0 + ins * 1024);
      }
    }
    wait_vm<0>();
    raw_barrier();
    const int i16 = lane & 15, kq = lane >> 4;
    const int rd = (4 * kq + (i16 >> 2)) * 288 + (i16 & 3) * 8;
    char* dst = ws + (is2 ? WS_W2P : WS_W1P) + (size_t)tt * 18432 + lane * 16;
#pragma unroll
    for (int i = 0; i < 5; ++i) {
      const int fr = wid + 4 * i;
      if (fr < 18) {
        const int ks = fr / 9, nb = fr % 9;
        s16x4 lo = __builtin_amdgcn_ds_read_tr16_b64_v4i16((lds_s16x4*)(smem + rd + ks * 32 * 288 + nb * 32));
        s16x4 hi = __builtin_amdgcn_ds_read_tr16_b64_v4i16((lds_s16x4*)(smem + rd + ks * 32 * 288 + 16 * 288 + nb * 32));
        const s16x8 v = s16x8{lo[0], lo[1], lo[2], lo[3], hi[0], hi[1], hi[2], hi[3]};
        st16_wt(dst + fr * 1024, __builtin_bit_cast(u32x4, v));
      }
    }
    return;
  }
  if (vb >= LN_BLOCKS && vb < LN_BLOCKS + KG_BLOCKS) {
    const int d = vb - LN_BLOCKS, lane = tid & 63, wid = tid >> 6;
    const int i16 = lane & 15, kq = lane >> 4;
    float* red = (float*)smem;
    float* kv = red + 4 * 768;
    u32x4 v[6];
    kg_load_params(P, d, wid * 32 + kq * 8, v);
    const float dt = rbf(expf(bf2f(P.log_dt[d])));
    f32x4 acc[3] = {f32x4{0, 0, 0, 0}, f32x4{0, 0, 0, 0}, f32x4{0, 0, 0, 0}};
    float posA[3];
#pragma unroll
    for (int t = 0; t < 3; ++t) posA[t] = (float)kg_row_pos(16 * t + i16);
    const float posB = (float)(8 * i16);
    KgFrag f;
#pragma unroll
    for (int g = 0; g < 8; ++g) { kg_mode(v, g, dt, posA, posB, f); if ((g & 3) == 3) kg_mma(f, acc); }
    mfma_drain();
#pragma unroll
    for (int t = 0; t < 3; ++t)
#pragma unroll
      for (int j = 0; j < 4; ++j) red[wid * 768 + (16 * t + 4 * kq + j) * 16 + i16] = acc[t][j];
    __syncthreads();
#pragma unroll
    for (int i = 0; i < 3; ++i) { int e = tid + 256 * i; kv[e] = (red[e] + red[768 + e]) + (red[2 * 768 + e] + red[3 * 768 + e]); }
    if (tid == 0) kv[768] = 0.f;
    __syncthreads();
    kg_gather_store<256>(kv, d, tid, ws);
    return;
  }
  const int bid = vb < LN_BLOCKS ? vb : vb - KG_BLOCKS;
  if (bid < LN_BLOCKS) {
    u16* tile = (u16*)smem;
    const int t0 = bid * LN_ROWS;
    const int r = tid >> 4, j16 = tid & 15;
    u32x2 v[9];
#pragma unroll
    for (int i = 0; i < 9; ++i) v[i] = *(const u32x2*)(x + (size_t)(t0 + r) * D_ + (j16 + 16 * i) * 4);
    float s = 0.f;
#pragma unroll
    for (int i = 0; i < 9; ++i)
#pragma unroll
      for (int e = 0; e < 2; ++e) s += lo16(v[i][e]) + hi16(v[i][e]);
    s += __shfl_xor(s, 1); s += __shfl_xor(s, 2); s += __shfl_xor(s, 4); s += __shfl_xor(s, 8);
    const float mean = s * (1.0f / 576.0f);
    float q = 0.f;
#pragma unroll
    for (int i = 0; i < 9; ++i)
#pragma unroll
      for (int e = 0; e < 2; ++e) { float a = lo16(v[i][e]) - mean, b = hi16(v[i][e]) - mean; q += a * a + b * b; }
    q += __shfl_xor(q, 1); q += __shfl_xor(q, 2); q += __shfl_xor(q, 4); q += __shfl_xor(q, 8);
    const float var = q * (1.0f / 576.0f);
    const float mb = rbf(mean), vb = rbf(var);
    const float den = rbf(sqrtf(rbf(vb + 1e-5f)));
    const float rden = 1.0f / den;
    auto qdiv = [&](float a) { float qq = a * rden; return __builtin_fmaf(__builtin_fmaf(-qq, den, a), rden, qq); };
#pragma unroll
    for (int i = 0; i < 9; ++i) {
      u32x2 o;
#pragma unroll
      for (int e = 0; e < 2; ++e) {
        const u32 c = pack2(lo16(v[i][e]) - mb, hi16(v[i][e]) - mb);
        o[e] = pack2(qdiv(lo16(c)), qdiv(hi16(c)));
      }
      *(u32x2*)(tile + r * LN_PITCH + (j16 + 16 * i) * 4) = o;
    }
    __syncthreads();
    char* xn_g = ws + WS_XN + (size_t)bid * 18 * 1024;
#pragma unroll
    for (int i = 0; i < 5; ++i) {
      const int c = tid + 256 * i;
      if (c < 1152) {
        const int P = c >> 6, w = c & 63;
        const int i16 = w >> 2, kq = (w & 3) ^ blk_swz(w >> 2);
        const u16* src = tile + i16 * LN_PITCH + 32 * P + 4 * kq;
        u32x2 a = *(const u32x2*)src, b = *(const u32x2*)(src + 16);
        *(u32x4*)(xn_g + (size_t)c * 16) = u32x4{a[0], a[1], b[0], b[1]};
      }
    }
    u16* xt_g = (u16*)(ws + WS_XNT);
#pragma unroll
    for (int i = 0; i < 5; ++i) {
      const int c = tid + 256 * i;
      if (c < 1152) {
        const int d = c >> 1, tq = c & 1;
        u32x4 o;
#pragma unroll
        for (int j = 0; j < 4; ++j) {
          u32 lo = tile[(tq * 8 + 2 * j) * LN_PITCH + d], hi = tile[(tq * 8 + 2 * j + 1) * LN_PITCH + d];
          o[j] = lo | (hi << 16);
        }
        *(u32x4*)(xt_g + (size_t)d * XT_PITCH + 128 + t0 + tq * 8) = o;
      }
    }
    if (tid < 72) {
      int c = bid * 72 + tid;
      *(u32x4*)(xt_g + (size_t)(c >> 4) * XT_PITCH + (c & 15) * 8) = u32x4{0u, 0u, 0u, 0u};
    }
  } else {
    const int e8 = (bid - LN_BLOCKS) * 256 + tid;
    const int sign = e8 >> 12, e = e8 & 4095;
    float* lut = (float*)(ws + WS_LUT);
    int a = LUT_LO + e;
    if (a > LUT_HI) a = LUT_HI;
    float h = bf2f((u16)(a | (sign << 15)));
    float ex = rbf(expf(-fabsf(h)));
    float lp = rbf(log1pf(ex));
    float sp = rbf(lp + (h > 0.f ? h : 0.f));
    float th = tanhf(sp);
    lut[e8] = rbf(th);
  }
}

constexpr int BIAS_LDS = 51200;
constexpr int LUT_LDS = 61440;
constexpr int KG_LDS = 3200;
constexpr int CV_ST = 14848;
constexpr int CV_LDS = CV_ST + 8192;

DI void conv_stage_part(char* st, int d, int lane, int first, int step, const char* __restrict__ ws) {
  const char* ug = ws + WS_XNT + (size_t)d * XT_PITCH * 2;
  const char* kg = ws + WS_KX + (size_t)d * 2 * KX_NW * 4;
  const u32 l0 = __builtin_amdgcn_readfirstlane(lds_addr_of(st));
#pragma unroll
  for (int i = 0; i < 14; ++i) {
    if (i % step == first) {
      if (i < 5) glds16(ug + i * 1024, (u32)lane * 16u, l0 + i * 1024);
      else if (i < 13) glds16(kg + (i - 5) * 1024, (u32)lane * 16u, l0 + 5120 + (i - 5) * 1024);
      else glds16(kg + 8192, (u32)(lane & 3) * 16u, l0 + 5120 + 8192);
    }
  }
}
DI s16x8 conv_bfrag(const u32* kp, u32 sh) {
  const u32 e0 = kp[0], e1 = kp[1], e2 = kp[2], e3 = kp[3], e4 = kp[4];
  const u32x4 v = {__builtin_amdgcn_alignbyte(e1, e0, sh), __builtin_amdgcn_alignbyte(e2, e1, sh),
                   __builtin_amdgcn_alignbyte(e3, e2, sh), __builtin_amdgcn_alignbyte(e4, e3, sh)};
  return __builtin_bit_cast(s16x8, v);
}
DI void conv_hlsum(const f32x4& a, int hl, float& o0, float& o1) {
  const float s0 = hl ? a[0] : a[2], s1 = hl ? a[1] : a[3];
  const float k0 = hl ? a[2] : a[0], k1 = hl ? a[3] : a[1];
  o0 = k0 + __builtin_bit_cast(float, __builtin_amdgcn_update_dpp(0, __builtin_bit_cast(int, s0), 0x128, 0xf, 0xf, false));
  o1 = k1 + __builtin_bit_cast(float, __builtin_amdgcn_update_dpp(0, __builtin_bit_cast(int, s1), 0x128, 0xf, 0xf, false));
}

DI void s4_side_gemm1(char* smem, int bid, int j, int lane, const char* __restrict__ Ag, const char* __restrict__ Bg,
                      const u16* __restrict__ biasg, char* __restrict__ ws) {
  const int i16 = lane & 15, kq = lane >> 4;
  {
    constexpr int STAGE = 51200;
    const u32 lds0 = __builtin_amdgcn_readfirstlane(lds_addr_of(smem));
    const u32 lo = (u32)lane * 16u;
    auto stage = [&](int kt, int buf) {
      const u32 sb = lds0 + buf * STAGE;
#pragma unroll
      for (int i = 0; i < 8; ++i) {
        const int bb = j + 4 * i;
        glds16(Ag + ((size_t)(bb >> 1) * 18 + 2 * kt + (bb & 1)) * 1024, lo, sb + bb * 1024);
      }
      const char* bk = Bg + (size_t)kt * 18432;
#pragma unroll
      for (int i = 0; i < 4; ++i) glds16(bk + (j + 4 * i) * 1024, lo, sb + 32768 + (j + 4 * i) * 1024);
      if (j < 2) glds16(bk + (j + 16) * 1024, lo, sb + 32768 + (j + 16) * 1024);
    };
    stage(0, 1); stage(1, 2);
    if (j < 2) wait_vm<13>(); else wait_vm<12>();
    __builtin_amdgcn_s_barrier();
#pragma unroll 1
    for (int kt = 0; kt < 7; ++kt) {
      stage(kt + 2, kt % 3);
      if (j < 2) wait_vm<13>(); else wait_vm<12>();
      __builtin_amdgcn_s_barrier();
    }
#pragma unroll
    for (int i = 0; i < 8; ++i) glds16(ws + WS_LUT + (j * 8 + i) * 1024, lo, lds0 + LUT_LDS + (j * 8 + i) * 1024);
#pragma unroll
    for (int i = 0; i < 2; ++i) glds16(biasg, lo < 288u ? lo : 272u, lds0 + BIAS_LDS + (j * 2 + i) * 1024);
    wait_vm<10>();
    __builtin_amdgcn_s_barrier();
    wait_vm<0>();
    __builtin_amdgcn_s_barrier();
  }
  const int dx = 512 + (bid >> 2), g = bid & 3;
  char* st = smem + (j < 3 ? j * CV_ST : 102400);
  conv_stage_part(st, dx, lane, 0, 1, ws);
  wait_vm<0>();
  {
    const int rho = i16 & 7, hl = i16 >> 3;
    const char* us = st + 256;
    const u32 sh = ((1 - rho) & 1) * 2;
    const u32* ks = (const u32*)(st + 5120) + hl * KX_NW + ((2047 - 8 * (4 * g + kq) - rho) >> 1);
    f32x4 ya[4] = {f32x4{0, 0, 0, 0}, f32x4{0, 0, 0, 0}, f32x4{0, 0, 0, 0}, f32x4{0, 0, 0, 0}};
    const char* fb = us + (16 * j - 4 * g + i16 - kq) * 16;
#pragma unroll
    for (int q = 0; q < 16; ++q) {
      const s16x8 B = conv_bfrag(ks - 64 * q, sh);
#pragma unroll
      for (int r = 0; r < 4; ++r) {
        if (4 * r + 3 >= q) {
          if (j + 4 * r >= q) {
            const s16x8 F = *(const s16x8*)(fb + (4 * r - q) * 256);
            mfma_ip(ya[r], F, B);
          }
        }
      }
    }
    mfma_drain();
    float* yp = (float*)(ws + WS_YP) + ((size_t)(dx - 512) * 4 + g) * L_;
#pragma unroll
    for (int r = 0; r < 4; ++r) {
      float o0, o1;
      conv_hlsum(ya[r], hl, o0, o1);
      float* q = yp + 128 * (j + 4 * r) + 32 * kq + 16 * hl + rho;
      q[0] = o0; q[8] = o1;
    }
  }
}

DI void s4_side_gemm2(char* scratch, int bid, int j, int lane, const u16* __restrict__ Dskip, char* __restrict__ ws) {
  const int d = 2 * bid + (j >> 1), gh = j & 1;
  char* st = scratch + (j >> 1) * CV_LDS;
  conv_stage_part(st, d, lane, gh, 2, ws);
  const float dsk = bf2f(Dskip[d]);
  __builtin_amdgcn_s_barrier(); __builtin_amdgcn_s_barrier(); __builtin_amdgcn_s_barrier(); __builtin_amdgcn_s_barrier();
  wait_vm<0>();
  __builtin_amdgcn_s_barrier();
  const int i16 = lane & 15, kq = lane >> 4;
  const int rho = i16 & 7, hl = i16 >> 3;
  const char* us = st + 256;
  const u32 sh = ((1 - rho) & 1) * 2;
  f32x4 acc[16];
#pragma unroll
  for (int R = 0; R < 16; ++R) acc[R] = f32x4{0, 0, 0, 0};
#pragma unroll
  for (int gi = 0; gi < 2; ++gi) {
    const int g = 2 * gh + gi;
    s16x8 F[16];
#pragma unroll
    for (int dl = 0; dl < 16; ++dl) F[dl] = *(const s16x8*)(us + (16 * dl - 4 * g + i16 - kq) * 16);
    const u32* ks = (const u32*)(st + 5120) + hl * KX_NW + ((2047 - 8 * (4 * g + kq) - rho) >> 1);
#pragma unroll
    for (int q = 0; q < 16; ++q) {
      const s16x8 B = conv_bfrag(ks - 64 * q, sh);
#pragma unroll
      for (int R = q; R < 16; ++R) mfma_ip(acc[R], F[R - q], B);
      if (q == 0 || q == 2 || q == 3 || q == 5 || q == 7 || q == 10 || (q == 15 && gi == 0)) { SCHED0(); __builtin_amdgcn_s_barrier(); }
    }
  }
  mfma_drain();
  float* ybuf = (float*)(st + CV_ST);
  float o[16][2];
#pragma unroll
  for (int R = 0; R < 16; ++R) conv_hlsum(acc[R], hl, o[R][0], o[R][1]);
  float* yl = ybuf + 32 * kq + 16 * hl + rho;
  if (gh) {
#pragma unroll
    for (int R = 0; R < 8; ++R) { yl[128 * R] = o[R][0]; yl[128 * R + 8] = o[R][1]; }
  } else {
#pragma unroll
    for (int R = 8; R < 16; ++R) { yl[128 * R] = o[R][0]; yl[128 * R + 8] = o[R][1]; }
  }
  raw_barrier();
  __builtin_amdgcn_s_barrier();
  if (gh) {
#pragma unroll
    for (int R = 8; R < 16; ++R) { yl[128 * R] += o[R][0]; yl[128 * R + 8] += o[R][1]; }
  } else {
#pragma unroll
    for (int R = 0; R < 8; ++R) { yl[128 * R] = o[R][0] + yl[128 * R]; yl[128 * R + 8] = o[R][1] + yl[128 * R + 8]; }
  }
  asm volatile("s_waitcnt lgkmcnt(0)" ::: "memory");
#pragma unroll
  for (int i = 0; i < 2; ++i) {
    const int t = gh * 1024 + (i * 64 + lane) * 8;
    const s16x8 uv = *(const s16x8*)(us + t * 2);
    const f32x4 s0 = *(const f32x4*)(ybuf + t), s1 = *(const f32x4*)(ybuf + t + 4);
    float sv[8] = {s0[0], s0[1], s0[2], s0[3], s1[0], s1[1], s1[2], s1[3]};
    u32x4 ov;
#pragma unroll
    for (int jj = 0; jj < 4; ++jj) {
      float r0 = rbf(sv[2 * jj]) + rbf(bf2f((u16)uv[2 * jj]) * dsk);
      float r1 = rbf(sv[2 * jj + 1]) + rbf(bf2f((u16)uv[2 * jj + 1]) * dsk);
      ov[jj] = pack2(r0, r1);
    }
    *(u32x4*)(ws + WS_YAT + ((size_t)d * L_ + t) * 2) = ov;
  }
}

template <int MODE> struct GemmCfg {
  static constexpr int BM = MODE ? 128 : 256;
  static constexpr int NPA = MODE ? F_ / 32 : D_ / 32;
  static constexpr int LDB = MODE ? D_ : F_;
  static constexpr int A_BYTES = BM * 128;
  static constexpr int B_BYTES = 64 * 288;
  static constexpr int STAGE = A_BYTES + B_BYTES;
  static constexpr int A_IPW = BM / 64;
  static constexpr int S4_OFF = 3 * STAGE;
  static constexpr int SMEM = 3 * STAGE + (MODE ? 2 * CV_LDS : 0);
};
#define NPRE 2

template <int MODE, int NB>
DI void gemm_wave_body(char* smem, const char* __restrict__ Ag, const char* __restrict__ Bg, const char* __restrict__ lutg,
                       const u16* __restrict__ biasg,
                       int wid, int lane, int wm, int wk, int pb0  , int sblk  ,
                       f32x4 (&acc)[4][5]) {
  using C = GemmCfg<MODE>;
  const int i16 = lane & 15, kq = lane >> 4;
  const bool extraB = wid < 2;
  const u32 aoffl = (u32)lane * 16u;
  const int a_rd = (wm * 8) * 1024 + i16 * 64 + ((kq ^ blk_swz(i16)) * 16);
  const int b_rd = C::A_BYTES + lane * 16 + pb0 * 1024;
  const int b_rs = C::A_BYTES + lane * 16 + sblk * 1024;

  const u32 lds0 = __builtin_amdgcn_readfirstlane(lds_addr_of(smem));
  auto stage = [&](int kt, int buf) {
    const u32 sb = lds0 + buf * C::STAGE;
#pragma unroll
    for (int i = 0; i < C::A_IPW; ++i) {
      const int b = wid * C::A_IPW + i;
      glds16(Ag + ((size_t)(b >> 1) * C::NPA + 2 * kt + (b & 1)) * 1024, aoffl, sb + b * 1024);
    }
    const char* bk = Bg + (size_t)kt * 18432;
#pragma unroll
    for (int i = 0; i < 2; ++i) glds16(bk + (wid + 8 * i) * 1024, aoffl, sb + C::A_BYTES + (wid + 8 * i) * 1024);
    if (extraB) glds16(bk + (wid + 16) * 1024, aoffl, sb + C::A_BYTES + (wid + 16) * 1024);
  };
  auto readA = [&](s16x8 (&a)[4], int buf, int ks) {
    const char* sb = smem + buf * C::STAGE;
#pragma unroll
    for (int mi = 0; mi < 4; ++mi) a[mi] = *(const s16x8*)(sb + a_rd + (mi * 2 + ks) * 1024);
  };
  auto readB = [&](int nb, int buf, int ks) -> s16x8 {
    const char* sb = smem + buf * C::STAGE;
    return *(const s16x8*)(sb + (nb < 4 ? b_rd + nb * 1024 : b_rs) + ks * 9 * 1024);
  };
  s16x8 a0[4], a1[4], b[NB];
  auto mm = [&](s16x8 (&ac)[4], int nb) {
#pragma unroll
    for (int mi = 0; mi < 4; ++mi)
      asm volatile("v_mfma_f32_16x16x32_bf16 %0, %1, %2, %0" : "+v"(acc[mi][nb]) : "v"(b[nb]), "v"(ac[mi]));
  };
  auto sub_pre = [&](s16x8 (&ac)[4]) {
#pragma unroll
    for (int nb = 0; nb < NPRE; ++nb) mm(ac, nb);
    SCHED0();
  };
  auto sub_post = [&](s16x8 (&ac)[4], s16x8 (&an)[4], bool ld, int nbuf, int nks) {
    if (ld) {
      readA(an, nbuf, nks);
#pragma unroll
      for (int nb = 0; nb < NPRE; ++nb) b[nb] = readB(nb, nbuf, nks);
    }
    SCHED0();
#pragma unroll
    for (int nb = NPRE; nb < NB; ++nb) {
      mm(ac, nb);
      if (ld) b[nb] = readB(nb, nbuf, nks);
      SCHED0();
    }
  };
  constexpr int NW0 = C::A_IPW + 2;
#pragma unroll
  for (int mi = 0; mi < 4; ++mi)
#pragma unroll
    for (int nb = 0; nb < NB; ++nb) asm volatile("" : "+v"(acc[mi][nb]));
  if (MODE == 0) {
    raw_barrier();
    readA(a0, 1, 0);
#pragma unroll
    for (int nb = 0; nb < NB; ++nb) b[nb] = readB(nb, 1, 0);
#pragma unroll 1
    for (int o = 0; o < 3; ++o) {
#pragma unroll
      for (int u = 0; u < 3; ++u) {
        SCHED0();
        sub_pre(a0); sub_post(a0, a1, true, (u + 1) % 3, 1);
        sub_pre(a1);
        raw_barrier();
        sub_post(a1, a0, true, (u + 2) % 3, 0);
      }
    }
  } else {
    stage(0, 0); stage(1, 1);
    if (extraB) wait_vm<NW0 + 1>(); else wait_vm<NW0>();
    raw_barrier();
    readA(a0, 0, wk);
#pragma unroll
    for (int nb = 0; nb < NB; ++nb) b[nb] = readB(nb, 0, wk);
#pragma unroll 1
    for (int o = 0; o < 3; ++o) {
#pragma unroll
      for (int u = 0; u < 6; ++u) {
        const int j = 6 * o + u;
        if (j + 2 < 18) stage(j + 2, (u + 2) % 3);
        SCHED0();
        if (u & 1) sub_pre(a1); else sub_pre(a0);
        if (j + 1 < 18) {
          if (j + 2 < 18) { if (extraB) wait_vm<NW0 + 1>(); else wait_vm<NW0>(); }
          else wait_vm<0>();
          raw_barrier();
        }
        SCHED0();
        if (u & 1) sub_post(a1, a0, true, (u + 1) % 3, wk);
        else       sub_post(a0, a1, true, (u + 1) % 3, wk);
      }
    }
    raw_barrier();
  }
  asm volatile("s_nop 15\n\ts_nop 15" ::: "memory");
#pragma unroll
  for (int mi = 0; mi < 4; ++mi)
#pragma unroll
    for (int nb = 0; nb < NB; ++nb) asm volatile("" : "+v"(acc[mi][nb]));
}

__global__ __launch_bounds__(768) void gemm1_kernel(const u16* __restrict__ w1, const u16* __restrict__ bias, char* __restrict__ ws) {
  using C = GemmCfg<0>;
  __shared__ __attribute__((aligned(1024))) char smem[C::SMEM];
  const int tid = threadIdx.x, lane = tid & 63, wid = __builtin_amdgcn_readfirstlane(tid >> 6);
  const int bid = blockIdx.x;
  const int xcd = bid & 7, idx = bid >> 3;
  const int nt = 4 * xcd + (idx & 3), m0 = (idx >> 2) * 256;
  const int n0 = nt * 144;
  const char* Ag = ws + WS_XN + (size_t)(m0 >> 4) * C::NPA * 1024;
  const char* Bg = ws + WS_W1P + (size_t)nt * 9 * 18432;
  if (wid >= 8) { s4_side_gemm1(smem, bid, wid - 8, lane, Ag, Bg, bias + n0, ws); return; }
  __builtin_amdgcn_s_setprio(1);

  const int wn = wid >> 2;
  const int wm = wid & 3;
  const int odd = nt & 1;
  const int pb0 = odd + 4 * wn;
  const int sblk = odd ? 0 : 8;
  f32x4 acc[4][5];
#pragma unroll
  for (int mi = 0; mi < 4; ++mi)
#pragma unroll
    for (int nb = 0; nb < 5; ++nb) acc[mi][nb] = f32x4{0, 0, 0, 0};
  const int i16 = lane & 15, kq = lane >> 4;
  const char* lutg = ws + WS_LUT;
  STAMP_DECL STAMP_RT(); STAMP_NOW();
  if (wn == 0) gemm_wave_body<0, 5>(smem, Ag, Bg, lutg, bias + n0, wid, lane, wm, 0, pb0, sblk, acc);
  else         gemm_wave_body<0, 4>(smem, Ag, Bg, lutg, bias + n0, wid, lane, wm, 0, pb0, sblk, acc);
  STAMP_NOW();
  f32x2 bf_[5][2];
#pragma unroll
  for (int nb = 0; nb < 5; ++nb) {
    const int blk = nb < 4 ? pb0 + nb : sblk;
    const u32x2 bb = *(const u32x2*)(smem + BIAS_LDS + wid * 1024 + (blk * 16 + 4 * kq) * 2);
#pragma unroll
    for (int e = 0; e < 2; ++e) { bf_[nb][e] = f32x2{lo16(bb[e]), hi16(bb[e])}; asm volatile("" : "+v"(bf_[nb][e])); }
  }
  const char* lut = smem + (LUT_LDS - LUT_LO * 4);
  char* act = ws + WS_ACT;
  const int inblk = i16 * 64 + ((kq ^ blk_swz(i16)) * 16);
#pragma unroll
  for (int mi = 0; mi < 4; ++mi) {
    float hf[5][4], th[5][4];
#pragma unroll
    for (int nb = 0; nb < 5; ++nb) {
      if (nb < 4 || wn == 0) {
#pragma unroll
        for (int e = 0; e < 2; ++e) {
          u32 a = pack2(acc[mi][nb][2 * e], acc[mi][nb][2 * e + 1]);
          u32 h = pack2(lo16(a) + bf_[nb][e][0], hi16(a) + bf_[nb][e][1]);
          hf[nb][2 * e] = lo16(h); hf[nb][2 * e + 1] = hi16(h);
        }
#pragma unroll
        for (int j = 0; j < 4; ++j) {
          const u32 u = __float_as_uint(hf[nb][j]);
          const float m = __builtin_amdgcn_fmed3f(__builtin_fabsf(hf[nb][j]), 0.0009765625f, 128.0f);
          u32 o;
          asm("v_lshl_add_u32 %0, %1, 14, %2" : "=v"(o) : "v"(u >> 31), "v"(__float_as_uint(m) >> 14));
          th[nb][j] = *(const float*)(lut + o);
        }
      }
    }
    const size_t mbg = (size_t)((m0 >> 4) + wm * 4 + mi);
    u32 r[5][2];
#pragma unroll
    for (int nb = 0; nb < 5; ++nb)
      if (nb < 4 || wn == 0) {
#pragma unroll
        for (int e = 0; e < 2; ++e) r[nb][e] = pack2(hf[nb][2 * e] * th[nb][2 * e], hf[nb][2 * e + 1] * th[nb][2 * e + 1]);
      }
#pragma unroll
    for (int pr = 0; pr < 2; ++pr) {
      const int Pb = (n0 + (pb0 + 2 * pr) * 16) >> 5;
      st16_wt(act + (mbg * 144 + Pb) * 1024 + inblk, u32x4{r[2 * pr][0], r[2 * pr][1], r[2 * pr + 1][0], r[2 * pr + 1][1]});
    }
    if (wn == 0) {
      const int nb16 = (n0 >> 4) + sblk;
      st8_wt(act + (mbg * 144 + (nb16 >> 1)) * 1024 + inblk + (nb16 & 1) * 8, u32x2{r[4][0], r[4][1]});
    }
  }
  STAMP_NOW(); STAMP_VM(); STAMP_NOW(); STAMP_RT();
  STAMP_OUT(2, ws);
}

__global__ __launch_bounds__(768) void gemm2_kernel(const u16* __restrict__ w2, const u16* __restrict__ Dskip, char* __restrict__ ws) {
  using C = GemmCfg<1>;
  __shared__ __attribute__((aligned(1024))) char smem[C::SMEM];
  const int tid = threadIdx.x, lane = tid & 63, wid = __builtin_amdgcn_readfirstlane(tid >> 6);
  const int bid = blockIdx.x;
  if (wid >= 8) {
    STAMP_DECL STAMP_RT(); STAMP_NOW();
    __builtin_amdgcn_s_setprio(2);
    s4_side_gemm2(smem + C::S4_OFF, bid, wid - 8, lane, Dskip, ws);
    STAMP_NOW(); STAMP_NOW(); STAMP_NOW(); STAMP_RT();
    STAMP_OUT(3, ws);
    return;
  }
  __builtin_amdgcn_s_setprio(1);
  const int xcd = bid & 7, idx = bid >> 3;
  const int ksplit = xcd >> 1, mt = (xcd & 1) * 8 + (idx >> 2), nt = idx & 3;
  const int m0 = mt * 128, n0 = nt * 144;
  const char* Ag = ws + WS_ACT + ((size_t)(m0 >> 4) * C::NPA + ksplit * 36) * 1024;
  const char* Bg = ws + WS_W2P + ((size_t)nt * 72 + ksplit * 18) * 18432;

  const int wn = wid >> 2;
  const int wmv = wid & 3;
  const int wk = wmv >> 1, wm = wmv & 1;
  const int odd = nt & 1;
  const int pb0 = odd + 4 * wn;
  const int sblk = odd ? 0 : 8;
  f32x4 acc[4][5];
#pragma unroll
  for (int mi = 0; mi < 4; ++mi)
#pragma unroll
    for (int nb = 0; nb < 5; ++nb) acc[mi][nb] = f32x4{0, 0, 0, 0};
  STAMP_DECL STAMP_RT(); STAMP_NOW();
  if (wn == 0) gemm_wave_body<1, 5>(smem, Ag, Bg, nullptr, nullptr, wid, lane, wm, wk, pb0, sblk, acc);
  else         gemm_wave_body<1, 4>(smem, Ag, Bg, nullptr, nullptr, wid, lane, wm, wk, pb0, sblk, acc);
  STAMP_NOW();

  const int i16 = lane & 15, kq = lane >> 4;
  f32x4* red = (f32x4*)smem;
  const int slot = wn * 2 + wm;
  if (wk == 1) {
#pragma unroll
    for (int mi = 0; mi < 4; ++mi)
#pragma unroll
      for (int nb = 0; nb < 5; ++nb)
        if (nb < 4 || wn == 0) red[(slot * 20 + mi * 5 + nb) * 64 + lane] = acc[mi][nb];
  }
  raw_barrier();
  char* partb = ws + WS_PART;
  constexpr size_t PSTRIDE = (size_t)L_ * D_ * 4;
  if (wk == 0) {
#pragma unroll
    for (int mi = 0; mi < 4; ++mi)
#pragma unroll
      for (int nb = 0; nb < 5; ++nb)
        if (nb < 4 || wn == 0) {
          acc[mi][nb] += red[(slot * 20 + mi * 5 + nb) * 64 + lane];
          const size_t mbg = (size_t)((m0 >> 4) + wm * 4 + mi);
          const int nbg = (n0 >> 4) + (nb < 4 ? pb0 + nb : sblk);
          st16_wt(partb + ksplit * PSTRIDE + (mbg * 36 + nbg) * 1024 + i16 * 64 + kq * 16, __builtin_bit_cast(u32x4, acc[mi][nb]));
        }
  }
  STAMP_NOW(); STAMP_VM(); STAMP_NOW(); STAMP_RT();
  STAMP_OUT(3, ws);
}

__global__ __launch_bounds__(256) void final_kernel(const u16* __restrict__ x, const u16* __restrict__ Dskip,
                                                    const char* __restrict__ ws, u16* __restrict__ out) {
  __shared__ __attribute__((aligned(16))) u16 yt[64 * 72];
  const int tid = threadIdx.x;
  const int tt = blockIdx.x / 9, dt = blockIdx.x % 9;
  const int t0 = tt * 64, d0 = dt * 64;
  const u16* yat = (const u16*)(ws + WS_YAT);
  const char* part = ws + WS_PART;
  u32x4 yv[2], xv[2];
  f32x4 p[2][4][2];
#pragma unroll
  for (int i = 0; i < 2; ++i) {
    const int c = tid + 256 * i;
    const int r = c >> 3, ch = c & 7;
    if (dt < 8) yv[i] = *(const u32x4*)(yat + (size_t)(d0 + r) * L_ + t0 + ch * 8);
    else {
      const float* yp = (const float*)(ws + WS_YP) + (size_t)r * 4 * L_ + t0 + ch * 8;
      f32x4 a0 = (*(const f32x4*)(yp) + *(const f32x4*)(yp + L_)) + (*(const f32x4*)(yp + 2 * L_) + *(const f32x4*)(yp + 3 * L_));
      f32x4 a1 = (*(const f32x4*)(yp + 4) + *(const f32x4*)(yp + L_ + 4)) + (*(const f32x4*)(yp + 2 * L_ + 4) + *(const f32x4*)(yp + 3 * L_ + 4));
      const u32x4 uv = *(const u32x4*)((const u16*)(ws + WS_XNT) + (size_t)(d0 + r) * XT_PITCH + 128 + t0 + ch * 8);
      const float dsk = bf2f(Dskip[d0 + r]);
      float sv[8] = {a0[0], a0[1], a0[2], a0[3], a1[0], a1[1], a1[2], a1[3]};
#pragma unroll
      for (int jj = 0; jj < 4; ++jj)
        yv[i][jj] = pack2(rbf(sv[2 * jj]) + rbf(lo16(uv[jj]) * dsk), rbf(sv[2 * jj + 1]) + rbf(hi16(uv[jj]) * dsk));
    }
    xv[i] = *(const u32x4*)(x + (size_t)(t0 + r) * D_ + d0 + ch * 8);
    const int t = t0 + r, d = d0 + ch * 8;
    const size_t po = ((size_t)(t >> 4) * 36 + (d >> 4)) * 1024 + (t & 15) * 64 + ((d >> 2) & 3) * 16;
#pragma unroll
    for (int s = 0; s < 4; ++s) {
      p[i][s][0] = *(const f32x4*)(part + (size_t)s * L_ * D_ * 4 + po);
      p[i][s][1] = *(const f32x4*)(part + (size_t)s * L_ * D_ * 4 + po + 16);
    }
  }
#pragma unroll
  for (int i = 0; i < 2; ++i) {
    const int c = tid + 256 * i;
    *(u32x4*)(yt + (c >> 3) * 72 + (c & 7) * 8) = yv[i];
  }
  __syncthreads();
#pragma unroll
  for (int i = 0; i < 2; ++i) {
    const int c = tid + 256 * i;
    const int r = c >> 3, ch = c & 7;
    f32x4 s0 = (p[i][0][0] + p[i][1][0]) + (p[i][2][0] + p[i][3][0]);
    f32x4 s1 = (p[i][0][1] + p[i][1][1]) + (p[i][2][1] + p[i][3][1]);
    float s[8] = {s0[0], s0[1], s0[2], s0[3], s1[0], s1[1], s1[2], s1[3]};
    u32x4 ov;
#pragma unroll
    for (int j = 0; j < 4; ++j) {
      float r2[2];
#pragma unroll
      for (int h = 0; h < 2; ++h) {
        int e = 2 * j + h;
        float xx = h ? hi16(xv[i][j]) : lo16(xv[i][j]);
        float ya = bf2f(yt[(ch * 8 + e) * 72 + r]);
        r2[h] = rbf(xx + ya) + rbf(s[e]);
      }
      ov[j] = pack2(r2[0], r2[1]);
    }
    *(u32x4*)(out + (size_t)(t0 + r) * D_ + d0 + ch * 8) = ov;
  }
}

extern "C" void kernel_launch(void* const* d_in, const int* in_sizes, int n_in,
                              void* d_out, int out_size, void* d_ws, size_t ws_size,
                              hipStream_t stream) {
  const u16* x      = (const u16*)d_in[0];
  const u16* A_re   = (const u16*)d_in[1];
  const u16* A_im   = (const u16*)d_in[2];
  const u16* B_re   = (const u16*)d_in[3];
  const u16* B_im   = (const u16*)d_in[4];
  const u16* C_re   = (const u16*)d_in[5];
  const u16* C_im   = (const u16*)d_in[6];
  const u16* log_dt = (const u16*)d_in[7];
  const u16* Dskip  = (const u16*)d_in[8];
  const u16* w1     = (const u16*)d_in[10];
  const u16* b1     = (const u16*)d_in[11];
  const u16* w2     = (const u16*)d_in[12];
  char* ws = (char*)d_ws;
  S4Params P{A_re, A_im, B_re, B_im, C_re, C_im, log_dt};
  prep_kernel<<<PREP_BLOCKS, 256, 0, stream>>>(x, P, w1, w2, ws);
  gemm1_kernel<<<256, 768, 0, stream>>>(w1, b1, ws);
  gemm2_kernel<<<256, 768, 0, stream>>>(w2, Dskip, ws);
  final_kernel<<<288, 256, 0, stream>>>(x, Dskip, ws, (u16*)d_out);
}
```

```cpp
#include <hip/hip_runtime.h>
#include <hip/hip_bf16.h>
#include <stdint.h>

typedef unsigned short u16;
typedef unsigned int u32;
using s16x8 = __attribute__((ext_vector_type(8))) short;
using s16x4 = __attribute__((ext_vector_type(4))) short;
using f32x4 = __attribute__((ext_vector_type(4))) float;
using u32x4 = __attribute__((ext_vector_type(4))) unsigned int;
using u32x2 = __attribute__((ext_vector_type(2))) unsigned int;
typedef u32x4 __attribute__((aligned(4))) u32x4_a4;

#define DI __device__ __forceinline__
#define STAMP_DECL
#define STAMP_NOW()
#define STAMP_RT()
#define STAMP_VM()
#define STAMP_OUT(kid, ws)

constexpr int L_ = 2048, D_ = 576, N_ = 128, F_ = 4608;
constexpr int XT_PITCH = 2048 + 128;
constexpr int KX_NW = 1032;
constexpr int LUT_LO = 0x3A80, LUT_HI = 0x4300;
constexpr int LUT_ENTRIES = 8192;

constexpr size_t WS_CNT  = 0;
constexpr size_t WS_XN   = 1024;
constexpr size_t WS_XNT  = WS_XN + (size_t)L_ * D_ * 2;
constexpr size_t WS_KX   = WS_XNT + (size_t)D_ * XT_PITCH * 2;
constexpr size_t WS_LUT  = WS_KX + (size_t)D_ * 2 * KX_NW * 4;
constexpr size_t WS_YAT  = WS_LUT + LUT_ENTRIES * 4;
constexpr size_t WS_ACT  = WS_YAT + (size_t)D_ * L_ * 2;
constexpr size_t WS_PART = WS_ACT + (size_t)L_ * F_ * 2;
constexpr size_t WS_YP   = WS_PART + (size_t)4 * L_ * D_ * 4;
constexpr size_t WS_W1P  = WS_YP + (size_t)64 * 4 * L_ * 4;
constexpr size_t WS_W2P  = WS_W1P + (size_t)288 * 18432;

DI float bf2f(u16 b) { return __uint_as_float(((u32)b) << 16); }
DI u16 f2bf(float f) {
  __hip_bfloat16 h = __float2bfloat16(f);
  return *reinterpret_cast<u16*>(&h);
}
DI float rbf(float f) { return bf2f(f2bf(f)); }
typedef __bf16 bf16x2_t __attribute__((ext_vector_type(2)));
using f32x2 = __attribute__((ext_vector_type(2))) float;
DI u32 pack2(float lo, float hi) {
  f32x2 v = {lo, hi};
  return __builtin_bit_cast(u32, __builtin_convertvector(v, bf16x2_t));
}
DI float lo16(u32 v) { return __uint_as_float(v << 16); }
DI float hi16(u32 v) { return __uint_as_float(v & 0xffff0000u); }
DI void st16_wt(void* p, u32x4 v) { asm volatile("global_store_dwordx4 %0, %1, off sc0 sc1\n\ts_nop 1" ::"v"(p), "v"(v) : "memory"); }
DI void st8_wt(void* p, u32x2 v) { asm volatile("global_store_dwordx2 %0, %1, off sc1\n\ts_nop 1" ::"v"(p), "v"(v) : "memory"); }

DI int blk_swz(int i16) { return (-(i16 >> 2)) & 3; }

DI void mfma_ip(f32x4& acc, const s16x8& a, const s16x8& b) {
  asm volatile("s_nop 1\n\tv_mfma_f32_16x16x32_bf16 %0, %1, %2, %0" : "+v"(acc) : "v"(a), "v"(b));
}
DI void mfma_drain() { asm volatile("s_nop 15\n\ts_nop 15" ::: "memory"); }

DI void cexp_pos(float lr2, float lh, float pos, float& er, float& ei) {
  const float mag = __builtin_amdgcn_exp2f(lr2 * pos);
  const float f = __builtin_amdgcn_fractf(lh * pos);
  er = mag * __builtin_amdgcn_cosf(f);
  ei = mag * __builtin_amdgcn_sinf(f);
}
DI int kg_row_pos(int row) {
  if (row < 16) return 128 * (row >> 3) + (row & 7);
  if (row < 24) { int r = row - 16; return 256 + 128 * (r >> 2) + 2 * (r & 3); }
  if (row < 32) { int r = row - 24; return 512 + 128 * (r >> 1) + 4 * (r & 1); }
  if (row < 40) return 1024 + 128 * (row - 32);
  if (row == 40) return 2048;
  return 0;
}
struct KIdxTab { u32 v[1088]; };
constexpr int kg_bf16_round(int l) {
  int sft = l >= 1024 ? 3 : l >= 512 ? 2 : l >= 256 ? 1 : 0;
  if (sft == 0) return l;
  int half = 1 << (sft - 1), q = l >> sft;
  return ((l + half - 1 + (q & 1)) >> sft) << sft;
}
constexpr int kg_pos_index(int p) {
  int sg = p >= 2048 ? 4 : p >= 1024 ? 3 : p >= 512 ? 2 : p >= 256 ? 1 : 0;
  int base = sg ? (128 << sg) : 0, rowbase = sg ? 8 + 8 * sg : 0;
  int q = p - base;
  int row = rowbase + (q >> 7) * (8 >> sg) + ((q & 7) >> sg);
  return row * 16 + ((q & 127) >> 3);
}
constexpr KIdxTab make_kidx() {
  KIdxTab t{};
  for (int w = 0; w < 1088; ++w) {
    int l0 = 2047 - 2 * w, l1 = 2046 - 2 * w;
    u32 i0 = l0 >= 0 ? kg_pos_index(kg_bf16_round(l0)) : 768, i1 = l1 >= 0 ? kg_pos_index(kg_bf16_round(l1)) : 768;
    t.v[w] = (i0 * 4) | ((i1 * 4) << 16);
  }
  return t;
}
__device__ const KIdxTab kIdxTab = make_kidx();

struct S4Params { const u16 *A_re, *A_im, *B_re, *B_im, *C_re, *C_im, *log_dt; };
struct KgFrag { u32x4 Bh, Bl, Ah[3], Al[3]; };

DI void kg_load_params(const S4Params& P, int d, int n0, u32x4 (&v)[6]) {
  const size_t o = (size_t)d * N_ + n0;
  v[0] = *(const u32x4*)(P.A_re + o); v[1] = *(const u32x4*)(P.A_im + o);
  v[2] = *(const u32x4*)(P.B_re + o); v[3] = *(const u32x4*)(P.B_im + o);
  v[4] = *(const u32x4*)(P.C_re + o); v[5] = *(const u32x4*)(P.C_im + o);
}
DI void kg_mode(const u32x4 (&v)[6], int g, float dt, const float (&posA)[3], float posB, KgFrag& f) {
  const int e = g >> 1, gg = g & 3;
  const float lr = dt * ((g & 1) ? hi16(v[0][e]) : lo16(v[0][e])), li = dt * ((g & 1) ? hi16(v[1][e]) : lo16(v[1][e]));
  const float br = dt * ((g & 1) ? hi16(v[2][e]) : lo16(v[2][e])), bi = dt * ((g & 1) ? hi16(v[3][e]) : lo16(v[3][e]));
  const float cr = (g & 1) ? hi16(v[4][e]) : lo16(v[4][e]), ci = (g & 1) ? hi16(v[5][e]) : lo16(v[5][e]);
  const float kr = cr * br - ci * bi, ki = cr * bi + ci * br;
  const float lr2 = lr * 1.44269504088896340736f;
  const float lh = li * 0.15915494309189535f;
  float zr, zi;
  cexp_pos(lr2, lh, posB, zr, zi);
  {
    const float xr = kr * zr - ki * zi, xi = -(kr * zi + ki * zr);
    const u32 h = pack2(xr, xi);
    f.Bh[gg] = h; f.Bl[gg] = pack2(xr - lo16(h), xi - hi16(h));
  }
#pragma unroll
  for (int t = 0; t < 3; ++t) {
    float er, ei;
    cexp_pos(lr2, lh, posA[t], er, ei);
    const u32 h = pack2(er, ei);
    f.Ah[t][gg] = h; f.Al[t][gg] = pack2(er - lo16(h), ei - hi16(h));
  }
}
DI void kg_mma(const KgFrag& f, f32x4 (&acc)[3]) {
#pragma unroll
  for (int t = 0; t < 3; ++t) {
    mfma_ip(acc[t], __builtin_bit_cast(s16x8, f.Al[t]), __builtin_bit_cast(s16x8, f.Bh));
    mfma_ip(acc[t], __builtin_bit_cast(s16x8, f.Ah[t]), __builtin_bit_cast(s16x8, f.Bl));
    mfma_ip(acc[t], __builtin_bit_cast(s16x8, f.Ah[t]), __builtin_bit_cast(s16x8, f.Bh));
  }
}
template <int NTHR> DI void kg_gather_store(const float* kv, int d, int t, char* __restrict__ ws) {
  u32* kx = (u32*)(ws + WS_KX) + (size_t)d * 2 * KX_NW;
#pragma unroll
  for (int i = 0; i < 256 / NTHR; ++i) {
    const int w = (i * NTHR + t) * 4;
    const u32x4 ti = *(const u32x4*)(kIdxTab.v + w);
    u32x4 hi, lo;
#pragma unroll
    for (int j = 0; j < 4; ++j) {
      const float k0 = *(const float*)((const char*)kv + (ti[j] & 0xffffu)), k1 = *(const float*)((const char*)kv + (ti[j] >> 16));
      const u32 h = pack2(k0, k1);
      hi[j] = h; lo[j] = pack2(k0 - lo16(h), k1 - hi16(h));
    }
    *(u32x4*)(kx + w) = hi;
    *(u32x4*)(kx + KX_NW + w) = lo;
  }
  if (t < 8) {
    const u32 tt = kIdxTab.v[1024 + t];
    const float k0 = *(const float*)((const char*)kv + (tt & 0xffffu)), k1 = *(const float*)((const char*)kv + (tt >> 16));
    const u32 h = pack2(k0, k1);
    kx[1024 + t] = h; kx[KX_NW + 1024 + t] = pack2(k0 - lo16(h), k1 - hi16(h));
  }
}

typedef __attribute__((address_space(3))) s16x4 lds_s16x4;
template <int N> DI void wait_vm() { asm volatile("s_waitcnt vmcnt(%0)" ::"n"(N) : "memory"); }
DI void raw_barrier() { asm volatile("s_waitcnt lgkmcnt(0)" ::: "memory"); __builtin_amdgcn_s_barrier(); }
#define SCHED0() __builtin_amdgcn_sched_barrier(0)
DI void glds16(const void* sbase, u32 voff, u32 lds_addr) {
  u32 keep;
  asm volatile("s_mov_b32 %0, m0\n\ts_mov_b32 m0, %3\n\ts_nop 0\n\tglobal_load_lds_dwordx4 %1, %2\n\ts_mov_b32 m0, %0"
               : "=&s"(keep) : "v"(voff), "s"(sbase), "s"(lds_addr) : "memory");
}
DI u32 lds_addr_of(const char* p) { return (u32)(size_t)(const __attribute__((address_space(3))) char*)p; }

constexpr int LN_ROWS = 16;
constexpr int LN_BLOCKS = L_ / LN_ROWS;
constexpr int LUT_BLOCKS = 32;
constexpr int KG_BLOCKS = D_;
constexpr int PK_BLOCKS = 576;
constexpr int PREP_BLOCKS = KG_BLOCKS + LN_BLOCKS + LUT_BLOCKS + PK_BLOCKS;
constexpr int LN_PITCH = D_ + 8;

__global__ __launch_bounds__(256) void prep_kernel(const u16* __restrict__ x, S4Params P, const u16* __restrict__ w1,
                                                   const u16* __restrict__ w2, char* __restrict__ ws) {
  __shared__ __attribute__((aligned(1024))) char smem[LN_ROWS * LN_PITCH * 2];
  const int tid = threadIdx.x;
  if (blockIdx.x >= KG_BLOCKS + LN_BLOCKS + LUT_BLOCKS) {
    const int tb = blockIdx.x - (KG_BLOCKS + LN_BLOCKS + LUT_BLOCKS);
    const int lane = tid & 63, wid = __builtin_amdgcn_readfirstlane(tid >> 6);
    const bool is2 = tb >= 288;
    const int tt = is2 ? tb - 288 : tb;
    const int nt = is2 ? tt / 72 : tt / 9, kt = is2 ? tt % 72 : tt % 9;
    const int ldb = is2 ? D_ : F_;
    const char* src = (const char*)((is2 ? w2 : w1) + (size_t)kt * 64 * ldb + nt * 144);
    const u32 l0 = __builtin_amdgcn_readfirstlane(lds_addr_of(smem));
#pragma unroll
    for (int i = 0; i < 5; ++i) {
      const int ins = wid + 4 * i;
      if (ins < 18) {
        const int Lc = ins * 64 + lane, k = Lc / 18, c8 = Lc % 18;
        glds16(src, (u32)(k * ldb + c8 * 8) * 2u, l0 + ins * 1024);
      }
    }
    wait_vm<0>();
    raw_barrier();
    const int i16 = lane & 15, kq = lane >> 4;
    const int rd = (4 * kq + (i16 >> 2)) * 288 + (i16 & 3) * 8;
    char* dst = ws + (is2 ? WS_W2P : WS_W1P) + (size_t)tt * 18432 + lane * 16;
#pragma unroll
    for (int i = 0; i < 5; ++i) {
      const int fr = wid + 4 * i;
      if (fr < 18) {
        const int ks = fr / 9, nb = fr % 9;
        s16x4 lo = __builtin_amdgcn_ds_read_tr16_b64_v4i16((lds_s16x4*)(smem + rd + ks * 32 * 288 + nb * 32));
        s16x4 hi = __builtin_amdgcn_ds_read_tr16_b64_v4i16((lds_s16x4*)(smem + rd + ks * 32 * 288 + 16 * 288 + nb * 32));
        const s16x8 v = s16x8{lo[0], lo[1], lo[2], lo[3], hi[0], hi[1], hi[2], hi[3]};
        st16_wt(dst + fr * 1024, __builtin_bit_cast(u32x4, v));
      }
    }
    return;
  }
  if (blockIdx.x >= LN_BLOCKS && blockIdx.x < LN_BLOCKS + KG_BLOCKS) {
    const int d = blockIdx.x - LN_BLOCKS, lane = tid & 63, wid = tid >> 6;
    const int i16 = lane & 15, kq = lane >> 4;
    float* red = (float*)smem;
    float* kv = red + 4 * 768;
    u32x4 v[6];
    kg_load_params(P, d, wid * 32 + kq * 8, v);
    const float dt = rbf(expf(bf2f(P.log_dt[d])));
    f32x4 acc[3] = {f32x4{0, 0, 0, 0}, f32x4{0, 0, 0, 0}, f32x4{0, 0, 0, 0}};
    float posA[3];
#pragma unroll
    for (int t = 0; t < 3; ++t) posA[t] = (float)kg_row_pos(16 * t + i16);
    const float posB = (float)(8 * i16);
    KgFrag f;
#pragma unroll
    for (int g = 0; g < 8; ++g) { kg_mode(v, g, dt, posA, posB, f); if ((g & 3) == 3) kg_mma(f, acc); }
    mfma_drain();
#pragma unroll
    for (int t = 0; t < 3; ++t)
#pragma unroll
      for (int j = 0; j < 4; ++j) red[wid * 768 + (16 * t + 4 * kq + j) * 16 + i16] = acc[t][j];
    __syncthreads();
#pragma unroll
    for (int i = 0; i < 3; ++i) { int e = tid + 256 * i; kv[e] = (red[e] + red[768 + e]) + (red[2 * 768 + e] + red[3 * 768 + e]); }
    if (tid == 0) kv[768] = 0.f;
    __syncthreads();
    kg_gather_store<256>(kv, d, tid, ws);
    return;
  }
  const int bid = blockIdx.x < LN_BLOCKS ? blockIdx.x : blockIdx.x - KG_BLOCKS;
  if (bid < LN_BLOCKS) {
    u16* tile = (u16*)smem;
    const int t0 = bid * LN_ROWS;
    const int r = tid >> 4, j16 = tid & 15;
    u32x2 v[9];
#pragma unroll
    for (int i = 0; i < 9; ++i) v[i] = *(const u32x2*)(x + (size_t)(t0 + r) * D_ + (j16 + 16 * i) * 4);
    float s = 0.f;
#pragma unroll
    for (int i = 0; i < 9; ++i)
#pragma unroll
      for (int e = 0; e < 2; ++e) s += lo16(v[i][e]) + hi16(v[i][e]);
    s += __shfl_xor(s, 1); s += __shfl_xor(s, 2); s += __shfl_xor(s, 4); s += __shfl_xor(s, 8);
    const float mean = s * (1.0f / 576.0f);
    float q = 0.f;
#pragma unroll
    for (int i = 0; i < 9; ++i)
#pragma unroll
      for (int e = 0; e < 2; ++e) { float a = lo16(v[i][e]) - mean, b = hi16(v[i][e]) - mean; q += a * a + b * b; }
    q += __shfl_xor(q, 1); q += __shfl_xor(q, 2); q += __shfl_xor(q, 4); q += __shfl_xor(q, 8);
    const float var = q * (1.0f / 576.0f);
    const float mb = rbf(mean), vb = rbf(var);
    const float den = rbf(sqrtf(rbf(vb + 1e-5f)));
    const float rden = 1.0f / den;
    auto qdiv = [&](float a) { float qq = a * rden; return __builtin_fmaf(__builtin_fmaf(-qq, den, a), rden, qq); };
#pragma unroll
    for (int i = 0; i < 9; ++i) {
      u32x2 o;
#pragma unroll
      for (int e = 0; e < 2; ++e) {
        const u32 c = pack2(lo16(v[i][e]) - mb, hi16(v[i][e]) - mb);
        o[e] = pack2(qdiv(lo16(c)), qdiv(hi16(c)));
      }
      *(u32x2*)(tile + r * LN_PITCH + (j16 + 16 * i) * 4) = o;
    }
    __syncthreads();
    char* xn_g = ws + WS_XN + (size_t)bid * 18 * 1024;
#pragma unroll
    for (int i = 0; i < 5; ++i) {
      const int c = tid + 256 * i;
      if (c < 1152) {
        const int P = c >> 6, w = c & 63;
        const int i16 = w >> 2, kq = (w & 3) ^ blk_swz(w >> 2);
        const u16* src = tile + i16 * LN_PITCH + 32 * P + 4 * kq;
        u32x2 a = *(const u32x2*)src, b = *(const u32x2*)(src + 16);
        *(u32x4*)(xn_g + (size_t)c * 16) = u32x4{a[0], a[1], b[0], b[1]};
      }
    }
    u16* xt_g = (u16*)(ws + WS_XNT);
#pragma unroll
    for (int i = 0; i < 5; ++i) {
      const int c = tid + 256 * i;
      if (c < 1152) {
        const int d = c >> 1, tq = c & 1;
        u32x4 o;
#pragma unroll
        for (int j = 0; j < 4; ++j) {
          u32 lo = tile[(tq * 8 + 2 * j) * LN_PITCH + d], hi = tile[(tq * 8 + 2 * j + 1) * LN_PITCH + d];
          o[j] = lo | (hi << 16);
        }
        *(u32x4*)(xt_g + (size_t)d * XT_PITCH + 128 + t0 + tq * 8) = o;
      }
    }
    if (tid < 72) {
      int c = bid * 72 + tid;
      *(u32x4*)(xt_g + (size_t)(c >> 4) * XT_PITCH + (c & 15) * 8) = u32x4{0u, 0u, 0u, 0u};
    }
  } else {
    const int e8 = (bid - LN_BLOCKS) * 256 + tid;
    const int sign = e8 >> 12, e = e8 & 4095;
    float* lut = (float*)(ws + WS_LUT);
    int a = LUT_LO + e;
    if (a > LUT_HI) a = LUT_HI;
    float h = bf2f((u16)(a | (sign << 15)));
    float ex = rbf(expf(-fabsf(h)));
    float lp = rbf(log1pf(ex));
    float sp = rbf(lp + (h > 0.f ? h : 0.f));
    float th = tanhf(sp);
    lut[e8] = rbf(th);
  }
}

constexpr int BIAS_LDS = 51200;
constexpr int LUT_LDS = 61440;
constexpr int KG_LDS = 3200;
constexpr int CV_ST = 14848;
constexpr int CV_LDS = CV_ST + 8192;

DI void conv_stage_part(char* st, int d, int lane, int first, int step, const char* __restrict__ ws) {
  const char* ug = ws + WS_XNT + (size_t)d * XT_PITCH * 2;
  const char* kg = ws + WS_KX + (size_t)d * 2 * KX_NW * 4;
  const u32 l0 = __builtin_amdgcn_readfirstlane(lds_addr_of(st));
#pragma unroll
  for (int i = 0; i < 14; ++i) {
    if (i % step == first) {
      if (i < 5) glds16(ug + i * 1024, (u32)lane * 16u, l0 + i * 1024);
      else if (i < 13) glds16(kg + (i - 5) * 1024, (u32)lane * 16u, l0 + 5120 + (i - 5) * 1024);
      else glds16(kg + 8192, (u32)(lane & 3) * 16u, l0 + 5120 + 8192);
    }
  }
}
DI s16x8 conv_bfrag(const u32* kp, u32 sh) {
  const u32 e0 = kp[0], e1 = kp[1], e2 = kp[2], e3 = kp[3], e4 = kp[4];
  const u32x4 v = {__builtin_amdgcn_alignbyte(e1, e0, sh), __builtin_amdgcn_alignbyte(e2, e1, sh),
                   __builtin_amdgcn_alignbyte(e3, e2, sh), __builtin_amdgcn_alignbyte(e4, e3, sh)};
  return __builtin_bit_cast(s16x8, v);
}
DI void conv_hlsum(const f32x4& a, int hl, float& o0, float& o1) {
  const float s0 = hl ? a[0] : a[2], s1 = hl ? a[1] : a[3];
  const float k0 = hl ? a[2] : a[0], k1 = hl ? a[3] : a[1];
  o0 = k0 + __builtin_bit_cast(float, __builtin_amdgcn_update_dpp(0, __builtin_bit_cast(int, s0), 0x128, 0xf, 0xf, false));
  o1 = k1 + __builtin_bit_cast(float, __builtin_amdgcn_update_dpp(0, __builtin_bit_cast(int, s1), 0x128, 0xf, 0xf, false));
}

DI void s4_side_gemm1(char* smem, int bid, int j, int lane, const char* __restrict__ Ag, const char* __restrict__ Bg,
                      const u16* __restrict__ biasg, char* __restrict__ ws) {
  const int i16 = lane & 15, kq = lane >> 4;
  {
    constexpr int STAGE = 51200;
    const u32 lds0 = __builtin_amdgcn_readfirstlane(lds_addr_of(smem));
    const u32 lo = (u32)lane * 16u;
    auto stage = [&](int kt, int buf) {
      const u32 sb = lds0 + buf * STAGE;
#pragma unroll
      for (int i = 0; i < 8; ++i) {
        const int bb = j + 4 * i;
        glds16(Ag + ((size_t)(bb >> 1) * 18 + 2 * kt + (bb & 1)) * 1024, lo, sb + bb * 1024);
      }
      const char* bk = Bg + (size_t)kt * 18432;
#pragma unroll
      for (int i = 0; i < 4; ++i) glds16(bk + (j + 4 * i) * 1024, lo, sb + 32768 + (j + 4 * i) * 1024);
      if (j < 2) glds16(bk + (j + 16) * 1024, lo, sb + 32768 + (j + 16) * 1024);
    };
    stage(0, 1); stage(1, 2);
    if (j < 2) wait_vm<13>(); else wait_vm<12>();
    __builtin_amdgcn_s_barrier();
#pragma unroll 1
    for (int kt = 0; kt < 7; ++kt) {
      stage(kt + 2, kt % 3);
      if (j < 2) wait_vm<13>(); else wait_vm<12>();
      __builtin_amdgcn_s_barrier();
    }
#pragma unroll
    for (int i = 0; i < 8; ++i) glds16(ws + WS_LUT + (j * 8 + i) * 1024, lo, lds0 + LUT_LDS + (j * 8 + i) * 1024);
#pragma unroll
    for (int i = 0; i < 2; ++i) glds16(biasg, lo < 288u ? lo : 272u, lds0 + BIAS_LDS + (j * 2 + i) * 1024);
    wait_vm<10>();
    __builtin_amdgcn_s_barrier();
    wait_vm<0>();
    __builtin_amdgcn_s_barrier();
  }
  const int dx = 512 + (bid >> 2), g = bid & 3;
  char* st = smem + (j < 3 ? j * CV_ST : 102400);
  conv_stage_part(st, dx, lane, 0, 1, ws);
  wait_vm<0>();
  {
    const int rho = i16 & 7, hl = i16 >> 3;
    const char* us = st + 256;
    const u32 sh = ((1 - rho) & 1) * 2;
    const u32* ks = (const u32*)(st + 5120) + hl * KX_NW + ((2047 - 8 * (4 * g + kq) - rho) >> 1);
    f32x4 ya[4] = {f32x4{0, 0, 0, 0}, f32x4{0, 0, 0, 0}, f32x4{0, 0, 0, 0}, f32x4{0, 0, 0, 0}};
    const char* fb = us + (16 * j - 4 * g + i16 - kq) * 16;
#pragma unroll
    for (int q = 0; q < 16; ++q) {
      const s16x8 B = conv_bfrag(ks - 64 * q, sh);
#pragma unroll
      for (int r = 0; r < 4; ++r) {
        if (4 * r + 3 >= q) {
          if (j + 4 * r >= q) {
            const s16x8 F = *(const s16x8*)(fb + (4 * r - q) * 256);
            mfma_ip(ya[r], F, B);
          }
        }
      }
    }
    mfma_drain();
    float* yp = (float*)(ws + WS_YP) + ((size_t)(dx - 512) * 4 + g) * L_;
#pragma unroll
    for (int r = 0; r < 4; ++r) {
      float o0, o1;
      conv_hlsum(ya[r], hl, o0, o1);
      float* q = yp + 128 * (j + 4 * r) + 32 * kq + 16 * hl + rho;
      q[0] = o0; q[8] = o1;
    }
  }
}

DI void s4_side_gemm2(char* scratch, int bid, int j, int lane, const u16* __restrict__ Dskip, char* __restrict__ ws) {
  const int d = 2 * bid + (j >> 1), gh = j & 1;
  char* st = scratch + (j >> 1) * CV_LDS;
  conv_stage_part(st, d, lane, gh, 2, ws);
  const float dsk = bf2f(Dskip[d]);
  __builtin_amdgcn_s_barrier(); __builtin_amdgcn_s_barrier(); __builtin_amdgcn_s_barrier(); __builtin_amdgcn_s_barrier();
  wait_vm<0>();
  __builtin_amdgcn_s_barrier();
  const int i16 = lane & 15, kq = lane >> 4;
  const int rho = i16 & 7, hl = i16 >> 3;
  const char* us = st + 256;
  const u32 sh = ((1 - rho) & 1) * 2;
  f32x4 acc[16];
#pragma unroll
  for (int R = 0; R < 16; ++R) acc[R] = f32x4{0, 0, 0, 0};
#pragma unroll
  for (int gi = 0; gi < 2; ++gi) {
    const int g = 2 * gh + gi;
    s16x8 F[16];
#pragma unroll
    for (int dl = 0; dl < 16; ++dl) F[dl] = *(const s16x8*)(us + (16 * dl - 4 * g + i16 - kq) * 16);
    const u32* ks = (const u32*)(st + 5120) + hl * KX_NW + ((2047 - 8 * (4 * g + kq) - rho) >> 1);
#pragma unroll
    for (int q = 0; q < 16; ++q) {
      const s16x8 B = conv_bfrag(ks - 64 * q, sh);
#pragma unroll
      for (int R = q; R < 16; ++R) mfma_ip(acc[R], F[R - q], B);
      if (q == 0 || q == 2 || q == 3 || q == 5 || q == 7 || q == 10 || (q == 15 && gi == 0)) { SCHED0(); __builtin_amdgcn_s_barrier(); }
    }
  }
  mfma_drain();
  float* ybuf = (float*)(st + CV_ST);
  float o[16][2];
#pragma unroll
  for (int R = 0; R < 16; ++R) conv_hlsum(acc[R], hl, o[R][0], o[R][1]);
  float* yl = ybuf + 32 * kq + 16 * hl + rho;
  if (gh) {
#pragma unroll
    for (int R = 0; R < 8; ++R) { yl[128 * R] = o[R][0]; yl[128 * R + 8] = o[R][1]; }
  } else {
#pragma unroll
    for (int R = 8; R < 16; ++R) { yl[128 * R] = o[R][0]; yl[128 * R + 8] = o[R][1]; }
  }
  raw_barrier();
  __builtin_amdgcn_s_barrier();
  if (gh) {
#pragma unroll
    for (int R = 8; R < 16; ++R) { yl[128 * R] += o[R][0]; yl[128 * R + 8] += o[R][1]; }
  } else {
#pragma unroll
    for (int R = 0; R < 8; ++R) { yl[128 * R] = o[R][0] + yl[128 * R]; yl[128 * R + 8] = o[R][1] + yl[128 * R + 8]; }
  }
  asm volatile("s_waitcnt lgkmcnt(0)" ::: "memory");
#pragma unroll
  for (int i = 0; i < 2; ++i) {
    const int t = gh * 1024 + (i * 64 + lane) * 8;
    const s16x8 uv = *(const s16x8*)(us + t * 2);
    const f32x4 s0 = *(const f32x4*)(ybuf + t), s1 = *(const f32x4*)(ybuf + t + 4);
    float sv[8] = {s0[0], s0[1], s0[2], s0[3], s1[0], s1[1], s1[2], s1[3]};
    u32x4 ov;
#pragma unroll
    for (int jj = 0; jj < 4; ++jj) {
      float r0 = rbf(sv[2 * jj]) + rbf(bf2f((u16)uv[2 * jj]) * dsk);
      float r1 = rbf(sv[2 * jj + 1]) + rbf(bf2f((u16)uv[2 * jj + 1]) * dsk);
      ov[jj] = pack2(r0, r1);
    }
    *(u32x4*)(ws + WS_YAT + ((size_t)d * L_ + t) * 2) = ov;
  }
}

template <int MODE> struct GemmCfg {
  static constexpr int BM = MODE ? 128 : 256;
  static constexpr int NPA = MODE ? F_ / 32 : D_ / 32;
  static constexpr int LDB = MODE ? D_ : F_;
  static constexpr int A_BYTES = BM * 128;
  static constexpr int B_BYTES = 64 * 288;
  static constexpr int STAGE = A_BYTES + B_BYTES;
  static constexpr int A_IPW = BM / 64;
  static constexpr int S4_OFF = 3 * STAGE;
  static constexpr int SMEM = 3 * STAGE + (MODE ? 2 * CV_LDS : 0);
};
#define NPRE 2

template <int MODE, int NB>
DI void gemm_wave_body(char* smem, const char* __restrict__ Ag, const char* __restrict__ Bg, const char* __restrict__ lutg,
                       const u16* __restrict__ biasg,
                       int wid, int lane, int wm, int wk, int pb0  , int sblk  ,
                       f32x4 (&acc)[4][5]) {
  using C = GemmCfg<MODE>;
  const int i16 = lane & 15, kq = lane >> 4;
  const bool extraB = wid < 2;
  const u32 aoffl = (u32)lane * 16u;
  const int a_rd = (wm * 8) * 1024 + i16 * 64 + ((kq ^ blk_swz(i16)) * 16);
  const int b_rd = C::A_BYTES + lane * 16 + pb0 * 1024;
  const int b_rs = C::A_BYTES + lane * 16 + sblk * 1024;

  const u32 lds0 = __builtin_amdgcn_readfirstlane(lds_addr_of(smem));
  auto stage = [&](int kt, int buf) {
    const u32 sb = lds0 + buf * C::STAGE;
#pragma unroll
    for (int i = 0; i < C::A_IPW; ++i) {
      const int b = wid * C::A_IPW + i;
      glds16(Ag + ((size_t)(b >> 1) * C::NPA + 2 * kt + (b & 1)) * 1024, aoffl, sb + b * 1024);
    }
    const char* bk = Bg + (size_t)kt * 18432;
#pragma unroll
    for (int i = 0; i < 2; ++i) glds16(bk + (wid + 8 * i) * 1024, aoffl, sb + C::A_BYTES + (wid + 8 * i) * 1024);
    if (extraB) glds16(bk + (wid + 16) * 1024, aoffl, sb + C::A_BYTES + (wid + 16) * 1024);
  };
  auto readA = [&](s16x8 (&a)[4], int buf, int ks) {
    const char* sb = smem + buf * C::STAGE;
#pragma unroll
    for (int mi = 0; mi < 4; ++mi) a[mi] = *(const s16x8*)(sb + a_rd + (mi * 2 + ks) * 1024);
  };
  auto readB = [&](int nb, int buf, int ks) -> s16x8 {
    const char* sb = smem + buf * C::STAGE;
    return *(const s16x8*)(sb + (nb < 4 ? b_rd + nb * 1024 : b_rs) + ks * 9 * 1024);
  };
  s16x8 a0[4], a1[4], b[NB];
  auto mm = [&](s16x8 (&ac)[4], int nb) {
#pragma unroll
    for (int mi = 0; mi < 4; ++mi)
      asm volatile("v_mfma_f32_16x16x32_bf16 %0, %1, %2, %0" : "+v"(acc[mi][nb]) : "v"(b[nb]), "v"(ac[mi]));
  };
  auto sub_pre = [&](s16x8 (&ac)[4]) {
#pragma unroll
    for (int nb = 0; nb < NPRE; ++nb) mm(ac, nb);
    SCHED0();
  };
  auto sub_post = [&](s16x8 (&ac)[4], s16x8 (&an)[4], bool ld, int nbuf, int nks) {
    if (ld) {
      readA(an, nbuf, nks);
#pragma unroll
      for (int nb = 0; nb < NPRE; ++nb) b[nb] = readB(nb, nbuf, nks);
    }
    SCHED0();
#pragma unroll
    for (int nb = NPRE; nb < NB; ++nb) {
      mm(ac, nb);
      if (ld) b[nb] = readB(nb, nbuf, nks);
      SCHED0();
    }
  };
  constexpr int NW0 = C::A_IPW + 2;
#pragma unroll
  for (int mi = 0; mi < 4; ++mi)
#pragma unroll
    for (int nb = 0; nb < NB; ++nb) asm volatile("" : "+v"(acc[mi][nb]));
  if (MODE == 0) {
    raw_barrier();
    readA(a0, 1, 0);
#pragma unroll
    for (int nb = 0; nb < NB; ++nb) b[nb] = readB(nb, 1, 0);
#pragma unroll 1
    for (int o = 0; o < 3; ++o) {
#pragma unroll
      for (int u = 0; u < 3; ++u) {
        SCHED0();
        sub_pre(a0); sub_post(a0, a1, true, (u + 1) % 3, 1);
        sub_pre(a1);
        raw_barrier();
        sub_post(a1, a0, true, (u + 2) % 3, 0);
      }
    }
  } else {
    stage(0, 0); stage(1, 1);
    if (extraB) wait_vm<NW0 + 1>(); else wait_vm<NW0>();
    raw_barrier();
    readA(a0, 0, wk);
#pragma unroll
    for (int nb = 0; nb < NB; ++nb) b[nb] = readB(nb, 0, wk);
#pragma unroll 1
    for (int o = 0; o < 3; ++o) {
#pragma unroll
      for (int u = 0; u < 6; ++u) {
        const int j = 6 * o + u;
        if (j + 2 < 18) stage(j + 2, (u + 2) % 3);
        SCHED0();
        if (u & 1) sub_pre(a1); else sub_pre(a0);
        if (j + 1 < 18) {
          if (j + 2 < 18) { if (extraB) wait_vm<NW0 + 1>(); else wait_vm<NW0>(); }
          else wait_vm<0>();
          raw_barrier();
        }
        SCHED0();
        if (u & 1) sub_post(a1, a0, true, (u + 1) % 3, wk);
        else       sub_post(a0, a1, true, (u + 1) % 3, wk);
      }
    }
    raw_barrier();
  }
  asm volatile("s_nop 15\n\ts_nop 15" ::: "memory");
#pragma unroll
  for (int mi = 0; mi < 4; ++mi)
#pragma unroll
    for (int nb = 0; nb < NB; ++nb) asm volatile("" : "+v"(acc[mi][nb]));
}

__global__ __launch_bounds__(768) void gemm1_kernel(const u16* __restrict__ w1, const u16* __restrict__ bias, char* __restrict__ ws) {
  using C = GemmCfg<0>;
  __shared__ __attribute__((aligned(1024))) char smem[C::SMEM];
  const int tid = threadIdx.x, lane = tid & 63, wid = __builtin_amdgcn_readfirstlane(tid >> 6);
  const int bid = blockIdx.x;
  const int xcd = bid & 7, idx = bid >> 3;
  const int nt = 4 * xcd + (idx & 3), m0 = (idx >> 2) * 256;
  const int n0 = nt * 144;
  const char* Ag = ws + WS_XN + (size_t)(m0 >> 4) * C::NPA * 1024;
  const char* Bg = ws + WS_W1P + (size_t)nt * 9 * 18432;
  if (wid >= 8) { s4_side_gemm1(smem, bid, wid - 8, lane, Ag, Bg, bias + n0, ws); return; }
  __builtin_amdgcn_s_setprio(1);

  const int wn = wid >> 2;
  const int wm = wid & 3;
  const int odd = nt & 1;
  const int pb0 = odd + 4 * wn;
  const int sblk = odd ? 0 : 8;
  f32x4 acc[4][5];
#pragma unroll
  for (int mi = 0; mi < 4; ++mi)
#pragma unroll
    for (int nb = 0; nb < 5; ++nb) acc[mi][nb] = f32x4{0, 0, 0, 0};
  const int i16 = lane & 15, kq = lane >> 4;
  const char* lutg = ws + WS_LUT;
  STAMP_DECL STAMP_RT(); STAMP_NOW();
  if (wn == 0) gemm_wave_body<0, 5>(smem, Ag, Bg, lutg, bias + n0, wid, lane, wm, 0, pb0, sblk, acc);
  else         gemm_wave_body<0, 4>(smem, Ag, Bg, lutg, bias + n0, wid, lane, wm, 0, pb0, sblk, acc);
  STAMP_NOW();
  f32x2 bf_[5][2];
#pragma unroll
  for (int nb = 0; nb < 5; ++nb) {
    const int blk = nb < 4 ? pb0 + nb : sblk;
    const u32x2 bb = *(const u32x2*)(smem + BIAS_LDS + wid * 1024 + (blk * 16 + 4 * kq) * 2);
#pragma unroll
    for (int e = 0; e < 2; ++e) { bf_[nb][e] = f32x2{lo16(bb[e]), hi16(bb[e])}; asm volatile("" : "+v"(bf_[nb][e])); }
  }
  const char* lut = smem + (LUT_LDS - LUT_LO * 4);
  char* act = ws + WS_ACT;
  const int inblk = i16 * 64 + ((kq ^ blk_swz(i16)) * 16);
#pragma unroll
  for (int mi = 0; mi < 4; ++mi) {
    float hf[5][4], th[5][4];
#pragma unroll
    for (int nb = 0; nb < 5; ++nb) {
      if (nb < 4 || wn == 0) {
#pragma unroll
        for (int e = 0; e < 2; ++e) {
          u32 a = pack2(acc[mi][nb][2 * e], acc[mi][nb][2 * e + 1]);
          u32 h = pack2(lo16(a) + bf_[nb][e][0], hi16(a) + bf_[nb][e][1]);
          hf[nb][2 * e] = lo16(h); hf[nb][2 * e + 1] = hi16(h);
        }
#pragma unroll
        for (int j = 0; j < 4; ++j) {
          const u32 u = __float_as_uint(hf[nb][j]);
          const float m = __builtin_amdgcn_fmed3f(__builtin_fabsf(hf[nb][j]), 0.0009765625f, 128.0f);
          u32 o;
          asm("v_lshl_add_u32 %0, %1, 14, %2" : "=v"(o) : "v"(u >> 31), "v"(__float_as_uint(m) >> 14));
          th[nb][j] = *(const float*)(lut + o);
        }
      }
    }
    const size_t mbg = (size_t)((m0 >> 4) + wm * 4 + mi);
    u32 r[5][2];
#pragma unroll
    for (int nb = 0; nb < 5; ++nb)
      if (nb < 4 || wn == 0) {
#pragma unroll
        for (int e = 0; e < 2; ++e) r[nb][e] = pack2(hf[nb][2 * e] * th[nb][2 * e], hf[nb][2 * e + 1] * th[nb][2 * e + 1]);
      }
#pragma unroll
    for (int pr = 0; pr < 2; ++pr) {
      const int Pb = (n0 + (pb0 + 2 * pr) * 16) >> 5;
      st16_wt(act + (mbg * 144 + Pb) * 1024 + inblk, u32x4{r[2 * pr][0], r[2 * pr][1], r[2 * pr + 1][0], r[2 * pr + 1][1]});
    }
    if (wn == 0) {
      const int nb16 = (n0 >> 4) + sblk;
      st8_wt(act + (mbg * 144 + (nb16 >> 1)) * 1024 + inblk + (nb16 & 1) * 8, u32x2{r[4][0], r[4][1]});
    }
  }
  STAMP_NOW(); STAMP_VM(); STAMP_NOW(); STAMP_RT();
  STAMP_OUT(2, ws);
}

__global__ __launch_bounds__(768) void gemm2_kernel(const u16* __restrict__ w2, const u16* __restrict__ Dskip, char* __restrict__ ws) {
  using C = GemmCfg<1>;
  __shared__ __attribute__((aligned(1024))) char smem[C::SMEM];
  const int tid = threadIdx.x, lane = tid & 63, wid = __builtin_amdgcn_readfirstlane(tid >> 6);
  const int bid = blockIdx.x;
  if (wid >= 8) {
    STAMP_DECL STAMP_RT(); STAMP_NOW();
    __builtin_amdgcn_s_setprio(2);
    s4_side_gemm2(smem + C::S4_OFF, bid, wid - 8, lane, Dskip, ws);
    STAMP_NOW(); STAMP_NOW(); STAMP_NOW(); STAMP_RT();
    STAMP_OUT(3, ws);
    return;
  }
  __builtin_amdgcn_s_setprio(1);
  const int xcd = bid & 7, idx = bid >> 3;
  const int ksplit = xcd >> 1, mt = (xcd & 1) * 8 + (idx >> 2), nt = idx & 3;
  const int m0 = mt * 128, n0 = nt * 144;
  const char* Ag = ws + WS_ACT + ((size_t)(m0 >> 4) * C::NPA + ksplit * 36) * 1024;
  const char* Bg = ws + WS_W2P + ((size_t)nt * 72 + ksplit * 18) * 18432;

  const int wn = wid >> 2;
  const int wmv = wid & 3;
  const int wk = wmv >> 1, wm = wmv & 1;
  const int odd = nt & 1;
  const int pb0 = odd + 4 * wn;
  const int sblk = odd ? 0 : 8;
  f32x4 acc[4][5];
#pragma unroll
  for (int mi = 0; mi < 4; ++mi)
#pragma unroll
    for (int nb = 0; nb < 5; ++nb) acc[mi][nb] = f32x4{0, 0, 0, 0};
  STAMP_DECL STAMP_RT(); STAMP_NOW();
  if (wn == 0) gemm_wave_body<1, 5>(smem, Ag, Bg, nullptr, nullptr, wid, lane, wm, wk, pb0, sblk, acc);
  else         gemm_wave_body<1, 4>(smem, Ag, Bg, nullptr, nullptr, wid, lane, wm, wk, pb0, sblk, acc);
  STAMP_NOW();

  const int i16 = lane & 15, kq = lane >> 4;
  f32x4* red = (f32x4*)smem;
  const int slot = wn * 2 + wm;
  if (wk == 1) {
#pragma unroll
    for (int mi = 0; mi < 4; ++mi)
#pragma unroll
      for (int nb = 0; nb < 5; ++nb)
        if (nb < 4 || wn == 0) red[(slot * 20 + mi * 5 + nb) * 64 + lane] = acc[mi][nb];
  }
  raw_barrier();
  char* partb = ws + WS_PART;
  constexpr size_t PSTRIDE = (size_t)L_ * D_ * 4;
  if (wk == 0) {
#pragma unroll
    for (int mi = 0; mi < 4; ++mi)
#pragma unroll
      for (int nb = 0; nb < 5; ++nb)
        if (nb < 4 || wn == 0) {
          acc[mi][nb] += red[(slot * 20 + mi * 5 + nb) * 64 + lane];
          const size_t mbg = (size_t)((m0 >> 4) + wm * 4 + mi);
          const int nbg = (n0 >> 4) + (nb < 4 ? pb0 + nb : sblk);
          st16_wt(partb + ksplit * PSTRIDE + (mbg * 36 + nbg) * 1024 + i16 * 64 + kq * 16, __builtin_bit_cast(u32x4, acc[mi][nb]));
        }
  }
  STAMP_NOW(); STAMP_VM(); STAMP_NOW(); STAMP_RT();
  STAMP_OUT(3, ws);
}

__global__ __launch_bounds__(256) void final_kernel(const u16* __restrict__ x, const u16* __restrict__ Dskip,
                                                    const char* __restrict__ ws, u16* __restrict__ out) {
  __shared__ __attribute__((aligned(16))) u16 yt[64 * 72];
  const int tid = threadIdx.x;
  const int tt = blockIdx.x / 9, dt = blockIdx.x % 9;
  const int t0 = tt * 64, d0 = dt * 64;
  const u16* yat = (const u16*)(ws + WS_YAT);
  const char* part = ws + WS_PART;
  u32x4 yv[2], xv[2];
  f32x4 p[2][4][2];
#pragma unroll
  for (int i = 0; i < 2; ++i) {
    const int c = tid + 256 * i;
    const int r = c >> 3, ch = c & 7;
    if (dt < 8) yv[i] = *(const u32x4*)(yat + (size_t)(d0 + r) * L_ + t0 + ch * 8);
    else {
      const float* yp = (const float*)(ws + WS_YP) + (size_t)r * 4 * L_ + t0 + ch * 8;
      f32x4 a0 = (*(const f32x4*)(yp) + *(const f32x4*)(yp + L_)) + (*(const f32x4*)(yp + 2 * L_) + *(const f32x4*)(yp + 3 * L_));
      f32x4 a1 = (*(const f32x4*)(yp + 4) + *(const f32x4*)(yp + L_ + 4)) + (*(const f32x4*)(yp + 2 * L_ + 4) + *(const f32x4*)(yp + 3 * L_ + 4));
      const u32x4 uv = *(const u32x4*)((const u16*)(ws + WS_XNT) + (size_t)(d0 + r) * XT_PITCH + 128 + t0 + ch * 8);
      const float dsk = bf2f(Dskip[d0 + r]);
      float sv[8] = {a0[0], a0[1], a0[2], a0[3], a1[0], a1[1], a1[2], a1[3]};
#pragma unroll
      for (int jj = 0; jj < 4; ++jj)
        yv[i][jj] = pack2(rbf(sv[2 * jj]) + rbf(lo16(uv[jj]) * dsk), rbf(sv[2 * jj + 1]) + rbf(hi16(uv[jj]) * dsk));
    }
    xv[i] = *(const u32x4*)(x + (size_t)(t0 + r) * D_ + d0 + ch * 8);
    const int t = t0 + r, d = d0 + ch * 8;
    const size_t po = ((size_t)(t >> 4) * 36 + (d >> 4)) * 1024 + (t & 15) * 64 + ((d >> 2) & 3) * 16;
#pragma unroll
    for (int s = 0; s < 4; ++s) {
      p[i][s][0] = *(const f32x4*)(part + (size_t)s * L_ * D_ * 4 + po);
      p[i][s][1] = *(const f32x4*)(part + (size_t)s * L_ * D_ * 4 + po + 16);
    }
  }
#pragma unroll
  for (int i = 0; i < 2; ++i) {
    const int c = tid + 256 * i;
    *(u32x4*)(yt + (c >> 3) * 72 + (c & 7) * 8) = yv[i];
  }
  __syncthreads();
#pragma unroll
  for (int i = 0; i < 2; ++i) {
    const int c = tid + 256 * i;
    const int r = c >> 3, ch = c & 7;
    f32x4 s0 = (p[i][0][0] + p[i][1][0]) + (p[i][2][0] + p[i][3][0]);
    f32x4 s1 = (p[i][0][1] + p[i][1][1]) + (p[i][2][1] + p[i][3][1]);
    float s[8] = {s0[0], s0[1], s0[2], s0[3], s1[0], s1[1], s1[2], s1[3]};
    u32x4 ov;
#pragma unroll
    for (int j = 0; j < 4; ++j) {
      float r2[2];
#pragma unroll
      for (int h = 0; h < 2; ++h) {
        int e = 2 * j + h;
        float xx = h ? hi16(xv[i][j]) : lo16(xv[i][j]);
        float ya = bf2f(yt[(ch * 8 + e) * 72 + r]);
        r2[h] = rbf(xx + ya) + rbf(s[e]);
      }
      ov[j] = pack2(r2[0], r2[1]);
    }
    *(u32x4*)(out + (size_t)(t0 + r) * D_ + d0 + ch * 8) = ov;
  }
}

extern "C" void kernel_launch(void* const* d_in, const int* in_sizes, int n_in,
                              void* d_out, int out_size, void* d_ws, size_t ws_size,
                              hipStream_t stream) {
  const u16* x      = (const u16*)d_in[0];
  const u16* A_re   = (const u16*)d_in[1];
  const u16* A_im   = (const u16*)d_in[2];
  const u16* B_re   = (const u16*)d_in[3];
  const u16* B_im   = (const u16*)d_in[4];
  const u16* C_re   = (const u16*)d_in[5];
  const u16* C_im   = (const u16*)d_in[6];
  const u16* log_dt = (const u16*)d_in[7];
  const u16* Dskip  = (const u16*)d_in[8];
  const u16* w1     = (const u16*)d_in[10];
  const u16* b1     = (const u16*)d_in[11];
  const u16* w2     = (const u16*)d_in[12];
  char* ws = (char*)d_ws;
  S4Params P{A_re, A_im, B_re, B_im, C_re, C_im, log_dt};
  prep_kernel<<<PREP_BLOCKS, 256, 0, stream>>>(x, P, w1, w2, ws);
  gemm1_kernel<<<256, 768, 0, stream>>>(w1, b1, ws);
  gemm2_kernel<<<256, 768, 0, stream>>>(w2, Dskip, ws);
  final_kernel<<<288, 256, 0, stream>>>(x, Dskip, ws, (u16*)d_out);
}
```

```cpp
#include <hip/hip_runtime.h>
#include <hip/hip_bf16.h>
#include <stdint.h>

typedef unsigned short u16;
typedef unsigned int u32;
using s16x8 = __attribute__((ext_vector_type(8))) short;
using s16x4 = __attribute__((ext_vector_type(4))) short;
using f32x4 = __attribute__((ext_vector_type(4))) float;
using u32x4 = __attribute__((ext_vector_type(4))) unsigned int;
using u32x2 = __attribute__((ext_vector_type(2))) unsigned int;
typedef u32x4 __attribute__((aligned(4))) u32x4_a4;

#define DI __device__ __forceinline__
#define STAMP_DECL
#define STAMP_NOW()
#define STAMP_RT()
#define STAMP_VM()
#define STAMP_OUT(kid, ws)

constexpr int L_ = 2048, D_ = 576, N_ = 128, F_ = 4608;
constexpr int XT_PITCH = 2048 + 128;
constexpr int KX_NW = 1032;
constexpr int LUT_LO = 0x3A80, LUT_HI = 0x4300;
constexpr int LUT_ENTRIES = 8192;

constexpr size_t WS_CNT  = 0;
constexpr size_t WS_XN   = 1024;
constexpr size_t WS_XNT  = WS_XN + (size_t)L_ * D_ * 2;
constexpr size_t WS_KX   = WS_XNT + (size_t)D_ * XT_PITCH * 2;
constexpr size_t WS_LUT  = WS_KX + (size_t)D_ * 2 * KX_NW * 4;
constexpr size_t WS_YAT  = WS_LUT + LUT_ENTRIES * 4;
constexpr size_t WS_ACT  = WS_YAT + (size_t)D_ * L_ * 2;
constexpr size_t WS_PART = WS_ACT + (size_t)L_ * F_ * 2;
constexpr size_t WS_YP   = WS_PART + (size_t)4 * L_ * D_ * 4;
constexpr size_t WS_W1P  = WS_YP + (size_t)64 * 4 * L_ * 4;
constexpr size_t WS_W2P  = WS_W1P + (size_t)288 * 18432;

DI float bf2f(u16 b) { return __uint_as_float(((u32)b) << 16); }
DI u16 f2bf(float f) {
  __hip_bfloat16 h = __float2bfloat16(f);
  return *reinterpret_cast<u16*>(&h);
}
DI float rbf(float f) { return bf2f(f2bf(f)); }
typedef __bf16 bf16x2_t __attribute__((ext_vector_type(2)));
using f32x2 = __attribute__((ext_vector_type(2))) float;
DI u32 pack2(float lo, float hi) {
  f32x2 v = {lo, hi};
  return __builtin_bit_cast(u32, __builtin_convertvector(v, bf16x2_t));
}
DI float lo16(u32 v) { return __uint_as_float(v << 16); }
DI float hi16(u32 v) { return __uint_as_float(v & 0xffff0000u); }
DI void st16_wt(void* p, u32x4 v) { asm volatile("global_store_dwordx4 %0, %1, off sc1\n\ts_nop 1" ::"v"(p), "v"(v) : "memory"); }
DI void st8_wt(void* p, u32x2 v) { asm volatile("global_store_dwordx2 %0, %1, off sc1\n\ts_nop 1" ::"v"(p), "v"(v) : "memory"); }

DI int blk_swz(int i16) { return (-(i16 >> 2)) & 3; }

DI void mfma_ip(f32x4& acc, const s16x8& a, const s16x8& b) {
  asm volatile("s_nop 1\n\tv_mfma_f32_16x16x32_bf16 %0, %1, %2, %0" : "+v"(acc) : "v"(a), "v"(b));
}
DI void mfma_drain() { asm volatile("s_nop 15\n\ts_nop 15" ::: "memory"); }

DI void cexp_pos(float lr2, float lh, float pos, float& er, float& ei) {
  const float a0 = lr2 * pos, f = __builtin_amdgcn_fractf(lh * pos);
  float mag;
  asm volatile("v_exp_f32 %2, %3\n\tv_cos_f32 %0, %4\n\tv_sin_f32 %1, %4\n\ts_nop 1\n\tv_mul_f32 %0, %2, %0\n\tv_mul_f32 %1, %2, %1"
               : "=&v"(er), "=&v"(ei), "=&v"(mag) : "v"(a0), "v"(f));
}
DI int kg_row_pos(int row) {
  if (row < 16) return 128 * (row >> 3) + (row & 7);
  if (row < 24) { int r = row - 16; return 256 + 128 * (r >> 2) + 2 * (r & 3); }
  if (row < 32) { int r = row - 24; return 512 + 128 * (r >> 1) + 4 * (r & 1); }
  if (row < 40) return 1024 + 128 * (row - 32);
  if (row == 40) return 2048;
  return 0;
}
struct KIdxTab { u32 v[1088]; };
constexpr int kg_bf16_round(int l) {
  int sft = l >= 1024 ? 3 : l >= 512 ? 2 : l >= 256 ? 1 : 0;
  if (sft == 0) return l;
  int half = 1 << (sft - 1), q = l >> sft;
  return ((l + half - 1 + (q & 1)) >> sft) << sft;
}
constexpr int kg_pos_index(int p) {
  int sg = p >= 2048 ? 4 : p >= 1024 ? 3 : p >= 512 ? 2 : p >= 256 ? 1 : 0;
  int base = sg ? (128 << sg) : 0, rowbase = sg ? 8 + 8 * sg : 0;
  int q = p - base;
  int row = rowbase + (q >> 7) * (8 >> sg) + ((q & 7) >> sg);
  return row * 16 + ((q & 127) >> 3);
}
constexpr KIdxTab make_kidx() {
  KIdxTab t{};
  for (int w = 0; w < 1088; ++w) {
    int l0 = 2047 - 2 * w, l1 = 2046 - 2 * w;
    u32 i0 = l0 >= 0 ? kg_pos_index(kg_bf16_round(l0)) : 768, i1 = l1 >= 0 ? kg_pos_index(kg_bf16_round(l1)) : 768;
    t.v[w] = (i0 * 4) | ((i1 * 4) << 16);
  }
  return t;
}
__device__ const KIdxTab kIdxTab = make_kidx();

struct S4Params { const u16 *A_re, *A_im, *B_re, *B_im, *C_re, *C_im, *log_dt; };
struct KgFrag { u32x4 Bh, Bl, Ah[3], Al[3]; };

DI void kg_load_params(const S4Params& P, int d, int n0, u32x4 (&v)[6]) {
  const size_t o = (size_t)d * N_ + n0;
  v[0] = *(const u32x4*)(P.A_re + o); v[1] = *(const u32x4*)(P.A_im + o);
  v[2] = *(const u32x4*)(P.B_re + o); v[3] = *(const u32x4*)(P.B_im + o);
  v[4] = *(const u32x4*)(P.C_re + o); v[5] = *(const u32x4*)(P.C_im + o);
}
DI void kg_mode(const u32x4 (&v)[6], int g, float dt, const float (&posA)[3], float posB, KgFrag& f) {
  const int e = g >> 1, gg = g & 3;
  const float lr = dt * ((g & 1) ? hi16(v[0][e]) : lo16(v[0][e])), li = dt * ((g & 1) ? hi16(v[1][e]) : lo16(v[1][e]));
  const float br = dt * ((g & 1) ? hi16(v[2][e]) : lo16(v[2][e])), bi = dt * ((g & 1) ? hi16(v[3][e]) : lo16(v[3][e]));
  const float cr = (g & 1) ? hi16(v[4][e]) : lo16(v[4][e]), ci = (g & 1) ? hi16(v[5][e]) : lo16(v[5][e]);
  const float kr = cr * br - ci * bi, ki = cr * bi + ci * br;
  const float lr2 = lr * 1.44269504088896340736f;
  const float lh = li * 0.15915494309189535f;
  float zr, zi;
  cexp_pos(lr2, lh, posB, zr, zi);
  {
    const float xr = kr * zr - ki * zi, xi = -(kr * zi + ki * zr);
    const u32 h = pack2(xr, xi);
    f.Bh[gg] = h; f.Bl[gg] = pack2(xr - lo16(h), xi - hi16(h));
  }
#pragma unroll
  for (int t = 0; t < 3; ++t) {
    float er, ei;
    cexp_pos(lr2, lh, posA[t], er, ei);
    const u32 h = pack2(er, ei);
    f.Ah[t][gg] = h; f.Al[t][gg] = pack2(er - lo16(h), ei - hi16(h));
  }
}
DI void kg_mma(const KgFrag& f, f32x4 (&acc)[3]) {
#pragma unroll
  for (int t = 0; t < 3; ++t) {
    mfma_ip(acc[t], __builtin_bit_cast(s16x8, f.Al[t]), __builtin_bit_cast(s16x8, f.Bh));
    mfma_ip(acc[t], __builtin_bit_cast(s16x8, f.Ah[t]), __builtin_bit_cast(s16x8, f.Bl));
    mfma_ip(acc[t], __builtin_bit_cast(s16x8, f.Ah[t]), __builtin_bit_cast(s16x8, f.Bh));
  }
}
template <int NTHR> DI void kg_gather_store(const float* kv, int d, int t, char* __restrict__ ws) {
  u32* kx = (u32*)(ws + WS_KX) + (size_t)d * 2 * KX_NW;
#pragma unroll
  for (int i = 0; i < 256 / NTHR; ++i) {
    const int w = (i * NTHR + t) * 4;
    const u32x4 ti = *(const u32x4*)(kIdxTab.v + w);
    u32x4 hi, lo;
#pragma unroll
    for (int j = 0; j < 4; ++j) {
      const float k0 = *(const float*)((const char*)kv + (ti[j] & 0xffffu)), k1 = *(const float*)((const char*)kv + (ti[j] >> 16));
      const u32 h = pack2(k0, k1);
      hi[j] = h; lo[j] = pack2(k0 - lo16(h), k1 - hi16(h));
    }
    *(u32x4*)(kx + w) = hi;
    *(u32x4*)(kx + KX_NW + w) = lo;
  }
  if (t < 8) {
    const u32 tt = kIdxTab.v[1024 + t];
    const float k0 = *(const float*)((const char*)kv + (tt & 0xffffu)), k1 = *(const float*)((const char*)kv + (tt >> 16));
    const u32 h = pack2(k0, k1);
    kx[1024 + t] = h; kx[KX_NW + 1024 + t] = pack2(k0 - lo16(h), k1 - hi16(h));
  }
}

typedef __attribute__((address_space(3))) s16x4 lds_s16x4;
template <int N> DI void wait_vm() { asm volatile("s_waitcnt vmcnt(%0)" ::"n"(N) : "memory"); }
DI void raw_barrier() { asm volatile("s_waitcnt lgkmcnt(0)" ::: "memory"); __builtin_amdgcn_s_barrier(); }
#define SCHED0() __builtin_amdgcn_sched_barrier(0)
DI void glds16(const void* sbase, u32 voff, u32 lds_addr) {
  u32 keep;
  asm volatile("s_mov_b32 %0, m0\n\ts_mov_b32 m0, %3\n\ts_nop 0\n\tglobal_load_lds_dwordx4 %1, %2\n\ts_mov_b32 m0, %0"
               : "=&s"(keep) : "v"(voff), "s"(sbase), "s"(lds_addr) : "memory");
}
DI u32 lds_addr_of(const char* p) { return (u32)(size_t)(const __attribute__((address_space(3))) char*)p; }

constexpr int LN_ROWS = 16;
constexpr int LN_BLOCKS = L_ / LN_ROWS;
constexpr int LUT_BLOCKS = 32;
constexpr int KG_BLOCKS = D_;
constexpr int PK_BLOCKS = 576;
constexpr int PREP_BLOCKS = KG_BLOCKS + LN_BLOCKS + LUT_BLOCKS + PK_BLOCKS;
constexpr int LN_PITCH = D_ + 8;

__global__ __launch_bounds__(256) void prep_kernel(const u16* __restrict__ x, S4Params P, const u16* __restrict__ w1,
                                                   const u16* __restrict__ w2, char* __restrict__ ws) {
  __shared__ __attribute__((aligned(1024))) char smem[LN_ROWS * LN_PITCH * 2];
  const int tid = threadIdx.x;
  if (blockIdx.x >= KG_BLOCKS + LN_BLOCKS + LUT_BLOCKS) {
    const int tb = blockIdx.x - (KG_BLOCKS + LN_BLOCKS + LUT_BLOCKS);
    const int lane = tid & 63, wid = __builtin_amdgcn_readfirstlane(tid >> 6);
    const bool is2 = tb >= 288;
    const int tt = is2 ? tb - 288 : tb;
    const int nt = is2 ? tt / 72 : tt / 9, kt = is2 ? tt % 72 : tt % 9;
    const int ldb = is2 ? D_ : F_;
    const char* src = (const char*)((is2 ? w2 : w1) + (size_t)kt * 64 * ldb + nt * 144);
    const u32 l0 = __builtin_amdgcn_readfirstlane(lds_addr_of(smem));
#pragma unroll
    for (int i = 0; i < 5; ++i) {
      const int ins = wid + 4 * i;
      if (ins < 18) {
        const int Lc = ins * 64 + lane, k = Lc / 18, c8 = Lc % 18;
        glds16(src, (u32)(k * ldb + c8 * 8) * 2u, l0 + ins * 1024);
      }
    }
    wait_vm<0>();
    raw_barrier();
    const int i16 = lane & 15, kq = lane >> 4;
    const int rd = (4 * kq + (i16 >> 2)) * 288 + (i16 & 3) * 8;
    char* dst = ws + (is2 ? WS_W2P : WS_W1P) + (size_t)tt * 18432 + lane * 16;
#pragma unroll
    for (int i = 0; i < 5; ++i) {
      const int fr = wid + 4 * i;
      if (fr < 18) {
        const int ks = fr / 9, nb = fr % 9;
        s16x4 lo = __builtin_amdgcn_ds_read_tr16_b64_v4i16((lds_s16x4*)(smem + rd + ks * 32 * 288 + nb * 32));
        s16x4 hi = __builtin_amdgcn_ds_read_tr16_b64_v4i16((lds_s16x4*)(smem + rd + ks * 32 * 288 + 16 * 288 + nb * 32));
        const s16x8 v = s16x8{lo[0], lo[1], lo[2], lo[3], hi[0], hi[1], hi[2], hi[3]};
        st16_wt(dst + fr * 1024, __builtin_bit_cast(u32x4, v));
      }
    }
    return;
  }
  if (blockIdx.x >= LN_BLOCKS && blockIdx.x < LN_BLOCKS + KG_BLOCKS) {
    const int d = blockIdx.x - LN_BLOCKS, lane = tid & 63, wid = tid >> 6;
    const int i16 = lane & 15, kq = lane >> 4;
    float* red = (float*)smem;
    float* kv = red + 4 * 768;
    u32x4 v[6];
    kg_load_params(P, d, wid * 32 + kq * 8, v);
    const float dt = rbf(expf(bf2f(P.log_dt[d])));
    f32x4 acc[3] = {f32x4{0, 0, 0, 0}, f32x4{0, 0, 0, 0}, f32x4{0, 0, 0, 0}};
    float posA[3];
#pragma unroll
    for (int t = 0; t < 3; ++t) posA[t] = (float)kg_row_pos(16 * t + i16);
    const float posB = (float)(8 * i16);
    KgFrag f;
#pragma unroll
    for (int g = 0; g < 8; ++g) { kg_mode(v, g, dt, posA, posB, f); if ((g & 3) == 3) kg_mma(f, acc); }
    mfma_drain();
#pragma unroll
    for (int t = 0; t < 3; ++t)
#pragma unroll
      for (int j = 0; j < 4; ++j) red[wid * 768 + (16 * t + 4 * kq + j) * 16 + i16] = acc[t][j];
    __syncthreads();
#pragma unroll
    for (int i = 0; i < 3; ++i) { int e = tid + 256 * i; kv[e] = (red[e] + red[768 + e]) + (red[2 * 768 + e] + red[3 * 768 + e]); }
    if (tid == 0) kv[768] = 0.f;
    __syncthreads();
    kg_gather_store<256>(kv, d, tid, ws);
    return;
  }
  const int bid = blockIdx.x < LN_BLOCKS ? blockIdx.x : blockIdx.x - KG_BLOCKS;
  if (bid < LN_BLOCKS) {
    u16* tile = (u16*)smem;
    const int t0 = bid * LN_ROWS;
    const int r = tid >> 4, j16 = tid & 15;
    u32x2 v[9];
#pragma unroll
    for (int i = 0; i < 9; ++i) v[i] = *(const u32x2*)(x + (size_t)(t0 + r) * D_ + (j16 + 16 * i) * 4);
    float s = 0.f;
#pragma unroll
    for (int i = 0; i < 9; ++i)
#pragma unroll
      for (int e = 0; e < 2; ++e) s += lo16(v[i][e]) + hi16(v[i][e]);
    s += __shfl_xor(s, 1); s += __shfl_xor(s, 2); s += __shfl_xor(s, 4); s += __shfl_xor(s, 8);
    const float mean = s * (1.0f / 576.0f);
    float q = 0.f;
#pragma unroll
    for (int i = 0; i < 9; ++i)
#pragma unroll
      for (int e = 0; e < 2; ++e) { float a = lo16(v[i][e]) - mean, b = hi16(v[i][e]) - mean; q += a * a + b * b; }
    q += __shfl_xor(q, 1); q += __shfl_xor(q, 2); q += __shfl_xor(q, 4); q += __shfl_xor(q, 8);
    const float var = q * (1.0f / 576.0f);
    const float mb = rbf(mean), vb = rbf(var);
    const float den = rbf(sqrtf(rbf(vb + 1e-5f)));
    const float rden = 1.0f / den;
    auto qdiv = [&](float a) { float qq = a * rden; return __builtin_fmaf(__builtin_fmaf(-qq, den, a), rden, qq); };
#pragma unroll
    for (int i = 0; i < 9; ++i) {
      u32x2 o;
#pragma unroll
      for (int e = 0; e < 2; ++e) {
        const u32 c = pack2(lo16(v[i][e]) - mb, hi16(v[i][e]) - mb);
        o[e] = pack2(qdiv(lo16(c)), qdiv(hi16(c)));
      }
      *(u32x2*)(tile + r * LN_PITCH + (j16 + 16 * i) * 4) = o;
    }
    __syncthreads();
    char* xn_g = ws + WS_XN + (size_t)bid * 18 * 1024;
#pragma unroll
    for (int i = 0; i < 5; ++i) {
      const int c = tid + 256 * i;
      if (c < 1152) {
        const int P = c >> 6, w = c & 63;
        const int i16 = w >> 2, kq = (w & 3) ^ blk_swz(w >> 2);
        const u16* src = tile + i16 * LN_PITCH + 32 * P + 4 * kq;
        u32x2 a = *(const u32x2*)src, b = *(const u32x2*)(src + 16);
        *(u32x4*)(xn_g + (size_t)c * 16) = u32x4{a[0], a[1], b[0], b[1]};
      }
    }
    u16* xt_g = (u16*)(ws + WS_XNT);
#pragma unroll
    for (int i = 0; i < 5; ++i) {
      const int c = tid + 256 * i;
      if (c < 1152) {
        const int d = c >> 1, tq = c & 1;
        u32x4 o;
#pragma unroll
        for (int j = 0; j < 4; ++j) {
          u32 lo = tile[(tq * 8 + 2 * j) * LN_PITCH + d], hi = tile[(tq * 8 + 2 * j + 1) * LN_PITCH + d];
          o[j] = lo | (hi << 16);
        }
        *(u32x4*)(xt_g + (size_t)d * XT_PITCH + 128 + t0 + tq * 8) = o;
      }
    }
    if (tid < 72) {
      int c = bid * 72 + tid;
      *(u32x4*)(xt_g + (size_t)(c >> 4) * XT_PITCH + (c & 15) * 8) = u32x4{0u, 0u, 0u, 0u};
    }
  } else {
    const int e8 = (bid - LN_BLOCKS) * 256 + tid;
    const int sign = e8 >> 12, e = e8 & 4095;
    float* lut = (float*)(ws + WS_LUT);
    int a = LUT_LO + e;
    if (a > LUT_HI) a = LUT_HI;
    float h = bf2f((u16)(a | (sign << 15)));
    float ex = rbf(expf(-fabsf(h)));
    float lp = rbf(log1pf(ex));
    float sp = rbf(lp + (h > 0.f ? h : 0.f));
    float th = tanhf(sp);
    lut[e8] = rbf(th);
  }
}

constexpr int BIAS_LDS = 51200;
constexpr int LUT_LDS = 61440;
constexpr int KG_LDS = 3200;
constexpr int CV_ST = 14848;
constexpr int CV_LDS = CV_ST + 8192;

DI void conv_stage_part(char* st, int d, int lane, int first, int step, const char* __restrict__ ws) {
  const char* ug = ws + WS_XNT + (size_t)d * XT_PITCH * 2;
  const char* kg = ws + WS_KX + (size_t)d * 2 * KX_NW * 4;
  const u32 l0 = __builtin_amdgcn_readfirstlane(lds_addr_of(st));
#pragma unroll
  for (int i = 0; i < 14; ++i) {
    if (i % step == first) {
      if (i < 5) glds16(ug + i * 1024, (u32)lane * 16u, l0 + i * 1024);
      else if (i < 13) glds16(kg + (i - 5) * 1024, (u32)lane * 16u, l0 + 5120 + (i - 5) * 1024);
      else glds16(kg + 8192, (u32)(lane & 3) * 16u, l0 + 5120 + 8192);
    }
  }
}
DI s16x8 conv_bfrag(const u32* kp, u32 sh) {
  const u32 e0 = kp[0], e1 = kp[1], e2 = kp[2], e3 = kp[3], e4 = kp[4];
  const u32x4 v = {__builtin_amdgcn_alignbyte(e1, e0, sh), __builtin_amdgcn_alignbyte(e2, e1, sh),
                   __builtin_amdgcn_alignbyte(e3, e2, sh), __builtin_amdgcn_alignbyte(e4, e3, sh)};
  return __builtin_bit_cast(s16x8, v);
}
DI void conv_hlsum(const f32x4& a, int hl, float& o0, float& o1) {
  const float s0 = hl ? a[0] : a[2], s1 = hl ? a[1] : a[3];
  const float k0 = hl ? a[2] : a[0], k1 = hl ? a[3] : a[1];
  o0 = k0 + __builtin_bit_cast(float, __builtin_amdgcn_update_dpp(0, __builtin_bit_cast(int, s0), 0x128, 0xf, 0xf, false));
  o1 = k1 + __builtin_bit_cast(float, __builtin_amdgcn_update_dpp(0, __builtin_bit_cast(int, s1), 0x128, 0xf, 0xf, false));
}

DI void s4_side_gemm1(char* smem, int bid, int j, int lane, const char* __restrict__ Ag, const char* __restrict__ Bg,
                      const u16* __restrict__ biasg, char* __restrict__ ws) {
  const int i16 = lane & 15, kq = lane >> 4;
  {
    constexpr int STAGE = 51200;
    const u32 lds0 = __builtin_amdgcn_readfirstlane(lds_addr_of(smem));
    const u32 lo = (u32)lane * 16u;
    auto stage = [&](int kt, int buf) {
      const u32 sb = lds0 + buf * STAGE;
#pragma unroll
      for (int i = 0; i < 8; ++i) {
        const int bb = j + 4 * i;
        glds16(Ag + ((size_t)(bb >> 1) * 18 + 2 * kt + (bb & 1)) * 1024, lo, sb + bb * 1024);
      }
      const char* bk = Bg + (size_t)kt * 18432;
#pragma unroll
      for (int i = 0; i < 4; ++i) glds16(bk + (j + 4 * i) * 1024, lo, sb + 32768 + (j + 4 * i) * 1024);
      if (j < 2) glds16(bk + (j + 16) * 1024, lo, sb + 32768 + (j + 16) * 1024);
    };
    stage(0, 1); stage(1, 2);
    if (j < 2) wait_vm<13>(); else wait_vm<12>();
    __builtin_amdgcn_s_barrier();
#pragma unroll 1
    for (int kt = 0; kt < 7; ++kt) {
      stage(kt + 2, kt % 3);
      if (j < 2) wait_vm<13>(); else wait_vm<12>();
      __builtin_amdgcn_s_barrier();
    }
#pragma unroll
    for (int i = 0; i < 8; ++i) glds16(ws + WS_LUT + (j * 8 + i) * 1024, lo, lds0 + LUT_LDS + (j * 8 + i) * 1024);
#pragma unroll
    for (int i = 0; i < 2; ++i) glds16(biasg, lo < 288u ? lo : 272u, lds0 + BIAS_LDS + (j * 2 + i) * 1024);
    wait_vm<10>();
    __builtin_amdgcn_s_barrier();
    wait_vm<0>();
    __builtin_amdgcn_s_barrier();
  }
  const int dx = 512 + (bid >> 2), g = bid & 3;
  char* st = smem + (j < 3 ? j * CV_ST : 102400);
  conv_stage_part(st, dx, lane, 0, 1, ws);
  wait_vm<0>();
  {
    const int rho = i16 & 7, hl = i16 >> 3;
    const char* us = st + 256;
    const u32 sh = ((1 - rho) & 1) * 2;
    const u32* ks = (const u32*)(st + 5120) + hl * KX_NW + ((2047 - 8 * (4 * g + kq) - rho) >> 1);
    f32x4 ya[4] = {f32x4{0, 0, 0, 0}, f32x4{0, 0, 0, 0}, f32x4{0, 0, 0, 0}, f32x4{0, 0, 0, 0}};
    const char* fb = us + (16 * j - 4 * g + i16 - kq) * 16;
#pragma unroll
    for (int q = 0; q < 16; ++q) {
      const s16x8 B = conv_bfrag(ks - 64 * q, sh);
#pragma unroll
      for (int r = 0; r < 4; ++r) {
        if (4 * r + 3 >= q) {
          if (j + 4 * r >= q) {
            const s16x8 F = *(const s16x8*)(fb + (4 * r - q) * 256);
            mfma_ip(ya[r], F, B);
          }
        }
      }
    }
    mfma_drain();
    float* yp = (float*)(ws + WS_YP) + ((size_t)(dx - 512) * 4 + g) * L_;
#pragma unroll
    for (int r = 0; r < 4; ++r) {
      float o0, o1;
      conv_hlsum(ya[r], hl, o0, o1);
      float* q = yp + 128 * (j + 4 * r) + 32 * kq + 16 * hl + rho;
      q[0] = o0; q[8] = o1;
    }
  }
}

DI void s4_side_gemm2(char* smem, char* scratch, int bid, int j, int lane, const char* __restrict__ Ag, const char* __restrict__ Bg,
                      const u16* __restrict__ Dskip, char* __restrict__ ws) {
  const int d = 2 * bid + (j >> 1), gh = j & 1;
  char* st = scratch + (j >> 1) * CV_LDS;
  conv_stage_part(st, d, lane, gh, 2, ws);
  const float dsk = bf2f(Dskip[d]);
  constexpr int STAGE2 = 34816;
  const u32 lds0 = __builtin_amdgcn_readfirstlane(lds_addr_of(smem));
  const u32 lo = (u32)lane * 16u;
  auto stage = [&](int kt) {
    const u32 sb = lds0 + (kt % 3) * STAGE2;
#pragma unroll
    for (int i = 0; i < 4; ++i) {
      const int bb = j + 4 * i;
      glds16(Ag + ((size_t)(bb >> 1) * 144 + 2 * kt + (bb & 1)) * 1024, lo, sb + bb * 1024);
    }
    const char* bk = Bg + (size_t)kt * 18432;
#pragma unroll
    for (int i = 0; i < 4; ++i) glds16(bk + (j + 4 * i) * 1024, lo, sb + 16384 + (j + 4 * i) * 1024);
    if (j < 2) glds16(bk + (j + 16) * 1024, lo, sb + 16384 + (j + 16) * 1024);
  };
  int jn = 0;
  auto step = [&]() {
    if (jn + 2 < 18) { if (j < 2) wait_vm<9>(); else wait_vm<8>(); }
    else wait_vm<0>();
    __builtin_amdgcn_s_barrier();
    ++jn;
    if (jn + 2 < 18) stage(jn + 2);
  };
  stage(0); stage(1);
  if (j < 2) wait_vm<9>(); else wait_vm<8>();
  __builtin_amdgcn_s_barrier();
  stage(2);
  step();
  const int i16 = lane & 15, kq = lane >> 4;
  const int rho = i16 & 7, hl = i16 >> 3;
  const char* us = st + 256;
  const u32 sh = ((1 - rho) & 1) * 2;
  f32x4 acc[16];
#pragma unroll
  for (int R = 0; R < 16; ++R) acc[R] = f32x4{0, 0, 0, 0};
#pragma unroll
  for (int gi = 0; gi < 2; ++gi) {
    const int g = 2 * gh + gi;
    s16x8 F[16];
#pragma unroll
    for (int dl = 0; dl < 16; ++dl) F[dl] = *(const s16x8*)(us + (16 * dl - 4 * g + i16 - kq) * 16);
    const u32* ks = (const u32*)(st + 5120) + hl * KX_NW + ((2047 - 8 * (4 * g + kq) - rho) >> 1);
#pragma unroll
    for (int q = 0; q < 16; ++q) {
      const s16x8 B = conv_bfrag(ks - 64 * q, sh);
#pragma unroll
      for (int R = q; R < 16; ++R) mfma_ip(acc[R], F[R - q], B);
      if (q <= 4 || q == 6 || q == 8 || q == 15) { SCHED0(); step(); }
    }
  }
  mfma_drain();
  float* ybuf = (float*)(st + CV_ST);
  float o[16][2];
#pragma unroll
  for (int R = 0; R < 16; ++R) conv_hlsum(acc[R], hl, o[R][0], o[R][1]);
  float* yl = ybuf + 32 * kq + 16 * hl + rho;
  if (gh) {
#pragma unroll
    for (int R = 0; R < 8; ++R) { yl[128 * R] = o[R][0]; yl[128 * R + 8] = o[R][1]; }
  } else {
#pragma unroll
    for (int R = 8; R < 16; ++R) { yl[128 * R] = o[R][0]; yl[128 * R + 8] = o[R][1]; }
  }
  raw_barrier();
  __builtin_amdgcn_s_barrier();
  if (gh) {
#pragma unroll
    for (int R = 8; R < 16; ++R) { yl[128 * R] += o[R][0]; yl[128 * R + 8] += o[R][1]; }
  } else {
#pragma unroll
    for (int R = 0; R < 8; ++R) { yl[128 * R] = o[R][0] + yl[128 * R]; yl[128 * R + 8] = o[R][1] + yl[128 * R + 8]; }
  }
  asm volatile("s_waitcnt lgkmcnt(0)" ::: "memory");
#pragma unroll
  for (int i = 0; i < 2; ++i) {
    const int t = gh * 1024 + (i * 64 + lane) * 8;
    const s16x8 uv = *(const s16x8*)(us + t * 2);
    const f32x4 s0 = *(const f32x4*)(ybuf + t), s1 = *(const f32x4*)(ybuf + t + 4);
    float sv[8] = {s0[0], s0[1], s0[2], s0[3], s1[0], s1[1], s1[2], s1[3]};
    u32x4 ov;
#pragma unroll
    for (int jj = 0; jj < 4; ++jj) {
      float r0 = rbf(sv[2 * jj]) + rbf(bf2f((u16)uv[2 * jj]) * dsk);
      float r1 = rbf(sv[2 * jj + 1]) + rbf(bf2f((u16)uv[2 * jj + 1]) * dsk);
      ov[jj] = pack2(r0, r1);
    }
    *(u32x4*)(ws + WS_YAT + ((size_t)d * L_ + t) * 2) = ov;
  }
}

template <int MODE> struct GemmCfg {
  static constexpr int BM = MODE ? 128 : 256;
  static constexpr int NPA = MODE ? F_ / 32 : D_ / 32;
  static constexpr int LDB = MODE ? D_ : F_;
  static constexpr int A_BYTES = BM * 128;
  static constexpr int B_BYTES = 64 * 288;
  static constexpr int STAGE = A_BYTES + B_BYTES;
  static constexpr int A_IPW = BM / 64;
  static constexpr int S4_OFF = 3 * STAGE;
  static constexpr int SMEM = 3 * STAGE + (MODE ? 2 * CV_LDS : 0);
};
#define NPRE 2

template <int MODE, int NB>
DI void gemm_wave_body(char* smem, const char* __restrict__ Ag, const char* __restrict__ Bg, const char* __restrict__ lutg,
                       const u16* __restrict__ biasg,
                       int wid, int lane, int wm, int wk, int pb0  , int sblk  ,
                       f32x4 (&acc)[4][5]) {
  using C = GemmCfg<MODE>;
  const int i16 = lane & 15, kq = lane >> 4;
  const bool extraB = wid < 2;
  const u32 aoffl = (u32)lane * 16u;
  const int a_rd = (wm * 8) * 1024 + i16 * 64 + ((kq ^ blk_swz(i16)) * 16);
  const int b_rd = C::A_BYTES + lane * 16 + pb0 * 1024;
  const int b_rs = C::A_BYTES + lane * 16 + sblk * 1024;

  const u32 lds0 = __builtin_amdgcn_readfirstlane(lds_addr_of(smem));
  auto stage = [&](int kt, int buf) {
    const u32 sb = lds0 + buf * C::STAGE;
#pragma unroll
    for (int i = 0; i < C::A_IPW; ++i) {
      const int b = wid * C::A_IPW + i;
      glds16(Ag + ((size_t)(b >> 1) * C::NPA + 2 * kt + (b & 1)) * 1024, aoffl, sb + b * 1024);
    }
    const char* bk = Bg + (size_t)kt * 18432;
#pragma unroll
    for (int i = 0; i < 2; ++i) glds16(bk + (wid + 8 * i) * 1024, aoffl, sb + C::A_BYTES + (wid + 8 * i) * 1024);
    if (extraB) glds16(bk + (wid + 16) * 1024, aoffl, sb + C::A_BYTES + (wid + 16) * 1024);
  };
  auto readA = [&](s16x8 (&a)[4], int buf, int ks) {
    const char* sb = smem + buf * C::STAGE;
#pragma unroll
    for (int mi = 0; mi < 4; ++mi) a[mi] = *(const s16x8*)(sb + a_rd + (mi * 2 + ks) * 1024);
  };
  auto readB = [&](int nb, int buf, int ks) -> s16x8 {
    const char* sb = smem + buf * C::STAGE;
    return *(const s16x8*)(sb + (nb < 4 ? b_rd + nb * 1024 : b_rs) + ks * 9 * 1024);
  };
  s16x8 a0[4], a1[4], b[NB];
  auto mm = [&](s16x8 (&ac)[4], int nb) {
#pragma unroll
    for (int mi = 0; mi < 4; ++mi)
      asm volatile("v_mfma_f32_16x16x32_bf16 %0, %1, %2, %0" : "+v"(acc[mi][nb]) : "v"(b[nb]), "v"(ac[mi]));
  };
  auto sub_pre = [&](s16x8 (&ac)[4]) {
#pragma unroll
    for (int nb = 0; nb < NPRE; ++nb) mm(ac, nb);
    SCHED0();
  };
  auto sub_post = [&](s16x8 (&ac)[4], s16x8 (&an)[4], bool ld, int nbuf, int nks) {
    if (ld) {
      readA(an, nbuf, nks);
#pragma unroll
      for (int nb = 0; nb < NPRE; ++nb) b[nb] = readB(nb, nbuf, nks);
    }
    SCHED0();
#pragma unroll
    for (int nb = NPRE; nb < NB; ++nb) {
      mm(ac, nb);
      if (ld) b[nb] = readB(nb, nbuf, nks);
      SCHED0();
    }
  };
  constexpr int NW0 = C::A_IPW + 2;
#pragma unroll
  for (int mi = 0; mi < 4; ++mi)
#pragma unroll
    for (int nb = 0; nb < NB; ++nb) asm volatile("" : "+v"(acc[mi][nb]));
  if (MODE == 0) {
    raw_barrier();
    readA(a0, 1, 0);
#pragma unroll
    for (int nb = 0; nb < NB; ++nb) b[nb] = readB(nb, 1, 0);
#pragma unroll 1
    for (int o = 0; o < 3; ++o) {
#pragma unroll
      for (int u = 0; u < 3; ++u) {
        SCHED0();
        sub_pre(a0); sub_post(a0, a1, true, (u + 1) % 3, 1);
        sub_pre(a1);
        raw_barrier();
        sub_post(a1, a0, true, (u + 2) % 3, 0);
      }
    }
  } else {
    raw_barrier();
    readA(a0, 0, wk);
#pragma unroll
    for (int nb = 0; nb < NB; ++nb) b[nb] = readB(nb, 0, wk);
#pragma unroll 1
    for (int o = 0; o < 3; ++o) {
#pragma unroll
      for (int u = 0; u < 6; ++u) {
        const int j = 6 * o + u;
        SCHED0();
        if (u & 1) sub_pre(a1); else sub_pre(a0);
        if (j + 1 < 18) raw_barrier();
        SCHED0();
        if (u & 1) sub_post(a1, a0, true, (u + 1) % 3, wk);
        else       sub_post(a0, a1, true, (u + 1) % 3, wk);
      }
    }
    raw_barrier();
  }
  asm volatile("s_nop 15\n\ts_nop 15" ::: "memory");
#pragma unroll
  for (int mi = 0; mi < 4; ++mi)
#pragma unroll
    for (int nb = 0; nb < NB; ++nb) asm volatile("" : "+v"(acc[mi][nb]));
}

__global__ __launch_bounds__(768) void gemm1_kernel(const u16* __restrict__ w1, const u16* __restrict__ bias, char* __restrict__ ws) {
  using C = GemmCfg<0>;
  __shared__ __attribute__((aligned(1024))) char smem[C::SMEM];
  const int tid = threadIdx.x, lane = tid & 63, wid = __builtin_amdgcn_readfirstlane(tid >> 6);
  const int bid = blockIdx.x;
  const int xcd = bid & 7, idx = bid >> 3;
  const int nt = 4 * xcd + (idx & 3), m0 = (idx >> 2) * 256;
  const int n0 = nt * 144;
  const char* Ag = ws + WS_XN + (size_t)(m0 >> 4) * C::NPA * 1024;
  const char* Bg = ws + WS_W1P + (size_t)nt * 9 * 18432;
  if (wid >= 8) { s4_side_gemm1(smem, bid, wid - 8, lane, Ag, Bg, bias + n0, ws); return; }
  __builtin_amdgcn_s_setprio(1);

  const int wn = wid >> 2;
  const int wm = wid & 3;
  const int odd = nt & 1;
  const int pb0 = odd + 4 * wn;
  const int sblk = odd ? 0 : 8;
  f32x4 acc[4][5];
#pragma unroll
  for (int mi = 0; mi < 4; ++mi)
#pragma unroll
    for (int nb = 0; nb < 5; ++nb) acc[mi][nb] = f32x4{0, 0, 0, 0};
  const int i16 = lane & 15, kq = lane >> 4;
  const char* lutg = ws + WS_LUT;
  STAMP_DECL STAMP_RT(); STAMP_NOW();
  if (wn == 0) gemm_wave_body<0, 5>(smem, Ag, Bg, lutg, bias + n0, wid, lane, wm, 0, pb0, sblk, acc);
  else         gemm_wave_body<0, 4>(smem, Ag, Bg, lutg, bias + n0, wid, lane, wm, 0, pb0, sblk, acc);
  STAMP_NOW();
  f32x2 bf_[5][2];
#pragma unroll
  for (int nb = 0; nb < 5; ++nb) {
    const int blk = nb < 4 ? pb0 + nb : sblk;
    const u32x2 bb = *(const u32x2*)(smem + BIAS_LDS + wid * 1024 + (blk * 16 + 4 * kq) * 2);
#pragma unroll
    for (int e = 0; e < 2; ++e) { bf_[nb][e] = f32x2{lo16(bb[e]), hi16(bb[e])}; asm volatile("" : "+v"(bf_[nb][e])); }
  }
  const char* lut = smem + (LUT_LDS - LUT_LO * 4);
  char* act = ws + WS_ACT;
  const int inblk = i16 * 64 + ((kq ^ blk_swz(i16)) * 16);
#pragma unroll
  for (int mi = 0; mi < 4; ++mi) {
    float hf[5][4], th[5][4];
#pragma unroll
    for (int nb = 0; nb < 5; ++nb) {
      if (nb < 4 || wn == 0) {
#pragma unroll
        for (int e = 0; e < 2; ++e) {
          u32 a = pack2(acc[mi][nb][2 * e], acc[mi][nb][2 * e + 1]);
          u32 h = pack2(lo16(a) + bf_[nb][e][0], hi16(a) + bf_[nb][e][1]);
          hf[nb][2 * e] = lo16(h); hf[nb][2 * e + 1] = hi16(h);
        }
#pragma unroll
        for (int j = 0; j < 4; ++j) {
          const u32 u = __float_as_uint(hf[nb][j]);
          const float m = __builtin_amdgcn_fmed3f(__builtin_fabsf(hf[nb][j]), 0.0009765625f, 128.0f);
          u32 o;
          asm("v_lshl_add_u32 %0, %1, 14, %2" : "=v"(o) : "v"(u >> 31), "v"(__float_as_uint(m) >> 14));
          th[nb][j] = *(const float*)(lut + o);
        }
      }
    }
    const size_t mbg = (size_t)((m0 >> 4) + wm * 4 + mi);
    u32 r[5][2];
#pragma unroll
    for (int nb = 0; nb < 5; ++nb)
      if (nb < 4 || wn == 0) {
#pragma unroll
        for (int e = 0; e < 2; ++e) r[nb][e] = pack2(hf[nb][2 * e] * th[nb][2 * e], hf[nb][2 * e + 1] * th[nb][2 * e + 1]);
      }
#pragma unroll
    for (int pr = 0; pr < 2; ++pr) {
      const int Pb = (n0 + (pb0 + 2 * pr) * 16) >> 5;
      st16_wt(act + (mbg * 144 + Pb) * 1024 + inblk, u32x4{r[2 * pr][0], r[2 * pr][1], r[2 * pr + 1][0], r[2 * pr + 1][1]});
    }
    if (wn == 0) {
      const int nb16 = (n0 >> 4) + sblk;
      st8_wt(act + (mbg * 144 + (nb16 >> 1)) * 1024 + inblk + (nb16 & 1) * 8, u32x2{r[4][0], r[4][1]});
    }
  }
  STAMP_NOW(); STAMP_VM(); STAMP_NOW(); STAMP_RT();
  STAMP_OUT(2, ws);
}

__global__ __launch_bounds__(768) void gemm2_kernel(const u16* __restrict__ w2, const u16* __restrict__ Dskip, char* __restrict__ ws) {
  using C = GemmCfg<1>;
  __shared__ __attribute__((aligned(1024))) char smem[C::SMEM];
  const int tid = threadIdx.x, lane = tid & 63, wid = __builtin_amdgcn_readfirstlane(tid >> 6);
  const int bid = blockIdx.x;
  const int xcd = bid & 7, idx = bid >> 3;
  const int ksplit = xcd >> 1, mt = (xcd & 1) * 8 + (idx >> 2), nt = idx & 3;
  const int m0 = mt * 128, n0 = nt * 144;
  const char* Ag = ws + WS_ACT + ((size_t)(m0 >> 4) * C::NPA + ksplit * 36) * 1024;
  const char* Bg = ws + WS_W2P + ((size_t)nt * 72 + ksplit * 18) * 18432;
  if (wid >= 8) {
    STAMP_DECL STAMP_RT(); STAMP_NOW();
    __builtin_amdgcn_s_setprio(2);
    s4_side_gemm2(smem, smem + C::S4_OFF, bid, wid - 8, lane, Ag, Bg, Dskip, ws);
    STAMP_NOW(); STAMP_NOW(); STAMP_NOW(); STAMP_RT();
    STAMP_OUT(3, ws);
    return;
  }
  __builtin_amdgcn_s_setprio(1);

  const int wn = wid >> 2;
  const int wmv = wid & 3;
  const int wk = wmv >> 1, wm = wmv & 1;
  const int odd = nt & 1;
  const int pb0 = odd + 4 * wn;
  const int sblk = odd ? 0 : 8;
  f32x4 acc[4][5];
#pragma unroll
  for (int mi = 0; mi < 4; ++mi)
#pragma unroll
    for (int nb = 0; nb < 5; ++nb) acc[mi][nb] = f32x4{0, 0, 0, 0};
  STAMP_DECL STAMP_RT(); STAMP_NOW();
  if (wn == 0) gemm_wave_body<1, 5>(smem, Ag, Bg, nullptr, nullptr, wid, lane, wm, wk, pb0, sblk, acc);
  else         gemm_wave_body<1, 4>(smem, Ag, Bg, nullptr, nullptr, wid, lane, wm, wk, pb0, sblk, acc);
  STAMP_NOW();

  const int i16 = lane & 15, kq = lane >> 4;
  f32x4* red = (f32x4*)smem;
  const int slot = wn * 2 + wm;
  if (wk == 1) {
#pragma unroll
    for (int mi = 0; mi < 4; ++mi)
#pragma unroll
      for (int nb = 0; nb < 5; ++nb)
        if (nb < 4 || wn == 0) red[(slot * 20 + mi * 5 + nb) * 64 + lane] = acc[mi][nb];
  }
  raw_barrier();
  char* partb = ws + WS_PART;
  constexpr size_t PSTRIDE = (size_t)L_ * D_ * 4;
  if (wk == 0) {
#pragma unroll
    for (int mi = 0; mi < 4; ++mi)
#pragma unroll
      for (int nb = 0; nb < 5; ++nb)
        if (nb < 4 || wn == 0) {
          acc[mi][nb] += red[(slot * 20 + mi * 5 + nb) * 64 + lane];
          const size_t mbg = (size_t)((m0 >> 4) + wm * 4 + mi);
          const int nbg = (n0 >> 4) + (nb < 4 ? pb0 + nb : sblk);
          st16_wt(partb + ksplit * PSTRIDE + (mbg * 36 + nbg) * 1024 + i16 * 64 + kq * 16, __builtin_bit_cast(u32x4, acc[mi][nb]));
        }
  }
  STAMP_NOW(); STAMP_VM(); STAMP_NOW(); STAMP_RT();
  STAMP_OUT(3, ws);
}

__global__ __launch_bounds__(256) void final_kernel(const u16* __restrict__ x, const u16* __restrict__ Dskip,
                                                    const char* __restrict__ ws, u16* __restrict__ out) {
  __shared__ __attribute__((aligned(16))) u16 yt[64 * 72];
  const int tid = threadIdx.x;
  const int tt = blockIdx.x / 9, dt = blockIdx.x % 9;
  const int t0 = tt * 64, d0 = dt * 64;
  const u16* yat = (const u16*)(ws + WS_YAT);
  const char* part = ws + WS_PART;
  u32x4 yv[2], xv[2];
  f32x4 p[2][4][2];
#pragma unroll
  for (int i = 0; i < 2; ++i) {
    const int c = tid + 256 * i;
    const int r = c >> 3, ch = c & 7;
    if (dt < 8) yv[i] = *(const u32x4*)(yat + (size_t)(d0 + r) * L_ + t0 + ch * 8);
    else {
      const float* yp = (const float*)(ws + WS_YP) + (size_t)r * 4 * L_ + t0 + ch * 8;
      f32x4 a0 = (*(const f32x4*)(yp) + *(const f32x4*)(yp + L_)) + (*(const f32x4*)(yp + 2 * L_) + *(const f32x4*)(yp + 3 * L_));
      f32x4 a1 = (*(const f32x4*)(yp + 4) + *(const f32x4*)(yp + L_ + 4)) + (*(const f32x4*)(yp + 2 * L_ + 4) + *(const f32x4*)(yp + 3 * L_ + 4));
      const u32x4 uv = *(const u32x4*)((const u16*)(ws + WS_XNT) + (size_t)(d0 + r) * XT_PITCH + 128 + t0 + ch * 8);
      const float dsk = bf2f(Dskip[d0 + r]);
      float sv[8] = {a0[0], a0[1], a0[2], a0[3], a1[0], a1[1], a1[2], a1[3]};
#pragma unroll
      for (int jj = 0; jj < 4; ++jj)
        yv[i][jj] = pack2(rbf(sv[2 * jj]) + rbf(lo16(uv[jj]) * dsk), rbf(sv[2 * jj + 1]) + rbf(hi16(uv[jj]) * dsk));
    }
    xv[i] = *(const u32x4*)(x + (size_t)(t0 + r) * D_ + d0 + ch * 8);
    const int t = t0 + r, d = d0 + ch * 8;
    const size_t po = ((size_t)(t >> 4) * 36 + (d >> 4)) * 1024 + (t & 15) * 64 + ((d >> 2) & 3) * 16;
#pragma unroll
    for (int s = 0; s < 4; ++s) {
      p[i][s][0] = *(const f32x4*)(part + (size_t)s * L_ * D_ * 4 + po);
      p[i][s][1] = *(const f32x4*)(part + (size_t)s * L_ * D_ * 4 + po + 16);
    }
  }
#pragma unroll
  for (int i = 0; i < 2; ++i) {
    const int c = tid + 256 * i;
    *(u32x4*)(yt + (c >> 3) * 72 + (c & 7) * 8) = yv[i];
  }
  __syncthreads();
#pragma unroll
  for (int i = 0; i < 2; ++i) {
    const int c = tid + 256 * i;
    const int r = c >> 3, ch = c & 7;
    f32x4 s0 = (p[i][0][0] + p[i][1][0]) + (p[i][2][0] + p[i][3][0]);
    f32x4 s1 = (p[i][0][1] + p[i][1][1]) + (p[i][2][1] + p[i][3][1]);
    float s[8] = {s0[0], s0[1], s0[2], s0[3], s1[0], s1[1], s1[2], s1[3]};
    u32x4 ov;
#pragma unroll
    for (int j = 0; j < 4; ++j) {
      float r2[2];
#pragma unroll
      for (int h = 0; h < 2; ++h) {
        int e = 2 * j + h;
        float xx = h ? hi16(xv[i][j]) : lo16(xv[i][j]);
        float ya = bf2f(yt[(ch * 8 + e) * 72 + r]);
        r2[h] = rbf(xx + ya) + rbf(s[e]);
      }
      ov[j] = pack2(r2[0], r2[1]);
    }
    *(u32x4*)(out + (size_t)(t0 + r) * D_ + d0 + ch * 8) = ov;
  }
}

extern "C" void kernel_launch(void* const* d_in, const int* in_sizes, int n_in,
                              void* d_out, int out_size, void* d_ws, size_t ws_size,
                              hipStream_t stream) {
  const u16* x      = (const u16*)d_in[0];
  const u16* A_re   = (const u16*)d_in[1];
  const u16* A_im   = (const u16*)d_in[2];
  const u16* B_re   = (const u16*)d_in[3];
  const u16* B_im   = (const u16*)d_in[4];
  const u16* C_re   = (const u16*)d_in[5];
  const u16* C_im   = (const u16*)d_in[6];
  const u16* log_dt = (const u16*)d_in[7];
  const u16* Dskip  = (const u16*)d_in[8];
  const u16* w1     = (const u16*)d_in[10];
  const u16* b1     = (const u16*)d_in[11];
  const u16* w2     = (const u16*)d_in[12];
  char* ws = (char*)d_ws;
  S4Params P{A_re, A_im, B_re, B_im, C_re, C_im, log_dt};
  prep_kernel<<<PREP_BLOCKS, 256, 0, stream>>>(x, P, w1, w2, ws);
  gemm1_kernel<<<256, 768, 0, stream>>>(w1, b1, ws);
  gemm2_kernel<<<256, 768, 0, stream>>>(w2, Dskip, ws);
  final_kernel<<<288, 256, 0, stream>>>(x, Dskip, ws, (u16*)d_out);
}
```

```cpp
#include <hip/hip_runtime.h>
#include <hip/hip_bf16.h>
#include <stdint.h>

typedef unsigned short u16;
typedef unsigned int u32;
using s16x8 = __attribute__((ext_vector_type(8))) short;
using s16x4 = __attribute__((ext_vector_type(4))) short;
using f32x4 = __attribute__((ext_vector_type(4))) float;
using u32x4 = __attribute__((ext_vector_type(4))) unsigned int;
using u32x2 = __attribute__((ext_vector_type(2))) unsigned int;
typedef u32x4 __attribute__((aligned(4))) u32x4_a4;

#define DI __device__ __forceinline__

constexpr int L_ = 2048, D_ = 576, N_ = 128, F_ = 4608;
constexpr int XT_PITCH = 2048 + 128;
constexpr int KX_NW = 1032;
constexpr int LUT_LO = 0x3A80, LUT_HI = 0x4300;
constexpr int LUT_ENTRIES = 8192;

constexpr size_t WS_CNT  = 0;
constexpr size_t WS_XN   = 1024;
constexpr size_t WS_XNT  = WS_XN + (size_t)L_ * D_ * 2;
constexpr size_t WS_KX   = WS_XNT + (size_t)D_ * XT_PITCH * 2;
constexpr size_t WS_LUT  = WS_KX + (size_t)D_ * 2 * KX_NW * 4;
constexpr size_t WS_YAT  = WS_LUT + LUT_ENTRIES * 4;
constexpr size_t WS_ACT  = WS_YAT + (size_t)D_ * L_ * 2;
constexpr size_t WS_PART = WS_ACT + (size_t)L_ * F_ * 2;
constexpr size_t WS_YP   = WS_PART + (size_t)4 * L_ * D_ * 4;
constexpr size_t WS_W1P  = WS_YP + (size_t)64 * 4 * L_ * 4;
constexpr size_t WS_W2P  = WS_W1P + (size_t)288 * 18432;

DI float bf2f(u16 b) { return __uint_as_float(((u32)b) << 16); }
DI u16 f2bf(float f) {
  __hip_bfloat16 h = __float2bfloat16(f);
  return *reinterpret_cast<u16*>(&h);
}
DI float rbf(float f) { return bf2f(f2bf(f)); }
typedef __bf16 bf16x2_t __attribute__((ext_vector_type(2)));
using f32x2 = __attribute__((ext_vector_type(2))) float;
DI u32 pack2(float lo, float hi) {
  f32x2 v = {lo, hi};
  return __builtin_bit_cast(u32, __builtin_convertvector(v, bf16x2_t));
}
DI float lo16(u32 v) { return __uint_as_float(v << 16); }
DI float hi16(u32 v) { return __uint_as_float(v & 0xffff0000u); }
DI void st16_wt(void* p, u32x4 v) { asm volatile("global_store_dwordx4 %0, %1, off sc1\n\ts_nop 1" ::"v"(p), "v"(v) : "memory"); }
DI void st8_wt(void* p, u32x2 v) { asm volatile("global_store_dwordx2 %0, %1, off sc1\n\ts_nop 1" ::"v"(p), "v"(v) : "memory"); }

DI int blk_swz(int i16) { return (-(i16 >> 2)) & 3; }

DI void mfma_ip(f32x4& acc, const s16x8& a, const s16x8& b) {
  asm volatile("s_nop 1\n\tv_mfma_f32_16x16x32_bf16 %0, %1, %2, %0" : "+v"(acc) : "v"(a), "v"(b));
}
DI void mfma_drain() { asm volatile("s_nop 15\n\ts_nop 15" ::: "memory"); }

DI void cexp_pos(float lr2, float lh, float pos, float& er, float& ei) {
  const float a0 = lr2 * pos, f = __builtin_amdgcn_fractf(lh * pos);
  float mag;
  asm volatile("v_exp_f32 %2, %3\n\tv_cos_f32 %0, %4\n\tv_sin_f32 %1, %4\n\ts_nop 1\n\tv_mul_f32 %0, %2, %0\n\tv_mul_f32 %1, %2, %1"
               : "=&v"(er), "=&v"(ei), "=&v"(mag) : "v"(a0), "v"(f));
}
DI int kg_row_pos(int row) {
  if (row < 16) return 128 * (row >> 3) + (row & 7);
  if (row < 24) { int r = row - 16; return 256 + 128 * (r >> 2) + 2 * (r & 3); }
  if (row < 32) { int r = row - 24; return 512 + 128 * (r >> 1) + 4 * (r & 1); }
  if (row < 40) return 1024 + 128 * (row - 32);
  if (row == 40) return 2048;
  return 0;
}
struct KIdxTab { u32 v[1088]; };
constexpr int kg_bf16_round(int l) {
  int sft = l >= 1024 ? 3 : l >= 512 ? 2 : l >= 256 ? 1 : 0;
  if (sft == 0) return l;
  int half = 1 << (sft - 1), q = l >> sft;
  return ((l + half - 1 + (q & 1)) >> sft) << sft;
}
constexpr int kg_pos_index(int p) {
  int sg = p >= 2048 ? 4 : p >= 1024 ? 3 : p >= 512 ? 2 : p >= 256 ? 1 : 0;
  int base = sg ? (128 << sg) : 0, rowbase = sg ? 8 + 8 * sg : 0;
  int q = p - base;
  int row = rowbase + (q >> 7) * (8 >> sg) + ((q & 7) >> sg);
  return row * 16 + ((q & 127) >> 3);
}
constexpr KIdxTab make_kidx() {
  KIdxTab t{};
  for (int w = 0; w < 1088; ++w) {
    int l0 = 2047 - 2 * w, l1 = 2046 - 2 * w;
    u32 i0 = l0 >= 0 ? kg_pos_index(kg_bf16_round(l0)) : 768, i1 = l1 >= 0 ? kg_pos_index(kg_bf16_round(l1)) : 768;
    t.v[w] = (i0 * 4) | ((i1 * 4) << 16);
  }
  return t;
}
__device__ const KIdxTab kIdxTab = make_kidx();

struct S4Params { const u16 *A_re, *A_im, *B_re, *B_im, *C_re, *C_im, *log_dt; };
struct KgFrag { u32x4 Bh, Bl, Ah[3], Al[3]; };

DI void kg_load_params(const S4Params& P, int d, int n0, u32x4 (&v)[6]) {
  const size_t o = (size_t)d * N_ + n0;
  v[0] = *(const u32x4*)(P.A_re + o); v[1] = *(const u32x4*)(P.A_im + o);
  v[2] = *(const u32x4*)(P.B_re + o); v[3] = *(const u32x4*)(P.B_im + o);
  v[4] = *(const u32x4*)(P.C_re + o); v[5] = *(const u32x4*)(P.C_im + o);
}
DI void kg_mode(const u32x4 (&v)[6], int g, float dt, const float (&posA)[3], float posB, KgFrag& f) {
  const int e = g >> 1, gg = g & 3;
  const float lr = dt * ((g & 1) ? hi16(v[0][e]) : lo16(v[0][e])), li = dt * ((g & 1) ? hi16(v[1][e]) : lo16(v[1][e]));
  const float br = dt * ((g & 1) ? hi16(v[2][e]) : lo16(v[2][e])), bi = dt * ((g & 1) ? hi16(v[3][e]) : lo16(v[3][e]));
  const float cr = (g & 1) ? hi16(v[4][e]) : lo16(v[4][e]), ci = (g & 1) ? hi16(v[5][e]) : lo16(v[5][e]);
  const float kr = cr * br - ci * bi, ki = cr * bi + ci * br;
  const float lr2 = lr * 1.44269504088896340736f;
  const float lh = li * 0.15915494309189535f;
  float zr, zi;
  cexp_pos(lr2, lh, posB, zr, zi);
  {
    const float xr = kr * zr - ki * zi, xi = -(kr * zi + ki * zr);
    const u32 h = pack2(xr, xi);
    f.Bh[gg] = h; f.Bl[gg] = pack2(xr - lo16(h), xi - hi16(h));
  }
#pragma unroll
  for (int t = 0; t < 3; ++t) {
    float er, ei;
    cexp_pos(lr2, lh, posA[t], er, ei);
    const u32 h = pack2(er, ei);
    f.Ah[t][gg] = h; f.Al[t][gg] = pack2(er - lo16(h), ei - hi16(h));
  }
}
DI void kg_mma(const KgFrag& f, f32x4 (&acc)[3]) {
#pragma unroll
  for (int t = 0; t < 3; ++t) {
    mfma_ip(acc[t], __builtin_bit_cast(s16x8, f.Al[t]), __builtin_bit_cast(s16x8, f.Bh));
    mfma_ip(acc[t], __builtin_bit_cast(s16x8, f.Ah[t]), __builtin_bit_cast(s16x8, f.Bl));
    mfma_ip(acc[t], __builtin_bit_cast(s16x8, f.Ah[t]), __builtin_bit_cast(s16x8, f.Bh));
  }
}
template <int NTHR> DI void kg_gather_store(const float* kv, int d, int t, char* __restrict__ ws) {
  u32* kx = (u32*)(ws + WS_KX) + (size_t)d * 2 * KX_NW;
#pragma unroll
  for (int i = 0; i < 256 / NTHR; ++i) {
    const int w = (i * NTHR + t) * 4;
    const u32x4 ti = *(const u32x4*)(kIdxTab.v + w);
    u32x4 hi, lo;
#pragma unroll
    for (int j = 0; j < 4; ++j) {
      const float k0 = *(const float*)((const char*)kv + (ti[j] & 0xffffu)), k1 = *(const float*)((const char*)kv + (ti[j] >> 16));
      const u32 h = pack2(k0, k1);
      hi[j] = h; lo[j] = pack2(k0 - lo16(h), k1 - hi16(h));
    }
    *(u32x4*)(kx + w) = hi;
    *(u32x4*)(kx + KX_NW + w) = lo;
  }
  if (t < 8) {
    const u32 tt = kIdxTab.v[1024 + t];
    const float k0 = *(const float*)((const char*)kv + (tt & 0xffffu)), k1 = *(const float*)((const char*)kv + (tt >> 16));
    const u32 h = pack2(k0, k1);
    kx[1024 + t] = h; kx[KX_NW + 1024 + t] = pack2(k0 - lo16(h), k1 - hi16(h));
  }
}

typedef __attribute__((address_space(3))) s16x4 lds_s16x4;
template <int N> DI void wait_vm() { asm volatile("s_waitcnt vmcnt(%0)" ::"n"(N) : "memory"); }
DI void raw_barrier() { asm volatile("s_waitcnt lgkmcnt(0)" ::: "memory"); __builtin_amdgcn_s_barrier(); }
#define SCHED0() __builtin_amdgcn_sched_barrier(0)
DI void glds16(const void* sbase, u32 voff, u32 lds_addr) {
  u32 keep;
  asm volatile("s_mov_b32 %0, m0\n\ts_mov_b32 m0, %3\n\ts_nop 0\n\tglobal_load_lds_dwordx4 %1, %2\n\ts_mov_b32 m0, %0"
               : "=&s"(keep) : "v"(voff), "s"(sbase), "s"(lds_addr) : "memory");
}
DI u32 lds_addr_of(const char* p) { return (u32)(size_t)(const __attribute__((address_space(3))) char*)p; }

constexpr int LN_ROWS = 16;
constexpr int LN_BLOCKS = L_ / LN_ROWS;
constexpr int LUT_BLOCKS = 32;
constexpr int KG_BLOCKS = D_;
constexpr int PK_BLOCKS = 576;
constexpr int PREP_BLOCKS = KG_BLOCKS + LN_BLOCKS + LUT_BLOCKS + PK_BLOCKS;
constexpr int LN_PITCH = D_ + 8;

__global__ __launch_bounds__(256) void prep_kernel(const u16* __restrict__ x, S4Params P, const u16* __restrict__ w1,
                                                   const u16* __restrict__ w2, char* __restrict__ ws) {
  __shared__ __attribute__((aligned(1024))) char smem[LN_ROWS * LN_PITCH * 2];
  const int tid = threadIdx.x;
  const int vbx = blockIdx.x < KG_BLOCKS ? blockIdx.x + LN_BLOCKS : (blockIdx.x < KG_BLOCKS + LN_BLOCKS ? blockIdx.x - KG_BLOCKS : blockIdx.x);
  if (vbx >= KG_BLOCKS + LN_BLOCKS + LUT_BLOCKS) {
    const int tb = vbx - (KG_BLOCKS + LN_BLOCKS + LUT_BLOCKS);
    const int lane = tid & 63, wid = __builtin_amdgcn_readfirstlane(tid >> 6);
    const bool is2 = tb >= 288;
    const int tt = is2 ? tb - 288 : tb;
    const int nt = is2 ? tt / 72 : tt / 9, kt = is2 ? tt % 72 : tt % 9;
    const int ldb = is2 ? D_ : F_;
    const char* src = (const char*)((is2 ? w2 : w1) + (size_t)kt * 64 * ldb + nt * 144);
    const u32 l0 = __builtin_amdgcn_readfirstlane(lds_addr_of(smem));
#pragma unroll
    for (int i = 0; i < 5; ++i) {
      const int ins = wid + 4 * i;
      if (ins < 18) {
        const int Lc = ins * 64 + lane, k = Lc / 18, c8 = Lc % 18;
        glds16(src, (u32)(k * ldb + c8 * 8) * 2u, l0 + ins * 1024);
      }
    }
    wait_vm<0>();
    raw_barrier();
    const int i16 = lane & 15, kq = lane >> 4;
    const int rd = (4 * kq + (i16 >> 2)) * 288 + (i16 & 3) * 8;
    char* dst = ws + (is2 ? WS_W2P : WS_W1P) + (size_t)tt * 18432 + lane * 16;
#pragma unroll
    for (int i = 0; i < 5; ++i) {
      const int fr = wid + 4 * i;
      if (fr < 18) {
        const int ks = fr / 9, nb = fr % 9;
        s16x4 lo = __builtin_amdgcn_ds_read_tr16_b64_v4i16((lds_s16x4*)(smem + rd + ks * 32 * 288 + nb * 32));
        s16x4 hi = __builtin_amdgcn_ds_read_tr16_b64_v4i16((lds_s16x4*)(smem + rd + ks * 32 * 288 + 16 * 288 + nb * 32));
        const s16x8 v = s16x8{lo[0], lo[1], lo[2], lo[3], hi[0], hi[1], hi[2], hi[3]};
        st16_wt(dst + fr * 1024, __builtin_bit_cast(u32x4, v));
      }
    }
    return;
  }
  if (vbx >= LN_BLOCKS && vbx < LN_BLOCKS + KG_BLOCKS) {
    const int d = vbx - LN_BLOCKS, lane = tid & 63, wid = tid >> 6;
    const int i16 = lane & 15, kq = lane >> 4;
    float* red = (float*)smem;
    float* kv = red + 4 * 768;
    u32x4 v[6];
    kg_load_params(P, d, wid * 32 + kq * 8, v);
    const float dt = rbf(expf(bf2f(P.log_dt[d])));
    f32x4 acc[3] = {f32x4{0, 0, 0, 0}, f32x4{0, 0, 0, 0}, f32x4{0, 0, 0, 0}};
    float posA[3];
#pragma unroll
    for (int t = 0; t < 3; ++t) posA[t] = (float)kg_row_pos(16 * t + i16);
    const float posB = (float)(8 * i16);
    KgFrag f;
#pragma unroll
    for (int g = 0; g < 8; ++g) { kg_mode(v, g, dt, posA, posB, f); if ((g & 3) == 3) kg_mma(f, acc); }
    mfma_drain();
#pragma unroll
    for (int t = 0; t < 3; ++t)
#pragma unroll
      for (int j = 0; j < 4; ++j) red[wid * 768 + (16 * t + 4 * kq + j) * 16 + i16] = acc[t][j];
    __syncthreads();
#pragma unroll
    for (int i = 0; i < 3; ++i) { int e = tid + 256 * i; kv[e] = (red[e] + red[768 + e]) + (red[2 * 768 + e] + red[3 * 768 + e]); }
    if (tid == 0) kv[768] = 0.f;
    __syncthreads();
    kg_gather_store<256>(kv, d, tid, ws);
    return;
  }
  const int bid = vbx < LN_BLOCKS ? vbx : vbx - KG_BLOCKS;
  if (bid < LN_BLOCKS) {
    u16* tile = (u16*)smem;
    const int t0 = bid * LN_ROWS;
    const int r = tid >> 4, j16 = tid & 15;
    u32x2 v[9];
#pragma unroll
    for (int i = 0; i < 9; ++i) v[i] = *(const u32x2*)(x + (size_t)(t0 + r) * D_ + (j16 + 16 * i) * 4);
    float s = 0.f;
#pragma unroll
    for (int i = 0; i < 9; ++i)
#pragma unroll
      for (int e = 0; e < 2; ++e) s += lo16(v[i][e]) + hi16(v[i][e]);
    s += __shfl_xor(s, 1); s += __shfl_xor(s, 2); s += __shfl_xor(s, 4); s += __shfl_xor(s, 8);
    const float mean = s * (1.0f / 576.0f);
    float q = 0.f;
#pragma unroll
    for (int i = 0; i < 9; ++i)
#pragma unroll
      for (int e = 0; e < 2; ++e) { float a = lo16(v[i][e]) - mean, b = hi16(v[i][e]) - mean; q += a * a + b * b; }
    q += __shfl_xor(q, 1); q += __shfl_xor(q, 2); q += __shfl_xor(q, 4); q += __shfl_xor(q, 8);
    const float var = q * (1.0f / 576.0f);
    const float mb = rbf(mean), vb = rbf(var);
    const float den = rbf(sqrtf(rbf(vb + 1e-5f)));
    const float rden = 1.0f / den;
    auto qdiv = [&](float a) { float qq = a * rden; return __builtin_fmaf(__builtin_fmaf(-qq, den, a), rden, qq); };
#pragma unroll
    for (int i = 0; i < 9; ++i) {
      u32x2 o;
#pragma unroll
      for (int e = 0; e < 2; ++e) {
        const u32 c = pack2(lo16(v[i][e]) - mb, hi16(v[i][e]) - mb);
        o[e] = pack2(qdiv(lo16(c)), qdiv(hi16(c)));
      }
      *(u32x2*)(tile + r * LN_PITCH + (j16 + 16 * i) * 4) = o;
    }
    __syncthreads();
    char* xn_g = ws + WS_XN + (size_t)bid * 18 * 1024;
#pragma unroll
    for (int i = 0; i < 5; ++i) {
      const int c = tid + 256 * i;
      if (c < 1152) {
        const int P = c >> 6, w = c & 63;
        const int i16 = w >> 2, kq = (w & 3) ^ blk_swz(w >> 2);
        const u16* src = tile + i16 * LN_PITCH + 32 * P + 4 * kq;
        u32x2 a = *(const u32x2*)src, b = *(const u32x2*)(src + 16);
        *(u32x4*)(xn_g + (size_t)c * 16) = u32x4{a[0], a[1], b[0], b[1]};
      }
    }
    u16* xt_g = (u16*)(ws + WS_XNT);
#pragma unroll
    for (int i = 0; i < 5; ++i) {
      const int c = tid + 256 * i;
      if (c < 1152) {
        const int d = c >> 1, tq = c & 1;
        u32x4 o;
#pragma unroll
        for (int j = 0; j < 4; ++j) {
          u32 lo = tile[(tq * 8 + 2 * j) * LN_PITCH + d], hi = tile[(tq * 8 + 2 * j + 1) * LN_PITCH + d];
          o[j] = lo | (hi << 16);
        }
        *(u32x4*)(xt_g + (size_t)d * XT_PITCH + 128 + t0 + tq * 8) = o;
      }
    }
    if (tid < 72) {
      int c = bid * 72 + tid;
      *(u32x4*)(xt_g + (size_t)(c >> 4) * XT_PITCH + (c & 15) * 8) = u32x4{0u, 0u, 0u, 0u};
    }
  } else {
    const int e8 = (bid - LN_BLOCKS) * 256 + tid;
    const int sign = e8 >> 12, e = e8 & 4095;
    float* lut = (float*)(ws + WS_LUT);
    int a = LUT_LO + e;
    if (a > LUT_HI) a = LUT_HI;
    float h = bf2f((u16)(a | (sign << 15)));
    float ex = rbf(expf(-fabsf(h)));
    float lp = rbf(log1pf(ex));
    float sp = rbf(lp + (h > 0.f ? h : 0.f));
    float th = tanhf(sp);
    lut[e8] = rbf(th);
  }
}

constexpr int BIAS_LDS = 51200;
constexpr int LUT_LDS = 61440;
constexpr int CV_ST = 14848;
constexpr int CV_LDS = CV_ST + 8192;

DI void conv_stage_part(char* st, int d, int lane, int first, int step, const char* __restrict__ ws) {
  const char* ug = ws + WS_XNT + (size_t)d * XT_PITCH * 2;
  const char* kg = ws + WS_KX + (size_t)d * 2 * KX_NW * 4;
  const u32 l0 = __builtin_amdgcn_readfirstlane(lds_addr_of(st));
#pragma unroll
  for (int i = 0; i < 14; ++i) {
    if (i % step == first) {
      if (i < 5) glds16(ug + i * 1024, (u32)lane * 16u, l0 + i * 1024);
      else if (i < 13) glds16(kg + (i - 5) * 1024, (u32)lane * 16u, l0 + 5120 + (i - 5) * 1024);
      else glds16(kg + 8192, (u32)(lane & 3) * 16u, l0 + 5120 + 8192);
    }
  }
}
DI s16x8 conv_bfrag(const u32* kp, u32 sh) {
  const u32 e0 = kp[0], e1 = kp[1], e2 = kp[2], e3 = kp[3], e4 = kp[4];
  const u32x4 v = {__builtin_amdgcn_alignbyte(e1, e0, sh), __builtin_amdgcn_alignbyte(e2, e1, sh),
                   __builtin_amdgcn_alignbyte(e3, e2, sh), __builtin_amdgcn_alignbyte(e4, e3, sh)};
  return __builtin_bit_cast(s16x8, v);
}
DI void conv_hlsum(const f32x4& a, int hl, float& o0, float& o1) {
  const float s0 = hl ? a[0] : a[2], s1 = hl ? a[1] : a[3];
  const float k0 = hl ? a[2] : a[0], k1 = hl ? a[3] : a[1];
  o0 = k0 + __builtin_bit_cast(float, __builtin_amdgcn_update_dpp(0, __builtin_bit_cast(int, s0), 0x128, 0xf, 0xf, false));
  o1 = k1 + __builtin_bit_cast(float, __builtin_amdgcn_update_dpp(0, __builtin_bit_cast(int, s1), 0x128, 0xf, 0xf, false));
}

DI void s4_side_gemm1(char* smem, int bid, int j, int lane, const char* __restrict__ Ag, const char* __restrict__ Bg,
                      const u16* __restrict__ biasg, char* __restrict__ ws) {
  const int i16 = lane & 15, kq = lane >> 4;
  {
    constexpr int STAGE = 51200;
    const u32 lds0 = __builtin_amdgcn_readfirstlane(lds_addr_of(smem));
    const u32 lo = (u32)lane * 16u;
    auto stage = [&](int kt, int buf) {
      const u32 sb = lds0 + buf * STAGE;
#pragma unroll
      for (int i = 0; i < 8; ++i) {
        const int bb = j + 4 * i;
        glds16(Ag + ((size_t)(bb >> 1) * 18 + 2 * kt + (bb & 1)) * 1024, lo, sb + bb * 1024);
      }
      const char* bk = Bg + (size_t)kt * 18432;
#pragma unroll
      for (int i = 0; i < 4; ++i) glds16(bk + (j + 4 * i) * 1024, lo, sb + 32768 + (j + 4 * i) * 1024);
      if (j < 2) glds16(bk + (j + 16) * 1024, lo, sb + 32768 + (j + 16) * 1024);
    };
    stage(0, 1); stage(1, 2);
    if (j < 2) wait_vm<13>(); else wait_vm<12>();
    __builtin_amdgcn_s_barrier();
#pragma unroll 1
    for (int kt = 0; kt < 7; ++kt) {
      stage(kt + 2, kt % 3);
      if (j < 2) wait_vm<13>(); else wait_vm<12>();
      __builtin_amdgcn_s_barrier();
    }
#pragma unroll
    for (int i = 0; i < 8; ++i) glds16(ws + WS_LUT + (j * 8 + i) * 1024, lo, lds0 + LUT_LDS + (j * 8 + i) * 1024);
#pragma unroll
    for (int i = 0; i < 2; ++i) glds16(biasg, lo < 288u ? lo : 272u, lds0 + BIAS_LDS + (j * 2 + i) * 1024);
    wait_vm<10>();
    __builtin_amdgcn_s_barrier();
    wait_vm<0>();
    __builtin_amdgcn_s_barrier();
  }
  const int dx = 512 + (bid >> 2), g = bid & 3;
  char* st = smem + (j < 3 ? j * CV_ST : 102400);
  conv_stage_part(st, dx, lane, 0, 1, ws);
  wait_vm<0>();
  {
    const int rho = i16 & 7, hl = i16 >> 3;
    const char* us = st + 256;
    const u32 sh = ((1 - rho) & 1) * 2;
    const u32* ks = (const u32*)(st + 5120) + hl * KX_NW + ((2047 - 8 * (4 * g + kq) - rho) >> 1);
    f32x4 ya[4] = {f32x4{0, 0, 0, 0}, f32x4{0, 0, 0, 0}, f32x4{0, 0, 0, 0}, f32x4{0, 0, 0, 0}};
    const char* fb = us + (16 * j - 4 * g + i16 - kq) * 16;
#pragma unroll
    for (int q = 0; q < 16; ++q) {
      const s16x8 B = conv_bfrag(ks - 64 * q, sh);
#pragma unroll
      for (int r = 0; r < 4; ++r) {
        if (4 * r + 3 >= q) {
          if (j + 4 * r >= q) {
            const s16x8 F = *(const s16x8*)(fb + (4 * r - q) * 256);
            mfma_ip(ya[r], F, B);
          }
        }
      }
    }
    mfma_drain();
    float* yp = (float*)(ws + WS_YP) + ((size_t)(dx - 512) * 4 + g) * L_;
#pragma unroll
    for (int r = 0; r < 4; ++r) {
      float o0, o1;
      conv_hlsum(ya[r], hl, o0, o1);
      float* q = yp + 128 * (j + 4 * r) + 32 * kq + 16 * hl + rho;
      q[0] = o0; q[8] = o1;
    }
  }
}

DI void s4_side_gemm2(char* smem, char* scratch, int bid, int j, int lane, const char* __restrict__ Ag, const char* __restrict__ Bg,
                      const u16* __restrict__ Dskip, char* __restrict__ ws) {
  const int d = 2 * bid + (j >> 1), gh = j & 1;
  char* st = scratch + (j >> 1) * CV_LDS;
  conv_stage_part(st, d, lane, gh, 2, ws);
  const float dsk = bf2f(Dskip[d]);
  constexpr int STAGE2 = 34816;
  const u32 lds0 = __builtin_amdgcn_readfirstlane(lds_addr_of(smem));
  const u32 lo = (u32)lane * 16u;
  auto stage = [&](int kt) {
    const u32 sb = lds0 + (kt % 3) * STAGE2;
#pragma unroll
    for (int i = 0; i < 4; ++i) {
      const int bb = j + 4 * i;
      glds16(Ag + ((size_t)(bb >> 1) * 144 + 2 * kt + (bb & 1)) * 1024, lo, sb + bb * 1024);
    }
    const char* bk = Bg + (size_t)kt * 18432;
#pragma unroll
    for (int i = 0; i < 4; ++i) glds16(bk + (j + 4 * i) * 1024, lo, sb + 16384 + (j + 4 * i) * 1024);
    if (j < 2) glds16(bk + (j + 16) * 1024, lo, sb + 16384 + (j + 16) * 1024);
  };
  int jn = 0;
  auto step = [&]() {
    if (jn + 2 < 18) { if (j < 2) wait_vm<9>(); else wait_vm<8>(); }
    else wait_vm<0>();
    __builtin_amdgcn_s_barrier();
    ++jn;
    if (jn + 2 < 18) stage(jn + 2);
  };
  stage(0); stage(1);
  if (j < 2) wait_vm<9>(); else wait_vm<8>();
  __builtin_amdgcn_s_barrier();
  stage(2);
  step();
  const int i16 = lane & 15, kq = lane >> 4;
  const int rho = i16 & 7, hl = i16 >> 3;
  const char* us = st + 256;
  const u32 sh = ((1 - rho) & 1) * 2;
  f32x4 acc[16];
#pragma unroll
  for (int R = 0; R < 16; ++R) acc[R] = f32x4{0, 0, 0, 0};
#pragma unroll
  for (int gi = 0; gi < 2; ++gi) {
    const int g = 2 * gh + gi;
    s16x8 F[16];
#pragma unroll
    for (int dl = 0; dl < 16; ++dl) F[dl] = *(const s16x8*)(us + (16 * dl - 4 * g + i16 - kq) * 16);
    const u32* ks = (const u32*)(st + 5120) + hl * KX_NW + ((2047 - 8 * (4 * g + kq) - rho) >> 1);
#pragma unroll
    for (int q = 0; q < 16; ++q) {
      const s16x8 B = conv_bfrag(ks - 64 * q, sh);
#pragma unroll
      for (int R = q; R < 16; ++R) mfma_ip(acc[R], F[R - q], B);
      if (q <= 4 || q == 6 || q == 8 || q == 15) { SCHED0(); step(); }
    }
  }
  mfma_drain();
  float* ybuf = (float*)(st + CV_ST);
  float o[16][2];
#pragma unroll
  for (int R = 0; R < 16; ++R) conv_hlsum(acc[R], hl, o[R][0], o[R][1]);
  float* yl = ybuf + 32 * kq + 16 * hl + rho;
  if (gh) {
#pragma unroll
    for (int R = 0; R < 8; ++R) { yl[128 * R] = o[R][0]; yl[128 * R + 8] = o[R][1]; }
  } else {
#pragma unroll
    for (int R = 8; R < 16; ++R) { yl[128 * R] = o[R][0]; yl[128 * R + 8] = o[R][1]; }
  }
  raw_barrier();
  __builtin_amdgcn_s_barrier();
  if (gh) {
#pragma unroll
    for (int R = 8; R < 16; ++R) { yl[128 * R] += o[R][0]; yl[128 * R + 8] += o[R][1]; }
  } else {
#pragma unroll
    for (int R = 0; R < 8; ++R) { yl[128 * R] = o[R][0] + yl[128 * R]; yl[128 * R + 8] = o[R][1] + yl[128 * R + 8]; }
  }
  asm volatile("s_waitcnt lgkmcnt(0)" ::: "memory");
#pragma unroll
  for (int i = 0; i < 2; ++i) {
    const int t = gh * 1024 + (i * 64 + lane) * 8;
    const s16x8 uv = *(const s16x8*)(us + t * 2);
    const f32x4 s0 = *(const f32x4*)(ybuf + t), s1 = *(const f32x4*)(ybuf + t + 4);
    float sv[8] = {s0[0], s0[1], s0[2], s0[3], s1[0], s1[1], s1[2], s1[3]};
    u32x4 ov;
#pragma unroll
    for (int jj = 0; jj < 4; ++jj) {
      float r0 = rbf(sv[2 * jj]) + rbf(bf2f((u16)uv[2 * jj]) * dsk);
      float r1 = rbf(sv[2 * jj + 1]) + rbf(bf2f((u16)uv[2 * jj + 1]) * dsk);
      ov[jj] = pack2(r0, r1);
    }
    *(u32x4*)(ws + WS_YAT + ((size_t)d * L_ + t) * 2) = ov;
  }
}

template <int MODE> struct GemmCfg {
  static constexpr int BM = MODE ? 128 : 256;
  static constexpr int NPA = MODE ? F_ / 32 : D_ / 32;
  static constexpr int LDB = MODE ? D_ : F_;
  static constexpr int A_BYTES = BM * 128;
  static constexpr int B_BYTES = 64 * 288;
  static constexpr int STAGE = A_BYTES + B_BYTES;
  static constexpr int A_IPW = BM / 64;
  static constexpr int S4_OFF = 3 * STAGE;
  static constexpr int SMEM = 3 * STAGE + (MODE ? 2 * CV_LDS : 0);
};
#define NPRE 2

template <int MODE, int NB>
DI void gemm_wave_body(char* smem, const char* __restrict__ Ag, const char* __restrict__ Bg, const char* __restrict__ lutg,
                       const u16* __restrict__ biasg,
                       int wid, int lane, int wm, int wk, int pb0  , int sblk  ,
                       f32x4 (&acc)[4][5]) {
  using C = GemmCfg<MODE>;
  const int i16 = lane & 15, kq = lane >> 4;
  const bool extraB = wid < 2;
  const u32 aoffl = (u32)lane * 16u;
  const int a_rd = (wm * 8) * 1024 + i16 * 64 + ((kq ^ blk_swz(i16)) * 16);
  const int b_rd = C::A_BYTES + lane * 16 + pb0 * 1024;
  const int b_rs = C::A_BYTES + lane * 16 + sblk * 1024;

  const u32 lds0 = __builtin_amdgcn_readfirstlane(lds_addr_of(smem));
  auto stage = [&](int kt, int buf) {
    const u32 sb = lds0 + buf * C::STAGE;
#pragma unroll
    for (int i = 0; i < C::A_IPW; ++i) {
      const int b = wid * C::A_IPW + i;
      glds16(Ag + ((size_t)(b >> 1) * C::NPA + 2 * kt + (b & 1)) * 1024, aoffl, sb + b * 1024);
    }
    const char* bk = Bg + (size_t)kt * 18432;
#pragma unroll
    for (int i = 0; i < 2; ++i) glds16(bk + (wid + 8 * i) * 1024, aoffl, sb + C::A_BYTES + (wid + 8 * i) * 1024);
    if (extraB) glds16(bk + (wid + 16) * 1024, aoffl, sb + C::A_BYTES + (wid + 16) * 1024);
  };
  auto readA = [&](s16x8 (&a)[4], int buf, int ks) {
    const char* sb = smem + buf * C::STAGE;
#pragma unroll
    for (int mi = 0; mi < 4; ++mi) a[mi] = *(const s16x8*)(sb + a_rd + (mi * 2 + ks) * 1024);
  };
  auto readB = [&](int nb, int buf, int ks) -> s16x8 {
    const char* sb = smem + buf * C::STAGE;
    return *(const s16x8*)(sb + (nb < 4 ? b_rd + nb * 1024 : b_rs) + ks * 9 * 1024);
  };
  s16x8 a0[4], a1[4], b[NB];
  auto mm = [&](s16x8 (&ac)[4], int nb) {
#pragma unroll
    for (int mi = 0; mi < 4; ++mi)
      asm volatile("v_mfma_f32_16x16x32_bf16 %0, %1, %2, %0" : "+v"(acc[mi][nb]) : "v"(b[nb]), "v"(ac[mi]));
  };
  auto sub_pre = [&](s16x8 (&ac)[4]) {
#pragma unroll
    for (int nb = 0; nb < NPRE; ++nb) mm(ac, nb);
    SCHED0();
  };
  auto sub_post = [&](s16x8 (&ac)[4], s16x8 (&an)[4], bool ld, int nbuf, int nks) {
    if (ld) {
      readA(an, nbuf, nks);
#pragma unroll
      for (int nb = 0; nb < NPRE; ++nb) b[nb] = readB(nb, nbuf, nks);
    }
    SCHED0();
#pragma unroll
    for (int nb = NPRE; nb < NB; ++nb) {
      mm(ac, nb);
      if (ld) b[nb] = readB(nb, nbuf, nks);
      SCHED0();
    }
  };
  constexpr int NW0 = C::A_IPW + 2;
#pragma unroll
  for (int mi = 0; mi < 4; ++mi)
#pragma unroll
    for (int nb = 0; nb < NB; ++nb) asm volatile("" : "+v"(acc[mi][nb]));
  if (MODE == 0) {
    raw_barrier();
    readA(a0, 1, 0);
#pragma unroll
    for (int nb = 0; nb < NB; ++nb) b[nb] = readB(nb, 1, 0);
#pragma unroll 1
    for (int o = 0; o < 3; ++o) {
#pragma unroll
      for (int u = 0; u < 3; ++u) {
        SCHED0();
        sub_pre(a0); sub_post(a0, a1, true, (u + 1) % 3, 1);
        sub_pre(a1);
        raw_barrier();
        sub_post(a1, a0, true, (u + 2) % 3, 0);
      }
    }
  } else {
    raw_barrier();
    readA(a0, 0, wk);
#pragma unroll
    for (int nb = 0; nb < NB; ++nb) b[nb] = readB(nb, 0, wk);
#pragma unroll 1
    for (int o = 0; o < 3; ++o) {
#pragma unroll
      for (int u = 0; u < 6; ++u) {
        const int j = 6 * o + u;
        SCHED0();
        if (u & 1) sub_pre(a1); else sub_pre(a0);
        if (j + 1 < 18) raw_barrier();
        SCHED0();
        if (u & 1) sub_post(a1, a0, true, (u + 1) % 3, wk);
        else       sub_post(a0, a1, true, (u + 1) % 3, wk);
      }
    }
    raw_barrier();
  }
  asm volatile("s_nop 15\n\ts_nop 15" ::: "memory");
#pragma unroll
  for (int mi = 0; mi < 4; ++mi)
#pragma unroll
    for (int nb = 0; nb < NB; ++nb) asm volatile("" : "+v"(acc[mi][nb]));
}

__global__ __launch_bounds__(768) void gemm1_kernel(const u16* __restrict__ w1, const u16* __restrict__ bias, char* __restrict__ ws) {
  using C = GemmCfg<0>;
  __shared__ __attribute__((aligned(1024))) char smem[C::SMEM];
  const int tid = threadIdx.x, lane = tid & 63, wid = __builtin_amdgcn_readfirstlane(tid >> 6);
  const int bid = blockIdx.x;
  const int xcd = bid & 7, idx = bid >> 3;
  const int nt = 4 * xcd + (idx & 3), m0 = (idx >> 2) * 256;
  const int n0 = nt * 144;
  const char* Ag = ws + WS_XN + (size_t)(m0 >> 4) * C::NPA * 1024;
  const char* Bg = ws + WS_W1P + (size_t)nt * 9 * 18432;
  if (wid >= 8) { s4_side_gemm1(smem, bid, wid - 8, lane, Ag, Bg, bias + n0, ws); return; }
  __builtin_amdgcn_s_setprio(1);

  const int wn = wid >> 2;
  const int wm = wid & 3;
  const int odd = nt & 1;
  const int pb0 = odd + 4 * wn;
  const int sblk = odd ? 0 : 8;
  f32x4 acc[4][5];
#pragma unroll
  for (int mi = 0; mi < 4; ++mi)
#pragma unroll
    for (int nb = 0; nb < 5; ++nb) acc[mi][nb] = f32x4{0, 0, 0, 0};
  const int i16 = lane & 15, kq = lane >> 4;
  const char* lutg = ws + WS_LUT;
  if (wn == 0) gemm_wave_body<0, 5>(smem, Ag, Bg, lutg, bias + n0, wid, lane, wm, 0, pb0, sblk, acc);
  else         gemm_wave_body<0, 4>(smem, Ag, Bg, lutg, bias + n0, wid, lane, wm, 0, pb0, sblk, acc);
  f32x2 bf_[5][2];
#pragma unroll
  for (int nb = 0; nb < 5; ++nb) {
    const int blk = nb < 4 ? pb0 + nb : sblk;
    const u32x2 bb = *(const u32x2*)(smem + BIAS_LDS + wid * 1024 + (blk * 16 + 4 * kq) * 2);
#pragma unroll
    for (int e = 0; e < 2; ++e) { bf_[nb][e] = f32x2{lo16(bb[e]), hi16(bb[e])}; asm volatile("" : "+v"(bf_[nb][e])); }
  }
  const char* lut = smem + (LUT_LDS - LUT_LO * 4);
  char* act = ws + WS_ACT;
  const int inblk = i16 * 64 + ((kq ^ blk_swz(i16)) * 16);
#pragma unroll
  for (int mi = 0; mi < 4; ++mi) {
    float hf[5][4], th[5][4];
#pragma unroll
    for (int nb = 0; nb < 5; ++nb) {
      if (nb < 4 || wn == 0) {
#pragma unroll
        for (int e = 0; e < 2; ++e) {
          u32 a = pack2(acc[mi][nb][2 * e], acc[mi][nb][2 * e + 1]);
          u32 h = pack2(lo16(a) + bf_[nb][e][0], hi16(a) + bf_[nb][e][1]);
          hf[nb][2 * e] = lo16(h); hf[nb][2 * e + 1] = hi16(h);
        }
#pragma unroll
        for (int j = 0; j < 4; ++j) {
          const u32 u = __float_as_uint(hf[nb][j]);
          const float m = __builtin_amdgcn_fmed3f(__builtin_fabsf(hf[nb][j]), 0.0009765625f, 128.0f);
          u32 o;
          asm("v_lshl_add_u32 %0, %1, 14, %2" : "=v"(o) : "v"(u >> 31), "v"(__float_as_uint(m) >> 14));
          th[nb][j] = *(const float*)(lut + o);
        }
      }
    }
    const size_t mbg = (size_t)((m0 >> 4) + wm * 4 + mi);
    u32 r[5][2];
#pragma unroll
    for (int nb = 0; nb < 5; ++nb)
      if (nb < 4 || wn == 0) {
#pragma unroll
        for (int e = 0; e < 2; ++e) r[nb][e] = pack2(hf[nb][2 * e] * th[nb][2 * e], hf[nb][2 * e + 1] * th[nb][2 * e + 1]);
      }
#pragma unroll
    for (int pr = 0; pr < 2; ++pr) {
      const int Pb = (n0 + (pb0 + 2 * pr) * 16) >> 5;
      st16_wt(act + (mbg * 144 + Pb) * 1024 + inblk, u32x4{r[2 * pr][0], r[2 * pr][1], r[2 * pr + 1][0], r[2 * pr + 1][1]});
    }
    if (wn == 0) {
      const int nb16 = (n0 >> 4) + sblk;
      st8_wt(act + (mbg * 144 + (nb16 >> 1)) * 1024 + inblk + (nb16 & 1) * 8, u32x2{r[4][0], r[4][1]});
    }
  }

}

__global__ __launch_bounds__(768) void gemm2_kernel(const u16* __restrict__ w2, const u16* __restrict__ Dskip, char* __restrict__ ws) {
  using C = GemmCfg<1>;
  __shared__ __attribute__((aligned(1024))) char smem[C::SMEM];
  const int tid = threadIdx.x, lane = tid & 63, wid = __builtin_amdgcn_readfirstlane(tid >> 6);
  const int bid = blockIdx.x;
  const int xcd = bid & 7, idx = bid >> 3;
  const int ksplit = xcd >> 1, mt = (xcd & 1) * 8 + (idx >> 2), nt = idx & 3;
  const int m0 = mt * 128, n0 = nt * 144;
  const char* Ag = ws + WS_ACT + ((size_t)(m0 >> 4) * C::NPA + ksplit * 36) * 1024;
  const char* Bg = ws + WS_W2P + ((size_t)nt * 72 + ksplit * 18) * 18432;
  if (wid >= 8) {
    __builtin_amdgcn_s_setprio(2);
    s4_side_gemm2(smem, smem + C::S4_OFF, bid, wid - 8, lane, Ag, Bg, Dskip, ws);

    return;
  }
  __builtin_amdgcn_s_setprio(1);

  const int wn = wid >> 2;
  const int wmv = wid & 3;
  const int wk = wmv >> 1, wm = wmv & 1;
  const int odd = nt & 1;
  const int pb0 = odd + 4 * wn;
  const int sblk = odd ? 0 : 8;
  f32x4 acc[4][5];
#pragma unroll
  for (int mi = 0; mi < 4; ++mi)
#pragma unroll
    for (int nb = 0; nb < 5; ++nb) acc[mi][nb] = f32x4{0, 0, 0, 0};
  if (wn == 0) gemm_wave_body<1, 5>(smem, Ag, Bg, nullptr, nullptr, wid, lane, wm, wk, pb0, sblk, acc);
  else         gemm_wave_body<1, 4>(smem, Ag, Bg, nullptr, nullptr, wid, lane, wm, wk, pb0, sblk, acc);

  const int i16 = lane & 15, kq = lane >> 4;
  f32x4* red = (f32x4*)smem;
  const int slot = wn * 2 + wm;
  if (wk == 1) {
#pragma unroll
    for (int mi = 0; mi < 4; ++mi)
#pragma unroll
      for (int nb = 0; nb < 5; ++nb)
        if (nb < 4 || wn == 0) red[(slot * 20 + mi * 5 + nb) * 64 + lane] = acc[mi][nb];
  }
  raw_barrier();
  char* partb = ws + WS_PART;
  constexpr size_t PSTRIDE = (size_t)L_ * D_ * 4;
  if (wk == 0) {
#pragma unroll
    for (int mi = 0; mi < 4; ++mi)
#pragma unroll
      for (int nb = 0; nb < 5; ++nb)
        if (nb < 4 || wn == 0) {
          acc[mi][nb] += red[(slot * 20 + mi * 5 + nb) * 64 + lane];
          const size_t mbg = (size_t)((m0 >> 4) + wm * 4 + mi);
          const int nbg = (n0 >> 4) + (nb < 4 ? pb0 + nb : sblk);
          st16_wt(partb + ksplit * PSTRIDE + (mbg * 36 + nbg) * 1024 + i16 * 64 + kq * 16, __builtin_bit_cast(u32x4, acc[mi][nb]));
        }
  }

}

__global__ __launch_bounds__(256) void final_kernel(const u16* __restrict__ x, const u16* __restrict__ Dskip,
                                                    const char* __restrict__ ws, u16* __restrict__ out) {
  __shared__ __attribute__((aligned(16))) u16 yt[64 * 40];
  const int tid = threadIdx.x;
  const int tt = blockIdx.x / 9, dt = blockIdx.x % 9;
  const int t0 = tt * 32, d0 = dt * 64;
  const char* part = ws + WS_PART;
  const int r = tid >> 3, ch = tid & 7;
  f32x4 p[4][2];
  {
    const int t = t0 + r, d = d0 + ch * 8;
    const size_t po = ((size_t)(t >> 4) * 36 + (d >> 4)) * 1024 + (t & 15) * 64 + ((d >> 2) & 3) * 16;
#pragma unroll
    for (int s = 0; s < 4; ++s) {
      p[s][0] = *(const f32x4*)(part + (size_t)s * L_ * D_ * 4 + po);
      p[s][1] = *(const f32x4*)(part + (size_t)s * L_ * D_ * 4 + po + 16);
    }
  }
  const u32x4 xv = *(const u32x4*)(x + (size_t)(t0 + r) * D_ + d0 + ch * 8);
  const int dd = tid >> 2, tq = tid & 3;
  u32x4 yv;
  if (dt < 8) yv = *(const u32x4*)((const u16*)(ws + WS_YAT) + (size_t)(d0 + dd) * L_ + t0 + tq * 8);
  else {
    const float* yp = (const float*)(ws + WS_YP) + (size_t)dd * 4 * L_ + t0 + tq * 8;
    f32x4 a0 = (*(const f32x4*)(yp) + *(const f32x4*)(yp + L_)) + (*(const f32x4*)(yp + 2 * L_) + *(const f32x4*)(yp + 3 * L_));
    f32x4 a1 = (*(const f32x4*)(yp + 4) + *(const f32x4*)(yp + L_ + 4)) + (*(const f32x4*)(yp + 2 * L_ + 4) + *(const f32x4*)(yp + 3 * L_ + 4));
    const u32x4 uv = *(const u32x4*)((const u16*)(ws + WS_XNT) + (size_t)(d0 + dd) * XT_PITCH + 128 + t0 + tq * 8);
    const float dsk = bf2f(Dskip[d0 + dd]);
    float sv[8] = {a0[0], a0[1], a0[2], a0[3], a1[0], a1[1], a1[2], a1[3]};
#pragma unroll
    for (int jj = 0; jj < 4; ++jj)
      yv[jj] = pack2(rbf(sv[2 * jj]) + rbf(lo16(uv[jj]) * dsk), rbf(sv[2 * jj + 1]) + rbf(hi16(uv[jj]) * dsk));
  }
  *(u32x4*)(yt + dd * 40 + tq * 8) = yv;
  __syncthreads();
  f32x4 s0 = (p[0][0] + p[1][0]) + (p[2][0] + p[3][0]);
  f32x4 s1 = (p[0][1] + p[1][1]) + (p[2][1] + p[3][1]);
  float sm[8] = {s0[0], s0[1], s0[2], s0[3], s1[0], s1[1], s1[2], s1[3]};
  u32x4 ov;
#pragma unroll
  for (int j = 0; j < 4; ++j) {
    float r2[2];
#pragma unroll
    for (int h = 0; h < 2; ++h) {
      int e = 2 * j + h;
      float xx = h ? hi16(xv[j]) : lo16(xv[j]);
      float ya = bf2f(yt[(ch * 8 + e) * 40 + r]);
      r2[h] = rbf(xx + ya) + rbf(sm[e]);
    }
    ov[j] = pack2(r2[0], r2[1]);
  }
  *(u32x4*)(out + (size_t)(t0 + r) * D_ + d0 + ch * 8) = ov;
}

extern "C" void kernel_launch(void* const* d_in, const int* in_sizes, int n_in,
                              void* d_out, int out_size, void* d_ws, size_t ws_size,
                              hipStream_t stream) {
  const u16* x      = (const u16*)d_in[0];
  const u16* A_re   = (const u16*)d_in[1];
  const u16* A_im   = (const u16*)d_in[2];
  const u16* B_re   = (const u16*)d_in[3];
  const u16* B_im   = (const u16*)d_in[4];
  const u16* C_re   = (const u16*)d_in[5];
  const u16* C_im   = (const u16*)d_in[6];
  const u16* log_dt = (const u16*)d_in[7];
  const u16* Dskip  = (const u16*)d_in[8];
  const u16* w1     = (const u16*)d_in[10];
  const u16* b1     = (const u16*)d_in[11];
  const u16* w2     = (const u16*)d_in[12];
  char* ws = (char*)d_ws;
  S4Params P{A_re, A_im, B_re, B_im, C_re, C_im, log_dt};
  prep_kernel<<<PREP_BLOCKS, 256, 0, stream>>>(x, P, w1, w2, ws);
  gemm1_kernel<<<256, 768, 0, stream>>>(w1, b1, ws);
  gemm2_kernel<<<256, 768, 0, stream>>>(w2, Dskip, ws);
  final_kernel<<<576, 256, 0, stream>>>(x, Dskip, ws, (u16*)d_out);
}
```

```cpp
#include <hip/hip_runtime.h>
#include <hip/hip_bf16.h>
#include <stdint.h>

typedef unsigned short u16;
typedef unsigned int u32;
using s16x8 = __attribute__((ext_vector_type(8))) short;
using s16x4 = __attribute__((ext_vector_type(4))) short;
using f32x4 = __attribute__((ext_vector_type(4))) float;
using u32x4 = __attribute__((ext_vector_type(4))) unsigned int;
using u32x2 = __attribute__((ext_vector_type(2))) unsigned int;
typedef u32x4 __attribute__((aligned(4))) u32x4_a4;

#define DI __device__ __forceinline__

constexpr int L_ = 2048, D_ = 576, N_ = 128, F_ = 4608;
constexpr int XT_PITCH = 2048 + 128;
constexpr int KX_NW = 1032;
constexpr int LUT_LO = 0x3A80, LUT_HI = 0x4300;
constexpr int LUT_ENTRIES = 8192;

constexpr size_t WS_CNT  = 0;
constexpr size_t WS_XN   = 1024;
constexpr size_t WS_XNT  = WS_XN + (size_t)L_ * D_ * 2;
constexpr size_t WS_KX   = WS_XNT + (size_t)D_ * XT_PITCH * 2;
constexpr size_t WS_LUT  = WS_KX + (size_t)D_ * 2 * KX_NW * 4;
constexpr size_t WS_YAT  = WS_LUT + LUT_ENTRIES * 4;
constexpr size_t WS_ACT  = WS_YAT + (size_t)D_ * L_ * 2;
constexpr size_t WS_PART = WS_ACT + (size_t)L_ * F_ * 2;
constexpr size_t WS_YP   = WS_PART + (size_t)4 * L_ * D_ * 4;
constexpr size_t WS_W1P  = WS_YP + (size_t)64 * 4 * L_ * 4;
constexpr size_t WS_W2P  = WS_W1P + (size_t)288 * 18432;

DI float bf2f(u16 b) { return __uint_as_float(((u32)b) << 16); }
DI u16 f2bf(float f) {
  __hip_bfloat16 h = __float2bfloat16(f);
  return *reinterpret_cast<u16*>(&h);
}
DI float rbf(float f) { return bf2f(f2bf(f)); }
typedef __bf16 bf16x2_t __attribute__((ext_vector_type(2)));
using f32x2 = __attribute__((ext_vector_type(2))) float;
DI u32 pack2(float lo, float hi) {
  f32x2 v = {lo, hi};
  return __builtin_bit_cast(u32, __builtin_convertvector(v, bf16x2_t));
}
DI float lo16(u32 v) { return __uint_as_float(v << 16); }
DI float hi16(u32 v) { return __uint_as_float(v & 0xffff0000u); }
DI void st16_wt(void* p, u32x4 v) { asm volatile("global_store_dwordx4 %0, %1, off sc1\n\ts_nop 1" ::"v"(p), "v"(v) : "memory"); }
DI void st8_wt(void* p, u32x2 v) { asm volatile("global_store_dwordx2 %0, %1, off sc1\n\ts_nop 1" ::"v"(p), "v"(v) : "memory"); }

DI int blk_swz(int i16) { return (-(i16 >> 2)) & 3; }

DI void mfma_ip(f32x4& acc, const s16x8& a, const s16x8& b) {
  asm volatile("s_nop 1\n\tv_mfma_f32_16x16x32_bf16 %0, %1, %2, %0" : "+v"(acc) : "v"(a), "v"(b));
}
DI void mfma_drain() { asm volatile("s_nop 15\n\ts_nop 15" ::: "memory"); }

DI void cexp_pos(float lr2, float lh, float pos, float& er, float& ei) {
  const float a0 = lr2 * pos, f = __builtin_amdgcn_fractf(lh * pos);
  float mag;
  asm volatile("v_exp_f32 %2, %3\n\tv_cos_f32 %0, %4\n\tv_sin_f32 %1, %4\n\ts_nop 1\n\tv_mul_f32 %0, %2, %0\n\tv_mul_f32 %1, %2, %1"
               : "=&v"(er), "=&v"(ei), "=&v"(mag) : "v"(a0), "v"(f));
}
DI int kg_row_pos(int row) {
  if (row < 16) return 128 * (row >> 3) + (row & 7);
  if (row < 24) { int r = row - 16; return 256 + 128 * (r >> 2) + 2 * (r & 3); }
  if (row < 32) { int r = row - 24; return 512 + 128 * (r >> 1) + 4 * (r & 1); }
  if (row < 40) return 1024 + 128 * (row - 32);
  if (row == 40) return 2048;
  return 0;
}
struct KIdxTab { u32 v[1088]; };
constexpr int kg_bf16_round(int l) {
  int sft = l >= 1024 ? 3 : l >= 512 ? 2 : l >= 256 ? 1 : 0;
  if (sft == 0) return l;
  int half = 1 << (sft - 1), q = l >> sft;
  return ((l + half - 1 + (q & 1)) >> sft) << sft;
}
constexpr int kg_pos_index(int p) {
  int sg = p >= 2048 ? 4 : p >= 1024 ? 3 : p >= 512 ? 2 : p >= 256 ? 1 : 0;
  int base = sg ? (128 << sg) : 0, rowbase = sg ? 8 + 8 * sg : 0;
  int q = p - base;
  int row = rowbase + (q >> 7) * (8 >> sg) + ((q & 7) >> sg);
  return row * 16 + ((q & 127) >> 3);
}
constexpr KIdxTab make_kidx() {
  KIdxTab t{};
  for (int w = 0; w < 1088; ++w) {
    int l0 = 2047 - 2 * w, l1 = 2046 - 2 * w;
    u32 i0 = l0 >= 0 ? kg_pos_index(kg_bf16_round(l0)) : 768, i1 = l1 >= 0 ? kg_pos_index(kg_bf16_round(l1)) : 768;
    t.v[w] = (i0 * 4) | ((i1 * 4) << 16);
  }
  return t;
}
__device__ const KIdxTab kIdxTab = make_kidx();

struct S4Params { const u16 *A_re, *A_im, *B_re, *B_im, *C_re, *C_im, *log_dt; };
struct KgFrag { u32x4 Bh, Bl, Ah[3], Al[3]; };

DI void kg_load_params(const S4Params& P, int d, int n0, u32x4 (&v)[6]) {
  const size_t o = (size_t)d * N_ + n0;
  v[0] = *(const u32x4*)(P.A_re + o); v[1] = *(const u32x4*)(P.A_im + o);
  v[2] = *(const u32x4*)(P.B_re + o); v[3] = *(const u32x4*)(P.B_im + o);
  v[4] = *(const u32x4*)(P.C_re + o); v[5] = *(const u32x4*)(P.C_im + o);
}
DI void kg_mode(const u32x4 (&v)[6], int g, float dt, const float (&posA)[3], float posB, KgFrag& f) {
  const int e = g >> 1, gg = g & 3;
  const float lr = dt * ((g & 1) ? hi16(v[0][e]) : lo16(v[0][e])), li = dt * ((g & 1) ? hi16(v[1][e]) : lo16(v[1][e]));
  const float br = dt * ((g & 1) ? hi16(v[2][e]) : lo16(v[2][e])), bi = dt * ((g & 1) ? hi16(v[3][e]) : lo16(v[3][e]));
  const float cr = (g & 1) ? hi16(v[4][e]) : lo16(v[4][e]), ci = (g & 1) ? hi16(v[5][e]) : lo16(v[5][e]);
  const float kr = cr * br - ci * bi, ki = cr * bi + ci * br;
  const float lr2 = lr * 1.44269504088896340736f;
  const float lh = li * 0.15915494309189535f;
  float zr, zi;
  cexp_pos(lr2, lh, posB, zr, zi);
  {
    const float xr = kr * zr - ki * zi, xi = -(kr * zi + ki * zr);
    const u32 h = pack2(xr, xi);
    f.Bh[gg] = h; f.Bl[gg] = pack2(xr - lo16(h), xi - hi16(h));
  }
#pragma unroll
  for (int t = 0; t < 3; ++t) {
    float er, ei;
    cexp_pos(lr2, lh, posA[t], er, ei);
    const u32 h = pack2(er, ei);
    f.Ah[t][gg] = h; f.Al[t][gg] = pack2(er - lo16(h), ei - hi16(h));
  }
}
DI void kg_mma(const KgFrag& f, f32x4 (&acc)[3]) {
#pragma unroll
  for (int t = 0; t < 3; ++t) {
    mfma_ip(acc[t], __builtin_bit_cast(s16x8, f.Al[t]), __builtin_bit_cast(s16x8, f.Bh));
    mfma_ip(acc[t], __builtin_bit_cast(s16x8, f.Ah[t]), __builtin_bit_cast(s16x8, f.Bl));
    mfma_ip(acc[t], __builtin_bit_cast(s16x8, f.Ah[t]), __builtin_bit_cast(s16x8, f.Bh));
  }
}
template <int NTHR> DI void kg_gather_store(const float* kv, int d, int t, char* __restrict__ ws) {
  u32* kx = (u32*)(ws + WS_KX) + (size_t)d * 2 * KX_NW;
#pragma unroll
  for (int i = 0; i < 256 / NTHR; ++i) {
    const int w = (i * NTHR + t) * 4;
    const u32x4 ti = *(const u32x4*)(kIdxTab.v + w);
    u32x4 hi, lo;
#pragma unroll
    for (int j = 0; j < 4; ++j) {
      const float k0 = *(const float*)((const char*)kv + (ti[j] & 0xffffu)), k1 = *(const float*)((const char*)kv + (ti[j] >> 16));
      const u32 h = pack2(k0, k1);
      hi[j] = h; lo[j] = pack2(k0 - lo16(h), k1 - hi16(h));
    }
    *(u32x4*)(kx + w) = hi;
    *(u32x4*)(kx + KX_NW + w) = lo;
  }
  if (t < 8) {
    const u32 tt = kIdxTab.v[1024 + t];
    const float k0 = *(const float*)((const char*)kv + (tt & 0xffffu)), k1 = *(const float*)((const char*)kv + (tt >> 16));
    const u32 h = pack2(k0, k1);
    kx[1024 + t] = h; kx[KX_NW + 1024 + t] = pack2(k0 - lo16(h), k1 - hi16(h));
  }
}

typedef __attribute__((address_space(3))) s16x4 lds_s16x4;
template <int N> DI void wait_vm() { asm volatile("s_waitcnt vmcnt(%0)" ::"n"(N) : "memory"); }
DI void raw_barrier() { asm volatile("s_waitcnt lgkmcnt(0)" ::: "memory"); __builtin_amdgcn_s_barrier(); }
#define SCHED0() __builtin_amdgcn_sched_barrier(0)
DI void glds16(const void* sbase, u32 voff, u32 lds_addr) {
  u32 keep;
  asm volatile("s_mov_b32 %0, m0\n\ts_mov_b32 m0, %3\n\ts_nop 0\n\tglobal_load_lds_dwordx4 %1, %2\n\ts_mov_b32 m0, %0"
               : "=&s"(keep) : "v"(voff), "s"(sbase), "s"(lds_addr) : "memory");
}
DI u32 lds_addr_of(const char* p) { return (u32)(size_t)(const __attribute__((address_space(3))) char*)p; }

constexpr int LN_ROWS = 16;
constexpr int LN_BLOCKS = L_ / LN_ROWS;
constexpr int LUT_BLOCKS = 32;
constexpr int KG_BLOCKS = D_;
constexpr int PK_BLOCKS = 576;
constexpr int PREP_BLOCKS = KG_BLOCKS + LN_BLOCKS + LUT_BLOCKS + PK_BLOCKS;
constexpr int LN_PITCH = D_ + 8;

__global__ __launch_bounds__(256) void prep_kernel(const u16* __restrict__ x, S4Params P, const u16* __restrict__ w1,
                                                   const u16* __restrict__ w2, char* __restrict__ ws) {
  __shared__ __attribute__((aligned(1024))) char smem[LN_ROWS * LN_PITCH * 2];
  const int tid = threadIdx.x;
  if (blockIdx.x >= KG_BLOCKS + LN_BLOCKS + LUT_BLOCKS) {
    const int tb = blockIdx.x - (KG_BLOCKS + LN_BLOCKS + LUT_BLOCKS);
    const int lane = tid & 63, wid = __builtin_amdgcn_readfirstlane(tid >> 6);
    const bool is2 = tb >= 288;
    const int tt = is2 ? tb - 288 : tb;
    const int nt = is2 ? tt / 72 : tt / 9, kt = is2 ? tt % 72 : tt % 9;
    const int ldb = is2 ? D_ : F_;
    const char* src = (const char*)((is2 ? w2 : w1) + (size_t)kt * 64 * ldb + nt * 144);
    const u32 l0 = __builtin_amdgcn_readfirstlane(lds_addr_of(smem));
#pragma unroll
    for (int i = 0; i < 5; ++i) {
      const int ins = wid + 4 * i;
      if (ins < 18) {
        const int Lc = ins * 64 + lane, k = Lc / 18, c8 = Lc % 18;
        glds16(src, (u32)(k * ldb + c8 * 8) * 2u, l0 + ins * 1024);
      }
    }
    wait_vm<0>();
    raw_barrier();
    const int i16 = lane & 15, kq = lane >> 4;
    const int rd = (4 * kq + (i16 >> 2)) * 288 + (i16 & 3) * 8;
    char* dst = ws + (is2 ? WS_W2P : WS_W1P) + (size_t)tt * 18432 + lane * 16;
#pragma unroll
    for (int i = 0; i < 5; ++i) {
      const int fr = wid + 4 * i;
      if (fr < 18) {
        const int ks = fr / 9, nb = fr % 9;
        s16x4 lo = __builtin_amdgcn_ds_read_tr16_b64_v4i16((lds_s16x4*)(smem + rd + ks * 32 * 288 + nb * 32));
        s16x4 hi = __builtin_amdgcn_ds_read_tr16_b64_v4i16((lds_s16x4*)(smem + rd + ks * 32 * 288 + 16 * 288 + nb * 32));
        const s16x8 v = s16x8{lo[0], lo[1], lo[2], lo[3], hi[0], hi[1], hi[2], hi[3]};
        st16_wt(dst + fr * 1024, __builtin_bit_cast(u32x4, v));
      }
    }
    return;
  }
  if (blockIdx.x >= LN_BLOCKS && blockIdx.x < LN_BLOCKS + KG_BLOCKS) {
    const int d = blockIdx.x - LN_BLOCKS, lane = tid & 63, wid = tid >> 6;
    const int i16 = lane & 15, kq = lane >> 4;
    float* red = (float*)smem;
    float* kv = red + 4 * 768;
    u32x4 v[6];
    kg_load_params(P, d, wid * 32 + kq * 8, v);
    const float dt = rbf(expf(bf2f(P.log_dt[d])));
    f32x4 acc[3] = {f32x4{0, 0, 0, 0}, f32x4{0, 0, 0, 0}, f32x4{0, 0, 0, 0}};
    float posA[3];
#pragma unroll
    for (int t = 0; t < 3; ++t) posA[t] = (float)kg_row_pos(16 * t + i16);
    const float posB = (float)(8 * i16);
    KgFrag f;
#pragma unroll
    for (int g = 0; g < 8; ++g) { kg_mode(v, g, dt, posA, posB, f); if ((g & 3) == 3) kg_mma(f, acc); }
    mfma_drain();
#pragma unroll
    for (int t = 0; t < 3; ++t)
#pragma unroll
      for (int j = 0; j < 4; ++j) red[wid * 768 + (16 * t + 4 * kq + j) * 16 + i16] = acc[t][j];
    __syncthreads();
#pragma unroll
    for (int i = 0; i < 3; ++i) { int e = tid + 256 * i; kv[e] = (red[e] + red[768 + e]) + (red[2 * 768 + e] + red[3 * 768 + e]); }
    if (tid == 0) kv[768] = 0.f;
    __syncthreads();
    kg_gather_store<256>(kv, d, tid, ws);
    return;
  }
  const int bid = blockIdx.x < LN_BLOCKS ? blockIdx.x : blockIdx.x - KG_BLOCKS;
  if (bid < LN_BLOCKS) {
    u16* tile = (u16*)smem;
    const int t0 = bid * LN_ROWS;
    const int r = tid >> 4, j16 = tid & 15;
    u32x2 v[9];
#pragma unroll
    for (int i = 0; i < 9; ++i) v[i] = *(const u32x2*)(x + (size_t)(t0 + r) * D_ + (j16 + 16 * i) * 4);
    float s = 0.f;
#pragma unroll
    for (int i = 0; i < 9; ++i)
#pragma unroll
      for (int e = 0; e < 2; ++e) s += lo16(v[i][e]) + hi16(v[i][e]);
    s += __shfl_xor(s, 1); s += __shfl_xor(s, 2); s += __shfl_xor(s, 4); s += __shfl_xor(s, 8);
    const float mean = s * (1.0f / 576.0f);
    float q = 0.f;
#pragma unroll
    for (int i = 0; i < 9; ++i)
#pragma unroll
      for (int e = 0; e < 2; ++e) { float a = lo16(v[i][e]) - mean, b = hi16(v[i][e]) - mean; q += a * a + b * b; }
    q += __shfl_xor(q, 1); q += __shfl_xor(q, 2); q += __shfl_xor(q, 4); q += __shfl_xor(q, 8);
    const float var = q * (1.0f / 576.0f);
    const float mb = rbf(mean), vb = rbf(var);
    const float den = rbf(sqrtf(rbf(vb + 1e-5f)));
    const float rden = 1.0f / den;
    auto qdiv = [&](float a) { float qq = a * rden; return __builtin_fmaf(__builtin_fmaf(-qq, den, a), rden, qq); };
#pragma unroll
    for (int i = 0; i < 9; ++i) {
      u32x2 o;
#pragma unroll
      for (int e = 0; e < 2; ++e) {
        const u32 c = pack2(lo16(v[i][e]) - mb, hi16(v[i][e]) - mb);
        o[e] = pack2(qdiv(lo16(c)), qdiv(hi16(c)));
      }
      *(u32x2*)(tile + r * LN_PITCH + (j16 + 16 * i) * 4) = o;
    }
    __syncthreads();
    char* xn_g = ws + WS_XN + (size_t)bid * 18 * 1024;
#pragma unroll
    for (int i = 0; i < 5; ++i) {
      const int c = tid + 256 * i;
      if (c < 1152) {
        const int P = c >> 6, w = c & 63;
        const int i16 = w >> 2, kq = (w & 3) ^ blk_swz(w >> 2);
        const u16* src = tile + i16 * LN_PITCH + 32 * P + 4 * kq;
        u32x2 a = *(const u32x2*)src, b = *(const u32x2*)(src + 16);
        *(u32x4*)(xn_g + (size_t)c * 16) = u32x4{a[0], a[1], b[0], b[1]};
      }
    }
    u16* xt_g = (u16*)(ws + WS_XNT);
#pragma unroll
    for (int i = 0; i < 5; ++i) {
      const int c = tid + 256 * i;
      if (c < 1152) {
        const int d = c >> 1, tq = c & 1;
        u32x4 o;
#pragma unroll
        for (int j = 0; j < 4; ++j) {
          u32 lo = tile[(tq * 8 + 2 * j) * LN_PITCH + d], hi = tile[(tq * 8 + 2 * j + 1) * LN_PITCH + d];
          o[j] = lo | (hi << 16);
        }
        *(u32x4*)(xt_g + (size_t)d * XT_PITCH + 128 + t0 + tq * 8) = o;
      }
    }
    if (tid < 72) {
      int c = bid * 72 + tid;
      *(u32x4*)(xt_g + (size_t)(c >> 4) * XT_PITCH + (c & 15) * 8) = u32x4{0u, 0u, 0u, 0u};
    }
  } else {
    const int e8 = (bid - LN_BLOCKS) * 256 + tid;
    const int sign = e8 >> 12, e = e8 & 4095;
    float* lut = (float*)(ws + WS_LUT);
    int a = LUT_LO + e;
    if (a > LUT_HI) a = LUT_HI;
    float h = bf2f((u16)(a | (sign << 15)));
    float ex = rbf(expf(-fabsf(h)));
    float lp = rbf(log1pf(ex));
    float sp = rbf(lp + (h > 0.f ? h : 0.f));
    float th = tanhf(sp);
    lut[e8] = rbf(th);
  }
}

constexpr int BIAS_LDS = 51200;
constexpr int LUT_LDS = 61440;
constexpr int CV_ST = 14848;
constexpr int CV_LDS = CV_ST + 8192;

DI void conv_stage_part(char* st, int d, int lane, int first, int step, const char* __restrict__ ws) {
  const char* ug = ws + WS_XNT + (size_t)d * XT_PITCH * 2;
  const char* kg = ws + WS_KX + (size_t)d * 2 * KX_NW * 4;
  const u32 l0 = __builtin_amdgcn_readfirstlane(lds_addr_of(st));
#pragma unroll
  for (int i = 0; i < 14; ++i) {
    if (i % step == first) {
      if (i < 5) glds16(ug + i * 1024, (u32)lane * 16u, l0 + i * 1024);
      else if (i < 13) glds16(kg + (i - 5) * 1024, (u32)lane * 16u, l0 + 5120 + (i - 5) * 1024);
      else glds16(kg + 8192, (u32)(lane & 3) * 16u, l0 + 5120 + 8192);
    }
  }
}
DI s16x8 conv_bfrag(const u32* kp, u32 sh) {
  const u32 e0 = kp[0], e1 = kp[1], e2 = kp[2], e3 = kp[3], e4 = kp[4];
  const u32x4 v = {__builtin_amdgcn_alignbyte(e1, e0, sh), __builtin_amdgcn_alignbyte(e2, e1, sh),
                   __builtin_amdgcn_alignbyte(e3, e2, sh), __builtin_amdgcn_alignbyte(e4, e3, sh)};
  return __builtin_bit_cast(s16x8, v);
}
DI void conv_hlsum(const f32x4& a, int hl, float& o0, float& o1) {
  const float s0 = hl ? a[0] : a[2], s1 = hl ? a[1] : a[3];
  const float k0 = hl ? a[2] : a[0], k1 = hl ? a[3] : a[1];
  o0 = k0 + __builtin_bit_cast(float, __builtin_amdgcn_update_dpp(0, __builtin_bit_cast(int, s0), 0x128, 0xf, 0xf, false));
  o1 = k1 + __builtin_bit_cast(float, __builtin_amdgcn_update_dpp(0, __builtin_bit_cast(int, s1), 0x128, 0xf, 0xf, false));
}

DI void s4_side_gemm1(char* smem, int bid, int j, int lane, const char* __restrict__ Ag, const char* __restrict__ Bg,
                      const u16* __restrict__ biasg, char* __restrict__ ws) {
  const int i16 = lane & 15, kq = lane >> 4;
  {
    constexpr int STAGE = 51200;
    const u32 lds0 = __builtin_amdgcn_readfirstlane(lds_addr_of(smem));
    const u32 lo = (u32)lane * 16u;
    auto stage = [&](int kt, int buf) {
      const u32 sb = lds0 + buf * STAGE;
#pragma unroll
      for (int i = 0; i < 8; ++i) {
        const int bb = j + 4 * i;
        glds16(Ag + ((size_t)(bb >> 1) * 18 + 2 * kt + (bb & 1)) * 1024, lo, sb + bb * 1024);
      }
      const char* bk = Bg + (size_t)kt * 18432;
#pragma unroll
      for (int i = 0; i < 4; ++i) glds16(bk + (j + 4 * i) * 1024, lo, sb + 32768 + (j + 4 * i) * 1024);
      if (j < 2) glds16(bk + (j + 16) * 1024, lo, sb + 32768 + (j + 16) * 1024);
    };
    stage(0, 1); stage(1, 2);
    if (j < 2) wait_vm<13>(); else wait_vm<12>();
    __builtin_amdgcn_s_barrier();
#pragma unroll 1
    for (int kt = 0; kt < 7; ++kt) {
      stage(kt + 2, kt % 3);
      if (j < 2) wait_vm<13>(); else wait_vm<12>();
      __builtin_amdgcn_s_barrier();
    }
#pragma unroll
    for (int i = 0; i < 8; ++i) glds16(ws + WS_LUT + (j * 8 + i) * 1024, lo, lds0 + LUT_LDS + (j * 8 + i) * 1024);
#pragma unroll
    for (int i = 0; i < 2; ++i) glds16(biasg, lo < 288u ? lo : 272u, lds0 + BIAS_LDS + (j * 2 + i) * 1024);
    wait_vm<10>();
    __builtin_amdgcn_s_barrier();
    wait_vm<0>();
    __builtin_amdgcn_s_barrier();
  }
  const int dx = 512 + (bid >> 2), g = bid & 3;
  char* st = smem + (j < 3 ? j * CV_ST : 102400);
  conv_stage_part(st, dx, lane, 0, 1, ws);
  wait_vm<0>();
  {
    const int rho = i16 & 7, hl = i16 >> 3;
    const char* us = st + 256;
    const u32 sh = ((1 - rho) & 1) * 2;
    const u32* ks = (const u32*)(st + 5120) + hl * KX_NW + ((2047 - 8 * (4 * g + kq) - rho) >> 1);
    f32x4 ya[4] = {f32x4{0, 0, 0, 0}, f32x4{0, 0, 0, 0}, f32x4{0, 0, 0, 0}, f32x4{0, 0, 0, 0}};
    const char* fb = us + (16 * j - 4 * g + i16 - kq) * 16;
#pragma unroll
    for (int q = 0; q < 16; ++q) {
      const s16x8 B = conv_bfrag(ks - 64 * q, sh);
#pragma unroll
      for (int r = 0; r < 4; ++r) {
        if (4 * r + 3 >= q) {
          if (j + 4 * r >= q) {
            const s16x8 F = *(const s16x8*)(fb + (4 * r - q) * 256);
            mfma_ip(ya[r], F, B);
          }
        }
      }
    }
    mfma_drain();
    float* yp = (float*)(ws + WS_YP) + ((size_t)(dx - 512) * 4 + g) * L_;
#pragma unroll
    for (int r = 0; r < 4; ++r) {
      float o0, o1;
      conv_hlsum(ya[r], hl, o0, o1);
      float* q = yp + 128 * (j + 4 * r) + 32 * kq + 16 * hl + rho;
      q[0] = o0; q[8] = o1;
    }
  }
}

DI void s4_side_gemm2(char* smem, char* scratch, int bid, int j, int lane, const char* __restrict__ Ag, const char* __restrict__ Bg,
                      const u16* __restrict__ Dskip, char* __restrict__ ws) {
  const int d = 2 * bid + (j >> 1), gh = j & 1;
  char* st = scratch + (j >> 1) * CV_LDS;
  conv_stage_part(st, d, lane, gh, 2, ws);
  const float dsk = bf2f(Dskip[d]);
  constexpr int STAGE2 = 34816;
  const u32 lds0 = __builtin_amdgcn_readfirstlane(lds_addr_of(smem));
  const u32 lo = (u32)lane * 16u;
  auto stage = [&](int kt) {
    const u32 sb = lds0 + (kt % 3) * STAGE2;
#pragma unroll
    for (int i = 0; i < 4; ++i) {
      const int bb = j + 4 * i;
      glds16(Ag + ((size_t)(bb >> 1) * 144 + 2 * kt + (bb & 1)) * 1024, lo, sb + bb * 1024);
    }
    const char* bk = Bg + (size_t)kt * 18432;
#pragma unroll
    for (int i = 0; i < 4; ++i) glds16(bk + (j + 4 * i) * 1024, lo, sb + 16384 + (j + 4 * i) * 1024);
    if (j < 2) glds16(bk + (j + 16) * 1024, lo, sb + 16384 + (j + 16) * 1024);
  };
  int jn = 0;
  auto step = [&]() {
    if (jn + 2 < 18) { if (j < 2) wait_vm<9>(); else wait_vm<8>(); }
    else wait_vm<0>();
    __builtin_amdgcn_s_barrier();
    ++jn;
    if (jn + 2 < 18) stage(jn + 2);
  };
  stage(0); stage(1);
  if (j < 2) wait_vm<9>(); else wait_vm<8>();
  __builtin_amdgcn_s_barrier();
  stage(2);
  step();
  const int i16 = lane & 15, kq = lane >> 4;
  const int rho = i16 & 7, hl = i16 >> 3;
  const char* us = st + 256;
  const u32 sh = ((1 - rho) & 1) * 2;
  f32x4 acc[16];
#pragma unroll
  for (int R = 0; R < 16; ++R) acc[R] = f32x4{0, 0, 0, 0};
#pragma unroll
  for (int gi = 0; gi < 2; ++gi) {
    const int g = 2 * gh + gi;
    s16x8 F[16];
#pragma unroll
    for (int dl = 0; dl < 16; ++dl) F[dl] = *(const s16x8*)(us + (16 * dl - 4 * g + i16 - kq) * 16);
    const u32* ks = (const u32*)(st + 5120) + hl * KX_NW + ((2047 - 8 * (4 * g + kq) - rho) >> 1);
#pragma unroll
    for (int q = 0; q < 16; ++q) {
      const s16x8 B = conv_bfrag(ks - 64 * q, sh);
#pragma unroll
      for (int R = q; R < 16; ++R) mfma_ip(acc[R], F[R - q], B);
      if (q <= 4 || q == 6 || q == 8 || q == 15) { SCHED0(); step(); }
    }
  }
  mfma_drain();
  float* ybuf = (float*)(st + CV_ST);
  float o[16][2];
#pragma unroll
  for (int R = 0; R < 16; ++R) conv_hlsum(acc[R], hl, o[R][0], o[R][1]);
  float* yl = ybuf + 32 * kq + 16 * hl + rho;
  if (gh) {
#pragma unroll
    for (int R = 0; R < 8; ++R) { yl[128 * R] = o[R][0]; yl[128 * R + 8] = o[R][1]; }
  } else {
#pragma unroll
    for (int R = 8; R < 16; ++R) { yl[128 * R] = o[R][0]; yl[128 * R + 8] = o[R][1]; }
  }
  raw_barrier();
  __builtin_amdgcn_s_barrier();
  if (gh) {
#pragma unroll
    for (int R = 8; R < 16; ++R) { yl[128 * R] += o[R][0]; yl[128 * R + 8] += o[R][1]; }
  } else {
#pragma unroll
    for (int R = 0; R < 8; ++R) { yl[128 * R] = o[R][0] + yl[128 * R]; yl[128 * R + 8] = o[R][1] + yl[128 * R + 8]; }
  }
  asm volatile("s_waitcnt lgkmcnt(0)" ::: "memory");
#pragma unroll
  for (int i = 0; i < 2; ++i) {
    const int t = gh * 1024 + (i * 64 + lane) * 8;
    const s16x8 uv = *(const s16x8*)(us + t * 2);
    const f32x4 s0 = *(const f32x4*)(ybuf + t), s1 = *(const f32x4*)(ybuf + t + 4);
    float sv[8] = {s0[0], s0[1], s0[2], s0[3], s1[0], s1[1], s1[2], s1[3]};
    u32x4 ov;
#pragma unroll
    for (int jj = 0; jj < 4; ++jj) {
      float r0 = rbf(sv[2 * jj]) + rbf(bf2f((u16)uv[2 * jj]) * dsk);
      float r1 = rbf(sv[2 * jj + 1]) + rbf(bf2f((u16)uv[2 * jj + 1]) * dsk);
      ov[jj] = pack2(r0, r1);
    }
    *(u32x4*)(ws + WS_YAT + ((size_t)d * L_ + t) * 2) = ov;
  }
}

template <int MODE> struct GemmCfg {
  static constexpr int BM = MODE ? 128 : 256;
  static constexpr int NPA = MODE ? F_ / 32 : D_ / 32;
  static constexpr int LDB = MODE ? D_ : F_;
  static constexpr int A_BYTES = BM * 128;
  static constexpr int B_BYTES = 64 * 288;
  static constexpr int STAGE = A_BYTES + B_BYTES;
  static constexpr int A_IPW = BM / 64;
  static constexpr int S4_OFF = 3 * STAGE;
  static constexpr int SMEM = 3 * STAGE + (MODE ? 2 * CV_LDS : 0);
};
#define NPRE 2

template <int MODE, int NB>
DI void gemm_wave_body(char* smem, const char* __restrict__ Ag, const char* __restrict__ Bg, const char* __restrict__ lutg,
                       const u16* __restrict__ biasg,
                       int wid, int lane, int wm, int wk, int pb0  , int sblk  ,
                       f32x4 (&acc)[4][5]) {
  using C = GemmCfg<MODE>;
  const int i16 = lane & 15, kq = lane >> 4;
  const bool extraB = wid < 2;
  const u32 aoffl = (u32)lane * 16u;
  const int a_rd = (wm * 8) * 1024 + i16 * 64 + ((kq ^ blk_swz(i16)) * 16);
  const int b_rd = C::A_BYTES + lane * 16 + pb0 * 1024;
  const int b_rs = C::A_BYTES + lane * 16 + sblk * 1024;

  const u32 lds0 = __builtin_amdgcn_readfirstlane(lds_addr_of(smem));
  auto stage = [&](int kt, int buf) {
    const u32 sb = lds0 + buf * C::STAGE;
#pragma unroll
    for (int i = 0; i < C::A_IPW; ++i) {
      const int b = wid * C::A_IPW + i;
      glds16(Ag + ((size_t)(b >> 1) * C::NPA + 2 * kt + (b & 1)) * 1024, aoffl, sb + b * 1024);
    }
    const char* bk = Bg + (size_t)kt * 18432;
#pragma unroll
    for (int i = 0; i < 2; ++i) glds16(bk + (wid + 8 * i) * 1024, aoffl, sb + C::A_BYTES + (wid + 8 * i) * 1024);
    if (extraB) glds16(bk + (wid + 16) * 1024, aoffl, sb + C::A_BYTES + (wid + 16) * 1024);
  };
  auto readA = [&](s16x8 (&a)[4], int buf, int ks) {
    const char* sb = smem + buf * C::STAGE;
#pragma unroll
    for (int mi = 0; mi < 4; ++mi) a[mi] = *(const s16x8*)(sb + a_rd + (mi * 2 + ks) * 1024);
  };
  auto readB = [&](int nb, int buf, int ks) -> s16x8 {
    const char* sb = smem + buf * C::STAGE;
    return *(const s16x8*)(sb + (nb < 4 ? b_rd + nb * 1024 : b_rs) + ks * 9 * 1024);
  };
  s16x8 a0[4], a1[4], b[NB];
  auto mm = [&](s16x8 (&ac)[4], int nb) {
#pragma unroll
    for (int mi = 0; mi < 4; ++mi)
      asm volatile("v_mfma_f32_16x16x32_bf16 %0, %1, %2, %0" : "+v"(acc[mi][nb]) : "v"(b[nb]), "v"(ac[mi]));
  };
  auto sub_pre = [&](s16x8 (&ac)[4]) {
#pragma unroll
    for (int nb = 0; nb < NPRE; ++nb) mm(ac, nb);
    SCHED0();
  };
  auto sub_post = [&](s16x8 (&ac)[4], s16x8 (&an)[4], bool ld, int nbuf, int nks) {
    if (ld) {
      readA(an, nbuf, nks);
#pragma unroll
      for (int nb = 0; nb < NPRE; ++nb) b[nb] = readB(nb, nbuf, nks);
    }
    SCHED0();
#pragma unroll
    for (int nb = NPRE; nb < NB; ++nb) {
      mm(ac, nb);
      if (ld) b[nb] = readB(nb, nbuf, nks);
      SCHED0();
    }
  };
  constexpr int NW0 = C::A_IPW + 2;
#pragma unroll
  for (int mi = 0; mi < 4; ++mi)
#pragma unroll
    for (int nb = 0; nb < NB; ++nb) asm volatile("" : "+v"(acc[mi][nb]));
  if (MODE == 0) {
    raw_barrier();
    readA(a0, 1, 0);
#pragma unroll
    for (int nb = 0; nb < NB; ++nb) b[nb] = readB(nb, 1, 0);
#pragma unroll 1
    for (int o = 0; o < 3; ++o) {
#pragma unroll
      for (int u = 0; u < 3; ++u) {
        SCHED0();
        sub_pre(a0); sub_post(a0, a1, true, (u + 1) % 3, 1);
        sub_pre(a1);
        raw_barrier();
        sub_post(a1, a0, true, (u + 2) % 3, 0);
      }
    }
  } else {
    raw_barrier();
    readA(a0, 0, wk);
#pragma unroll
    for (int nb = 0; nb < NB; ++nb) b[nb] = readB(nb, 0, wk);
#pragma unroll 1
    for (int o = 0; o < 3; ++o) {
#pragma unroll
      for (int u = 0; u < 6; ++u) {
        const int j = 6 * o + u;
        SCHED0();
        if (u & 1) sub_pre(a1); else sub_pre(a0);
        if (j + 1 < 18) raw_barrier();
        SCHED0();
        if (u & 1) sub_post(a1, a0, true, (u + 1) % 3, wk);
        else       sub_post(a0, a1, true, (u + 1) % 3, wk);
      }
    }
    raw_barrier();
  }
  asm volatile("s_nop 15\n\ts_nop 15" ::: "memory");
#pragma unroll
  for (int mi = 0; mi < 4; ++mi)
#pragma unroll
    for (int nb = 0; nb < NB; ++nb) asm volatile("" : "+v"(acc[mi][nb]));
}

__global__ __launch_bounds__(768) void gemm1_kernel(const u16* __restrict__ w1, const u16* __restrict__ bias, char* __restrict__ ws) {
  using C = GemmCfg<0>;
  __shared__ __attribute__((aligned(1024))) char smem[C::SMEM];
  const int tid = threadIdx.x, lane = tid & 63, wid = __builtin_amdgcn_readfirstlane(tid >> 6);
  const int bid = blockIdx.x;
  const int xcd = bid & 7, idx = bid >> 3;
  const int nt = 4 * xcd + (idx & 3), m0 = (idx >> 2) * 256;
  const int n0 = nt * 144;
  const char* Ag = ws + WS_XN + (size_t)(m0 >> 4) * C::NPA * 1024;
  const char* Bg = ws + WS_W1P + (size_t)nt * 9 * 18432;
  if (wid >= 8) { s4_side_gemm1(smem, bid, wid - 8, lane, Ag, Bg, bias + n0, ws); return; }
  __builtin_amdgcn_s_setprio(1);

  const int wn = wid >> 2;
  const int wm = wid & 3;
  const int odd = nt & 1;
  const int pb0 = odd + 4 * wn;
  const int sblk = odd ? 0 : 8;
  f32x4 acc[4][5];
#pragma unroll
  for (int mi = 0; mi < 4; ++mi)
#pragma unroll
    for (int nb = 0; nb < 5; ++nb) acc[mi][nb] = f32x4{0, 0, 0, 0};
  const int i16 = lane & 15, kq = lane >> 4;
  const char* lutg = ws + WS_LUT;
  if (wn == 0) gemm_wave_body<0, 5>(smem, Ag, Bg, lutg, bias + n0, wid, lane, wm, 0, pb0, sblk, acc);
  else         gemm_wave_body<0, 4>(smem, Ag, Bg, lutg, bias + n0, wid, lane, wm, 0, pb0, sblk, acc);
  f32x2 bf_[5][2];
#pragma unroll
  for (int nb = 0; nb < 5; ++nb) {
    const int blk = nb < 4 ? pb0 + nb : sblk;
    const u32x2 bb = *(const u32x2*)(smem + BIAS_LDS + wid * 1024 + (blk * 16 + 4 * kq) * 2);
#pragma unroll
    for (int e = 0; e < 2; ++e) { bf_[nb][e] = f32x2{lo16(bb[e]), hi16(bb[e])}; asm volatile("" : "+v"(bf_[nb][e])); }
  }
  const char* lut = smem + (LUT_LDS - LUT_LO * 4);
  char* act = ws + WS_ACT;
  const int inblk = i16 * 64 + ((kq ^ blk_swz(i16)) * 16);
#pragma unroll
  for (int mi = 0; mi < 4; ++mi) {
    float hf[5][4], th[5][4];
#pragma unroll
    for (int nb = 0; nb < 5; ++nb) {
      if (nb < 4 || wn == 0) {
#pragma unroll
        for (int e = 0; e < 2; ++e) {
          u32 a = pack2(acc[mi][nb][2 * e], acc[mi][nb][2 * e + 1]);
          u32 h = pack2(lo16(a) + bf_[nb][e][0], hi16(a) + bf_[nb][e][1]);
          hf[nb][2 * e] = lo16(h); hf[nb][2 * e + 1] = hi16(h);
        }
#pragma unroll
        for (int j = 0; j < 4; ++j) {
          const u32 u = __float_as_uint(hf[nb][j]);
          const float m = __builtin_amdgcn_fmed3f(__builtin_fabsf(hf[nb][j]), 0.0009765625f, 128.0f);
          u32 o;
          asm("v_lshl_add_u32 %0, %1, 14, %2" : "=v"(o) : "v"(u >> 31), "v"(__float_as_uint(m) >> 14));
          th[nb][j] = *(const float*)(lut + o);
        }
      }
    }
    const size_t mbg = (size_t)((m0 >> 4) + wm * 4 + mi);
    u32 r[5][2];
#pragma unroll
    for (int nb = 0; nb < 5; ++nb)
      if (nb < 4 || wn == 0) {
#pragma unroll
        for (int e = 0; e < 2; ++e) r[nb][e] = pack2(hf[nb][2 * e] * th[nb][2 * e], hf[nb][2 * e + 1] * th[nb][2 * e + 1]);
      }
#pragma unroll
    for (int pr = 0; pr < 2; ++pr) {
      const int Pb = (n0 + (pb0 + 2 * pr) * 16) >> 5;
      st16_wt(act + (mbg * 144 + Pb) * 1024 + inblk, u32x4{r[2 * pr][0], r[2 * pr][1], r[2 * pr + 1][0], r[2 * pr + 1][1]});
    }
    if (wn == 0) {
      const int nb16 = (n0 >> 4) + sblk;
      st8_wt(act + (mbg * 144 + (nb16 >> 1)) * 1024 + inblk + (nb16 & 1) * 8, u32x2{r[4][0], r[4][1]});
    }
  }

}

__global__ __launch_bounds__(768) void gemm2_kernel(const u16* __restrict__ w2, const u16* __restrict__ Dskip, char* __restrict__ ws) {
  using C = GemmCfg<1>;
  __shared__ __attribute__((aligned(1024))) char smem[C::SMEM];
  const int tid = threadIdx.x, lane = tid & 63, wid = __builtin_amdgcn_readfirstlane(tid >> 6);
  const int bid = blockIdx.x;
  const int xcd = bid & 7, idx = bid >> 3;
  const int ksplit = xcd >> 1, mt = (xcd & 1) * 8 + (idx >> 2), nt = idx & 3;
  const int m0 = mt * 128, n0 = nt * 144;
  const char* Ag = ws + WS_ACT + ((size_t)(m0 >> 4) * C::NPA + ksplit * 36) * 1024;
  const char* Bg = ws + WS_W2P + ((size_t)nt * 72 + ksplit * 18) * 18432;
  if (wid >= 8) {
    __builtin_amdgcn_s_setprio(2);
    s4_side_gemm2(smem, smem + C::S4_OFF, bid, wid - 8, lane, Ag, Bg, Dskip, ws);

    return;
  }
  __builtin_amdgcn_s_setprio(1);

  const int wn = wid >> 2;
  const int wmv = wid & 3;
  const int wk = wmv >> 1, wm = wmv & 1;
  const int odd = nt & 1;
  const int pb0 = odd + 4 * wn;
  const int sblk = odd ? 0 : 8;
  f32x4 acc[4][5];
#pragma unroll
  for (int mi = 0; mi < 4; ++mi)
#pragma unroll
    for (int nb = 0; nb < 5; ++nb) acc[mi][nb] = f32x4{0, 0, 0, 0};
  if (wn == 0) gemm_wave_body<1, 5>(smem, Ag, Bg, nullptr, nullptr, wid, lane, wm, wk, pb0, sblk, acc);
  else         gemm_wave_body<1, 4>(smem, Ag, Bg, nullptr, nullptr, wid, lane, wm, wk, pb0, sblk, acc);

  const int i16 = lane & 15, kq = lane >> 4;
  f32x4* red = (f32x4*)smem;
  const int slot = wn * 2 + wm;
  if (wk == 1) {
#pragma unroll
    for (int mi = 0; mi < 4; ++mi)
#pragma unroll
      for (int nb = 0; nb < 5; ++nb)
        if (nb < 4 || wn == 0) red[(slot * 20 + mi * 5 + nb) * 64 + lane] = acc[mi][nb];
  }
  raw_barrier();
  char* partb = ws + WS_PART;
  constexpr size_t PSTRIDE = (size_t)L_ * D_ * 4;
  if (wk == 0) {
#pragma unroll
    for (int mi = 0; mi < 4; ++mi)
#pragma unroll
      for (int nb = 0; nb < 5; ++nb)
        if (nb < 4 || wn == 0) {
          acc[mi][nb] += red[(slot * 20 + mi * 5 + nb) * 64 + lane];
          const size_t mbg = (size_t)((m0 >> 4) + wm * 4 + mi);
          const int nbg = (n0 >> 4) + (nb < 4 ? pb0 + nb : sblk);
          st16_wt(partb + ksplit * PSTRIDE + (mbg * 36 + nbg) * 1024 + i16 * 64 + kq * 16, __builtin_bit_cast(u32x4, acc[mi][nb]));
        }
  }

}

__global__ __launch_bounds__(256) void final_kernel(const u16* __restrict__ x, const u16* __restrict__ Dskip,
                                                    const char* __restrict__ ws, u16* __restrict__ out) {
  __shared__ __attribute__((aligned(16))) u16 yt[64 * 40];
  const int tid = threadIdx.x;
  const int tt = blockIdx.x / 9, dt = blockIdx.x % 9;
  const int t0 = tt * 32, d0 = dt * 64;
  const char* part = ws + WS_PART;
  const int r = tid >> 3, ch = tid & 7;
  f32x4 p[4][2];
  {
    const int t = t0 + r, d = d0 + ch * 8;
    const size_t po = ((size_t)(t >> 4) * 36 + (d >> 4)) * 1024 + (t & 15) * 64 + ((d >> 2) & 3) * 16;
#pragma unroll
    for (int s = 0; s < 4; ++s) {
      p[s][0] = *(const f32x4*)(part + (size_t)s * L_ * D_ * 4 + po);
      p[s][1] = *(const f32x4*)(part + (size_t)s * L_ * D_ * 4 + po + 16);
    }
  }
  const u32x4 xv = *(const u32x4*)(x + (size_t)(t0 + r) * D_ + d0 + ch * 8);
  const int dd = tid >> 2, tq = tid & 3;
  u32x4 yv;
  if (dt < 8) yv = *(const u32x4*)((const u16*)(ws + WS_YAT) + (size_t)(d0 + dd) * L_ + t0 + tq * 8);
  else {
    const float* yp = (const float*)(ws + WS_YP) + (size_t)dd * 4 * L_ + t0 + tq * 8;
    f32x4 a0 = (*(const f32x4*)(yp) + *(const f32x4*)(yp + L_)) + (*(const f32x4*)(yp + 2 * L_) + *(const f32x4*)(yp + 3 * L_));
    f32x4 a1 = (*(const f32x4*)(yp + 4) + *(const f32x4*)(yp + L_ + 4)) + (*(const f32x4*)(yp + 2 * L_ + 4) + *(const f32x4*)(yp + 3 * L_ + 4));
    const u32x4 uv = *(const u32x4*)((const u16*)(ws + WS_XNT) + (size_t)(d0 + dd) * XT_PITCH + 128 + t0 + tq * 8);
    const float dsk = bf2f(Dskip[d0 + dd]);
    float sv[8] = {a0[0], a0[1], a0[2], a0[3], a1[0], a1[1], a1[2], a1[3]};
#pragma unroll
    for (int jj = 0; jj < 4; ++jj)
      yv[jj] = pack2(rbf(sv[2 * jj]) + rbf(lo16(uv[jj]) * dsk), rbf(sv[2 * jj + 1]) + rbf(hi16(uv[jj]) * dsk));
  }
  *(u32x4*)(yt + dd * 40 + tq * 8) = yv;
  __syncthreads();
  f32x4 s0 = (p[0][0] + p[1][0]) + (p[2][0] + p[3][0]);
  f32x4 s1 = (p[0][1] + p[1][1]) + (p[2][1] + p[3][1]);
  float sm[8] = {s0[0], s0[1], s0[2], s0[3], s1[0], s1[1], s1[2], s1[3]};
  u32x4 ov;
#pragma unroll
  for (int j = 0; j < 4; ++j) {
    float r2[2];
#pragma unroll
    for (int h = 0; h < 2; ++h) {
      int e = 2 * j + h;
      float xx = h ? hi16(xv[j]) : lo16(xv[j]);
      float ya = bf2f(yt[(ch * 8 + e) * 40 + r]);
      r2[h] = rbf(xx + ya) + rbf(sm[e]);
    }
    ov[j] = pack2(r2[0], r2[1]);
  }
  *(u32x4*)(out + (size_t)(t0 + r) * D_ + d0 + ch * 8) = ov;
}

extern "C" void kernel_launch(void* const* d_in, const int* in_sizes, int n_in,
                              void* d_out, int out_size, void* d_ws, size_t ws_size,
                              hipStream_t stream) {
  const u16* x      = (const u16*)d_in[0];
  const u16* A_re   = (const u16*)d_in[1];
  const u16* A_im   = (const u16*)d_in[2];
  const u16* B_re   = (const u16*)d_in[3];
  const u16* B_im   = (const u16*)d_in[4];
  const u16* C_re   = (const u16*)d_in[5];
  const u16* C_im   = (const u16*)d_in[6];
  const u16* log_dt = (const u16*)d_in[7];
  const u16* Dskip  = (const u16*)d_in[8];
  const u16* w1     = (const u16*)d_in[10];
  const u16* b1     = (const u16*)d_in[11];
  const u16* w2     = (const u16*)d_in[12];
  char* ws = (char*)d_ws;
  S4Params P{A_re, A_im, B_re, B_im, C_re, C_im, log_dt};
  prep_kernel<<<PREP_BLOCKS, 256, 0, stream>>>(x, P, w1, w2, ws);
  gemm1_kernel<<<256, 768, 0, stream>>>(w1, b1, ws);
  gemm2_kernel<<<256, 768, 0, stream>>>(w2, Dskip, ws);
  final_kernel<<<576, 256, 0, stream>>>(x, Dskip, ws, (u16*)d_out);
}
```
